```python
import torch

def _softmax(x, axis=-1):
    return torch.softmax(x, dim=axis)
def _gelu(x):
    return torch.nn.functional.gelu(x, approximate="tanh")
def _silu(x):
    return torch.nn.functional.silu(x)
def _sigmoid(x):
    return torch.sigmoid(x)
def _layernorm(x, axis=-1, eps=1e-5):
    m = x.mean(dim=axis, keepdim=True)
    v = x.var(dim=axis, keepdim=True, unbiased=False)
    return (x - m) / torch.sqrt(v + eps)
def _scatter_add(tgt, idx, src, axis=0):
    return tgt.clone().index_add_(axis, idx, src)
def _segment_sum(src, seg, n):
    out = torch.zeros((n,) + src.shape[1:], dtype=src.dtype)
    return out.index_add_(0, seg, src)
def _pad(x, pw):
    flat = tuple(v for pair in reversed(pw) for v in pair)
    return torch.nn.functional.pad(x, flat)

kernel_name = 'gated_mix__spectral_gfnet__swiglu_ffn__prompt'

def setup_inputs():

    _w = (lambda a: (lambda t: t.to(torch.bfloat16) if t.is_floating_point() else t)(torch.as_tensor(a)))
    def _A_setup():
        B, S, D = 1, 2048, 1024
        Df = D // 2 + 1
        return {
            "x": _w((lambda *s: torch.randn(*s, dtype=torch.bfloat16) * 0.05)(B, S, D)),
            "w_r": _w((lambda *s: torch.randn(*s, dtype=torch.bfloat16) * 1.6)(S, Df) * 0.02),
            "w_i": _w((lambda *s: torch.randn(*s, dtype=torch.bfloat16) * 1.6)(S, Df) * 0.02),
        }
    _A_in = _A_setup()

    def _B_setup():
        B, S, D, F = 1, 2048, 1024, 3584
        return {
            "x": _w((lambda *s: torch.randn(*s, dtype=torch.bfloat16) * 0.05)(B, S, D)),
            "w_gate": _w((lambda *s: torch.randn(*s, dtype=torch.bfloat16) * 0.05)(D, F)),
            "w_up": _w((lambda *s: torch.randn(*s, dtype=torch.bfloat16) * 0.05)(D, F)),
            "w_down": _w((lambda *s: torch.randn(*s, dtype=torch.bfloat16) * 0.05)(F, D)),
        }
    _B_in = _B_setup()

    return {
        "A_x": _A_in["x"],
        "A_w_r": _A_in["w_r"],
        "A_w_i": _A_in["w_i"],
        "B_x": _B_in["x"],
        "B_w_gate": _B_in["w_gate"],
        "B_w_up": _B_in["w_up"],
        "B_w_down": _B_in["w_down"],
        "gate_w": _w((lambda *s: torch.randn(*s, dtype=torch.bfloat16) * 0.05)(1024) * 16.0),
    }

def reference(A_x, A_w_r, A_w_i, B_x, B_w_gate, B_w_up, B_w_down, gate_w):
    def _A_ref(x, w_r, w_i):
        B, S, D = x.shape
        X = (lambda x, axes=(-2,-1): torch.fft.rfft2(x.float(), dim=axes))(x, axes=(-2, -1))
        W = w_r + 1j * w_i
        X = X * W
        y = (lambda x, s, axes=(-2,-1): torch.fft.irfft2(x, s=s, dim=axes).to(torch.bfloat16))(X, (S, D), axes=(-2, -1))
        return y

    def _B_ref(x, w_gate, w_up, w_down):
        g = _silu(x @ w_gate)
        u = x @ w_up
        return (g * u) @ w_down

    x = A_x
    g = _sigmoid((x * gate_w).sum(-1, keepdims=True))
    a = _A_ref(x, A_w_r, A_w_i)
    b = _B_ref(x, B_w_gate, B_w_up, B_w_down)
    return g * a + (1 - g) * b
```

```cpp
#include <hip/hip_runtime.h>
#include <hip/hip_bf16.h>
#include <stdint.h>
typedef unsigned short u16;
#define S_ 2048
#define D_ 1024
#define F_ 3584
#define DF 513
__device__ __forceinline__ float b2f(u16 v){ return __uint_as_float(((unsigned)v)<<16); }
__device__ __forceinline__ u16 f2b(float f){ unsigned u=__float_as_uint(f); u += 0x7fffu + ((u>>16)&1u); return (u16)(u>>16); }
__device__ __forceinline__ float rb(float f){ return b2f(f2b(f)); }

__global__ void k_gate(const u16* x, const u16* gw, float* g){
  int s=blockIdx.x; float acc=0;
  for(int d=threadIdx.x; d<D_; d+=64) acc += rb(b2f(x[s*D_+d])*b2f(gw[d]));
  for(int o=32;o;o>>=1) acc += __shfl_xor(acc,o);
  if(threadIdx.x==0){ float z=rb(acc); g[s]=rb(1.f/(1.f+expf(-z))); }
}
__global__ void k_rowfft(const u16* x, float2* X1){
  __shared__ float xs[D_]; __shared__ float2 tw[D_];
  int s=blockIdx.x;
  for(int d=threadIdx.x; d<D_; d+=blockDim.x){ xs[d]=b2f(x[s*D_+d]); float sn,cs; sincospif(-2.f*d/D_,&sn,&cs); tw[d]=make_float2(cs,sn);}
  __syncthreads();
  for(int k=threadIdx.x;k<DF;k+=blockDim.x){ float re=0,im=0; for(int d=0;d<D_;d++){ float2 t=tw[(k*d)&(D_-1)]; re+=xs[d]*t.x; im+=xs[d]*t.y;} X1[(size_t)s*DF+k]=make_float2(re,im);}
}
__global__ void k_colfft(const float2* in, float2* out, const u16* wr, const u16* wi, float sgn, int mul){
  __shared__ float2 col[S_]; __shared__ float2 tw[S_];
  int k=blockIdx.x;
  for(int s=threadIdx.x;s<S_;s+=blockDim.x){ col[s]=in[(size_t)s*DF+k]; float sn,cs; sincospif(sgn*2.f*s/S_,&sn,&cs); tw[s]=make_float2(cs,sn);}
  __syncthreads();
  for(int j=threadIdx.x;j<S_;j+=blockDim.x){ float re=0,im=0; for(int s=0;s<S_;s++){ float2 t=tw[(j*s)&(S_-1)]; float2 v=col[s]; re+=v.x*t.x-v.y*t.y; im+=v.x*t.y+v.y*t.x;}
    if(mul){ float a=b2f(wr[(size_t)j*DF+k]), b=b2f(wi[(size_t)j*DF+k]); float r2=re*a-im*b, i2=re*b+im*a; re=r2; im=i2;}
    out[(size_t)j*DF+k]=make_float2(re,im);}
}
__global__ void k_rowifft(const float2* Z, float* a){
  __shared__ float2 zs[DF]; __shared__ float2 tw[D_];
  int s=blockIdx.x;
  for(int d=threadIdx.x; d<D_; d+=blockDim.x){ float sn,cs; sincospif(2.f*d/D_,&sn,&cs); tw[d]=make_float2(cs,sn);}
  for(int k=threadIdx.x;k<DF;k+=blockDim.x) zs[k]=Z[(size_t)s*DF+k];
  __syncthreads();
  for(int d=threadIdx.x; d<D_; d+=blockDim.x){ float acc=zs[0].x + ((d&1)?-zs[512].x:zs[512].x); float t2=0; for(int k=1;k<512;k++){ float2 t=tw[(k*d)&(D_-1)]; t2+=zs[k].x*t.x-zs[k].y*t.y;} acc+=2*t2; a[(size_t)s*D_+d]=rb(acc*(1.f/(S_*D_))); }
}
template<int MODE>
__global__ void k_gemm(const u16* A, const u16* B, const u16* B2, int K, int N, u16* H, const float* g, const float* a, u16* out){
  __shared__ float As[16][65], Bs[16][65], B2s[16][65];
  int tx=threadIdx.x&15, ty=threadIdx.x>>4; int m0=blockIdx.y*64, n0=blockIdx.x*64;
  float c[4][4]={}, c2[4][4]={};
  for(int k0=0;k0<K;k0+=16){
    for(int i=threadIdx.x;i<1024;i+=256){ int r=i>>4,kk=i&15; As[kk][r]=b2f(A[(size_t)(m0+r)*K+k0+kk]); int kb=i>>6, cc=i&63; Bs[kb][cc]=b2f(B[(size_t)(k0+kb)*N+n0+cc]); if(MODE==0) B2s[kb][cc]=b2f(B2[(size_t)(k0+kb)*N+n0+cc]); }
    __syncthreads();
    for(int kk=0;kk<16;kk++){ float av[4],bv[4],b2v[4]; for(int i=0;i<4;i++){av[i]=As[kk][ty*4+i]; bv[i]=Bs[kk][tx*4+i]; if(MODE==0) b2v[i]=B2s[kk][tx*4+i];}
      for(int i=0;i<4;i++)for(int j=0;j<4;j++){ c[i][j]+=av[i]*bv[j]; if(MODE==0) c2[i][j]+=av[i]*b2v[j]; } }
    __syncthreads();
  }
  for(int i=0;i<4;i++)for(int j=0;j<4;j++){ int m=m0+ty*4+i,n=n0+tx*4+j;
    if(MODE==0){ float z=rb(c[i][j]); float sl=rb(z/(1.f+expf(-z))); float u=rb(c2[i][j]); H[(size_t)m*N+n]=f2b(sl*u);}
    else { float b=rb(c[i][j]); float gg=g[m]; float t1=rb(gg*a[(size_t)m*N+n]); float om=rb(1.f-gg); float t2=rb(om*b); out[(size_t)m*N+n]=f2b(t1+t2);} }
}
extern "C" void kernel_launch(void* const* d_in, const int* in_sizes, int n_in, void* d_out, int out_size, void* d_ws, size_t ws_size, hipStream_t stream){
  const u16* x=(const u16*)d_in[0]; const u16* wr=(const u16*)d_in[1]; const u16* wi=(const u16*)d_in[2];
  const u16* wg=(const u16*)d_in[4]; const u16* wu=(const u16*)d_in[5]; const u16* wd=(const u16*)d_in[6]; const u16* gw=(const u16*)d_in[7];
  char* ws=(char*)d_ws; float* g=(float*)ws; float2* X1=(float2*)(ws+(1<<20)); float2* X2=(float2*)(ws+(17<<20)); float* a=(float*)(ws+(33<<20)); u16* H=(u16*)(ws+(49<<20));
  k_gate<<<S_,64,0,stream>>>(x,gw,g);
  k_rowfft<<<S_,256,0,stream>>>(x,X1);
  k_colfft<<<DF,256,0,stream>>>(X1,X2,wr,wi,-1.f,1);
  k_colfft<<<DF,256,0,stream>>>(X2,X1,wr,wi,1.f,0);
  k_rowifft<<<S_,256,0,stream>>>(X1,a);
  k_gemm<0><<<dim3(F_/64,S_/64),256,0,stream>>>(x,wg,wu,D_,F_,H,nullptr,nullptr,nullptr);
  k_gemm<1><<<dim3(D_/64,S_/64),256,0,stream>>>(H,wd,nullptr,F_,D_,nullptr,g,a,(u16*)d_out);
}
```

```cpp
#include <hip/hip_runtime.h>
#include <stdint.h>
typedef unsigned short u16; typedef unsigned long long u64;
typedef unsigned u32x2 __attribute__((ext_vector_type(2)));
typedef unsigned u32x4 __attribute__((ext_vector_type(4)));
typedef unsigned u32x8 __attribute__((ext_vector_type(8)));
typedef unsigned u32x16 __attribute__((ext_vector_type(16)));
typedef unsigned u32x32 __attribute__((ext_vector_type(32)));
typedef float f32x2 __attribute__((ext_vector_type(2)));
typedef __bf16 bf16x2 __attribute__((ext_vector_type(2)));
#define DI __device__ __forceinline__
constexpr unsigned WS_E=0, WS_FP=4096, WS_FTT=36864, WS_Z=65536, WS_MISC=589824;
constexpr size_t WS_T0=1u<<20, WS_T=8u<<20, WS_X=16u<<20, WS_H=32u<<20, WS_ST=64u<<20;
constexpr unsigned BLK=16896, UNIT=4*BLK;
DI unsigned rfl(unsigned v){ return __builtin_amdgcn_readfirstlane(v); }
DI u32x4 mksrd(const void* p, unsigned n){ u64 a=(u64)p; u32x4 r; r[0]=(unsigned)a; r[1]=(unsigned)(a>>32); r[2]=n; r[3]=0x20000u; return r; }
DI float cosv(float rev){ return __builtin_amdgcn_cosf(rev); }
DI float sinv(float rev){ return __builtin_amdgcn_sinf(rev); }
DI unsigned pk2(float a,float b){ f32x2 v={a,b}; return __builtin_bit_cast(unsigned,__builtin_convertvector(v,bf16x2)); }
DI unsigned fu(float f){ return __float_as_uint(f); }
DI unsigned sel(bool c,unsigned a,unsigned b){ const unsigned m=0u-(unsigned)c; return (a&m)|(b&~m); }
DI unsigned sig(unsigned r){ return ((r&3u)<<2)|((r>>2)&3u); }
static __device__ const unsigned TAB[14336]={
897594752,13696,13696,13696,0xb5803580,0xb3493580,0xb4c4356d,0xb4db3567,897594752,0xb3c9357f,0xb5353535,0xb5463522,0xb5803580,0xb416357d,0xb56d34c4,0xb5783479,897594752,0xb448357b,0xb5800000,0xb57bb448,0xb5803580,0xb4793578,0xb56db4c4,0xb54eb518,897594752,0xb4953575,0xb535b535,0xb4f1b562,0xb5803580,0xb4ac3571,0xb4c4b56d,0xb349b580,
897594752,0xb4c4356d,46464,885306733,0xb5803580,0xb4db3567,885306733,893302060,897594752,0xb4f13562,892712245,896906389,0xb5803580,0xb504355c,896382148,897397782,897594752,0xb50e3555,897581056,894776590,0xb5803580,0xb518354e,896349380,889468252,897594752,0xb5223546,892679477,868824447,0xb5803580,0xb52c353e,885273965,0xb4ac3571,
13696,13696,13696,13696,0xb3c9357f,0xb416357d,0xb4f13562,0xb504355c,0xb448357b,0xb4953575,0xb555350e,0xb56234f1,0xb4953575,0xb4db3567,0xb57f33c9,0xb580b349,0xb4c4356d,0xb50e3555,0xb56db4c4,0xb555b50e,0xb4f13562,0xb52c353e,0xb522b546,0xb4dbb567,0xb50e3555,0xb5463522,0xb448b57b,868857215,0xb5223546,0xb55c3504,882226549,890811726,
0xb5353535,0xb56d34c4,892712245,896382148,0xb5463522,0xb5783479,896906389,897397782,0xb555350e,0xb57f33c9,897266760,893793570,0xb56234f1,0xb580b349,893793570,883701105,0xb56d34c4,0xb57bb448,885273965,0xb448357b,0xb5753495,0xb571b4ac,0xb3c9357f,0xb52c353e,0xb57b3448,0xb562b4f1,0xb50e3555,0xb5753495,0xb57f33c9,0xb54eb518,0xb56234f1,0xb578b479,
13696,13696,13696,13696,0xb448357b,0xb4793578,0xb50e3555,0xb518354e,0xb4c4356d,0xb4f13562,0xb56d34c4,0xb5753495,0xb50e3555,0xb52c353e,0xb57bb448,0xb571b4ac,0xb5353535,0xb555350e,0xb535b535,0xb50eb555,0xb555350e,0xb57134ac,0xb448b57b,860468608,0xb56d34c4,0xb57f33c9,885306733,891467078,0xb57b3448,0xb57db416,894809358,897102969,
0xb5800000,0xb56db4c4,897581056,896349380,0xb57bb448,0xb54eb518,894776590,889468252,0xb56db4c4,0xb522b546,885273965,0xb3c9357f,0xb555b50e,0xb4dbb567,0xb448357b,0xb52c353e,0xb535b535,0xb448b57b,0xb5353535,0xb57b3448,0xb50eb555,860468608,0xb57b3448,0xb567b4db,0xb4c4b56d,882226549,0xb56db4c4,0xb4f1b562,0xb448b57b,889501020,0xb50eb555,873903485,
13696,13696,13696,13696,0xb4953575,0xb4ac3571,0xb5223546,0xb52c353e,0xb50e3555,0xb5223546,0xb57b3448,0xb57f33c9,0xb5463522,0xb55c3504,0xb562b4f1,0xb54eb518,0xb56d34c4,0xb57b3448,0xb4c4b56d,0xb448b57b,0xb57f33c9,0xb57db416,882226549,889501020,0xb57bb448,0xb562b4f1,894809358,896906389,0xb562b4f1,0xb52cb53e,897528777,895956187,
0xb535b535,0xb4c4b56d,892679477,885273965,0xb4f1b562,0xb349b580,868824447,0xb4ac3571,0xb448b57b,882226549,0xb50e3555,0xb56234f1,868857215,890811726,0xb5753495,0xb578b479,885306733,894809358,0xb56db4c4,0xb50eb555,891467078,897102969,0xb4f1b562,873903485,894809358,897528777,877180283,893826338,896906389,895956187,893826338,897594185,
897594752,0xb5353535,13696,0xb5353535,0xb5803580,0xb53e352c,0xb4c4356d,0xb57134ac,897594752,0xb5463522,0xb5353535,0xb57fb3c9,0xb5803580,0xb54e3518,0xb56d34c4,0xb55cb504,897594752,0xb555350e,0xb5800000,0xb50eb555,0xb5803580,0xb55c3504,0xb56db4c4,0xb416b57d,897594752,0xb56234f1,0xb535b535,882226549,0xb5803580,0xb56734db,0xb4c4b56d,892122430,
897594752,0xb56d34c4,46464,896382148,0xb5803580,0xb57134ac,885306733,897594185,897594752,0xb5753495,892712245,895628529,0xb5803580,0xb5783479,896382148,890778958,897594752,0xb57b3448,897581056,877147515,0xb5803580,0xb57d3416,896349380,0xb4793578,897594752,0xb57f33c9,892679477,0xb5223546,0xb5803580,0xb5803349,885273965,0xb56734db,
0xb5800000,0xb535b535,0xb5800000,0xb535b535,0xb57fb3c9,0xb518b54e,0xb562b4f1,0xb479b578,0xb57bb448,0xb4f1b562,0xb50eb555,882226549,0xb575b495,0xb4acb571,0xb3c9b57f,893302060,0xb56db4c4,0xb448b57b,885306733,897299528,0xb562b4f1,0xb349b580,893826338,896611500,0xb555b50e,868857215,897299528,891434310,0xb546b522,880391544,896873621,873870717,
0xb535b535,885306733,892679477,0xb4c4356d,0xb522b546,889501020,882193781,0xb54e3518,0xb50eb555,891467078,0xb448357b,0xb57f33c9,0xb4f1b562,893302060,0xb5223546,0xb567b4db,0xb4c4b56d,894809358,0xb56d34c4,0xb50eb555,0xb495b575,895988955,0xb57fb3c9,0xb349b580,0xb448b57b,896906389,0xb555b50e,888255842,0xb3c9b57f,897430550,0xb4f1b562,895268100,
46464,892712245,46464,892712245,877180283,895268100,890156373,897430550,885306733,896906389,896382148,895628529,890156373,897626953,897266760,886781287,892712245,897266760,892679477,0xb448357b,894809358,895956187,877147515,0xb53e352c,896382148,893793570,0xb4c4356d,0xb57f33c9,897299528,890778958,0xb555350e,0xb55cb504,
897581056,885273965,0xb5800000,0xb4c4b56d,897266760,873870717,0xb555b50e,880391544,896349380,0xb3c9357f,0xb4c4b56d,893826338,894776590,0xb4ac3571,877180283,897626953,892679477,0xb50e3555,892712245,894776590,890123605,0xb53e352c,897299528,883701105,885273965,0xb56234f1,896349380,0xb4953575,877147515,0xb5783479,890123605,0xb54e3518,
897581056,892679477,897581056,892679477,896873621,886781287,893793570,860435840,894776590,868824447,877147515,0xb5223546,891434310,0xb4793578,0xb4f13562,0xb57d3416,885273965,0xb50e3555,0xb56d34c4,0xb555b50e,868824447,0xb54e3518,0xb575b495,0xb479b578,0xb448357b,0xb5753495,0xb50eb555,888255842,0xb4f13562,0xb580b349,868857215,896644268,
0xb5353535,0xb56db4c4,892712245,896349380,0xb56234f1,0xb53eb52c,897561545,886781287,0xb57b3448,0xb4f1b562,894776590,0xb4953575,0xb57fb3c9,0xb416b57d,882193781,0xb55c3504,0xb56db4c4,877180283,0xb4c4356d,0xb57bb448,0xb546b522,889501020,0xb56234f1,0xb518b54e,0xb50eb555,893826338,0xb57bb448,868857215,0xb495b575,896644268,0xb522b546,893302060,
897594752,0xb5800000,13696,0xb5800000,0xb5803580,0xb580b349,0xb4c4356d,0xb567b4db,897594752,0xb57fb3c9,0xb5353535,0xb522b546,0xb5803580,0xb57db416,0xb56d34c4,0xb479b578,897594752,0xb57bb448,0xb5800000,877180283,0xb5803580,0xb578b479,0xb56db4c4,890811726,897594752,0xb575b495,0xb535b535,895661297,0xb5803580,0xb571b4ac,0xb4c4b56d,897626953,
897594752,0xb56db4c4,46464,896349380,0xb5803580,0xb567b4db,885306733,892089662,897594752,0xb562b4f1,892712245,882193781,0xb5803580,0xb55cb504,896382148,0xb416357d,897594752,0xb555b50e,897581056,0xb50e3555,0xb5803580,0xb54eb518,896349380,0xb55c3504,897594752,0xb546b522,892679477,0xb57f33c9,0xb5803580,0xb53eb52c,885273965,0xb571b4ac,
46464,897581056,46464,897581056,868857215,897397782,888255842,895235332,877180283,896873621,894809358,888223074,882226549,895956187,897561545,0xb3493580,885306733,894776590,896349380,0xb50e3555,888255842,893269292,891434310,0xb56734db,890156373,891434310,877147515,0xb57fb3c9,891467078,889468252,0xb4953575,0xb54eb518,
892712245,885273965,0xb5353535,0xb4c4b56d,893826338,880358776,0xb5753495,873903485,894809358,868824447,0xb57bb448,891467078,895661297,0xb3493580,0xb546b522,896644268,896382148,0xb448357b,0xb4c4b56d,897266760,896906389,0xb4ac3571,868857215,893269292,897299528,0xb4f13562,890156373,882193781,897561545,0xb518354e,895661297,0xb4793578,
13696,0xb5800000,13696,0xb5800000,0xb448357b,0xb578b479,0xb50e3555,0xb54eb518,0xb4c4356d,0xb562b4f1,0xb56d34c4,0xb495b575,0xb50e3555,0xb53eb52c,0xb57bb448,883733873,0xb5353535,0xb50eb555,0xb535b535,894809358,0xb555350e,0xb4acb571,0xb448b57b,897594185,0xb56d34c4,0xb3c9b57f,885306733,893793570,0xb57b3448,873903485,894809358,880358776,
0xb5800000,885306733,897581056,0xb4c4356d,0xb57bb448,890811726,894776590,0xb55c3504,0xb56db4c4,893826338,885273965,0xb57fb3c9,0xb555b50e,895988955,0xb448357b,0xb53eb52c,0xb535b535,897299528,0xb5353535,0xb448b57b,0xb50eb555,897594185,0xb57b3448,886814055,0xb4c4b56d,896873621,0xb56db4c4,895661297,0xb448b57b,895235332,0xb50eb555,897397782,
46464,897581056,46464,897581056,882226549,896611500,891467078,893269292,890156373,893793570,897299528,868824447,893826338,889468252,895628529,0xb518354e,896382148,877147515,885273965,0xb57b3448,897561545,0xb416357d,0xb4953575,0xb55cb504,897266760,0xb4f13562,0xb555350e,0xb495b575,895628529,0xb53e352c,0xb57fb3c9,886814055,
892679477,0xb56d34c4,0xb535b535,896382148,888223074,0xb5803349,0xb3c9b57f,896611500,877147515,0xb575b495,890156373,888223074,0xb3c9357f,0xb54eb518,896906389,0xb4793578,0xb4c4356d,0xb50eb555,896349380,0xb555350e,0xb5223546,0xb479b578,888223074,0xb57db416,0xb555350e,868857215,0xb448357b,0xb522b546,0xb5753495,886814055,0xb5463522,860468608,
897594752,0xb535b535,13696,0xb535b535,0xb5803580,0xb52cb53e,0xb4c4356d,0xb4acb571,897594752,0xb522b546,0xb5353535,868857215,0xb5803580,0xb518b54e,0xb56d34c4,889501020,897594752,0xb50eb555,0xb5800000,894809358,0xb5803580,0xb504b55c,0xb56db4c4,897430550,897594752,0xb4f1b562,0xb535b535,896873621,0xb5803580,0xb4dbb567,0xb4c4b56d,893269292,
897594752,0xb4c4b56d,46464,885273965,0xb5803580,0xb4acb571,885306733,0xb3493580,897594752,0xb495b575,892712245,0xb4f13562,0xb5803580,0xb479b578,896382148,0xb54e3518,897594752,0xb448b57b,897581056,0xb57b3448,0xb5803580,0xb416b57d,896349380,0xb578b479,897594752,0xb3c9b57f,892679477,0xb546b522,0xb5803580,0xb349b580,885273965,0xb4dbb567,
897581056,0xb5353535,897581056,0xb5353535,897528777,0xb54e3518,895628529,0xb5783479,897266760,0xb56234f1,890123605,0xb575b495,896873621,0xb57134ac,868824447,0xb52cb53e,896349380,0xb57b3448,0xb4c4356d,0xb448b57b,895628529,0xb5803349,0xb5463522,883733873,894776590,0xb57fb3c9,0xb57b3448,893826338,893793570,0xb578b479,0xb575b495,897430550,
892679477,0xb56db4c4,0xb535b535,896349380,891434310,0xb55cb504,0xb495b575,890778958,890123605,0xb546b522,877180283,868824447,888223074,0xb52cb53e,891467078,0xb4db3567,885273965,0xb50eb555,896382148,0xb555350e,882193781,0xb4dbb567,897528777,0xb5803349,877147515,0xb495b575,894776590,0xb562b4f1,868824447,0xb416b57d,888223074,0xb504b55c,
46464,892679477,46464,892679477,877180283,889468252,890156373,873870717,885306733,882193781,896382148,0xb4f13562,890156373,860435840,897266760,0xb56734db,892712245,0xb448357b,892679477,0xb57bb448,894809358,0xb4db3567,877147515,0xb52cb53e,896382148,0xb5223546,0xb4c4356d,0xb3c9b57f,897299528,0xb54e3518,0xb555350e,889501020,
897581056,0xb56d34c4,0xb5800000,896382148,897266760,0xb57d3416,0xb555b50e,897070201,896349380,0xb57fb3c9,0xb4c4b56d,891434310,894776590,0xb571b4ac,877180283,860435840,892679477,0xb555b50e,892712245,0xb50e3555,890123605,0xb52cb53e,897299528,0xb57134ac,885273965,0xb4f1b562,896349380,0xb575b495,877147515,0xb479b578,890123605,0xb518b54e,
0xb5800000,892712245,0xb5800000,892712245,0xb575b495,895988955,0xb546b522,897626953,0xb555b50e,897561545,0xb448b57b,893793570,0xb522b546,897070201,888255842,873870717,0xb4c4b56d,894776590,896382148,0xb50e3555,0xb3c9b57f,890778958,896873621,0xb5783479,877180283,882193781,890123605,0xb562b4f1,888255842,0xb3493580,0xb3c9357f,0xb4acb571,
892712245,0xb4c4356d,0xb5353535,885306733,895661297,0xb52c353e,0xb57f33c9,895988955,897299528,0xb56234f1,0xb555b50e,896873621,897528777,0xb57d3416,0xb495b575,889468252,896349380,0xb57bb448,885306733,0xb448357b,893793570,0xb55cb504,895661297,0xb54e3518,890123605,0xb522b546,897266760,0xb57fb3c9,882193781,0xb4acb571,891434310,0xb52cb53e,
897594752,46464,13696,46464,0xb5803580,860468608,0xb4c4356d,886814055,897594752,868857215,0xb5353535,893826338,0xb5803580,873903485,0xb56d34c4,897102969,897594752,877180283,0xb5800000,897266760,0xb5803580,880391544,0xb56db4c4,894317848,897594752,882226549,0xb535b535,888223074,0xb5803580,883733873,0xb4c4b56d,860435840,
897594752,885306733,46464,0xb4c4356d,0xb5803580,886814055,885306733,0xb53e352c,897594752,888255842,892712245,0xb5753495,0xb5803580,889501020,896382148,0xb57db416,897594752,890156373,897581056,0xb555b50e,0xb5803580,890811726,896349380,0xb504b55c,897594752,891467078,892679477,0xb3c9b57f,0xb5803580,892122430,885273965,883733873,
13696,46464,13696,46464,0xb3c9357f,873903485,0xb4f13562,889501020,0xb448357b,882226549,0xb555350e,895661297,0xb4953575,886814055,0xb57f33c9,897594185,0xb4c4356d,890156373,0xb56db4c4,894776590,0xb4f13562,892122430,0xb522b546,886781287,0xb50e3555,893826338,0xb448b57b,0xb3c9357f,0xb5223546,895268100,882226549,0xb518354e,
0xb5353535,896382148,892712245,0xb56d34c4,0xb5463522,897102969,896906389,0xb57db416,0xb555350e,897561545,897266760,0xb546b522,0xb56234f1,897594185,893793570,0xb4acb571,0xb56d34c4,897266760,885273965,877180283,0xb5753495,896611500,0xb3c9357f,892122430,0xb57b3448,895628529,0xb50e3555,896906389,0xb57f33c9,894317848,0xb56234f1,897070201,
13696,46464,13696,46464,0xb448357b,880391544,0xb50e3555,890811726,0xb4c4356d,888255842,0xb56d34c4,896906389,0xb50e3555,892122430,0xb57bb448,896611500,0xb5353535,894809358,0xb535b535,890123605,0xb555350e,896644268,0xb448b57b,0xb3493580,0xb56d34c4,897561545,885306733,0xb5223546,0xb57b3448,897397782,894809358,0xb5783479,
0xb5800000,896349380,897581056,0xb56db4c4,0xb57bb448,894317848,894776590,0xb504b55c,0xb56db4c4,891434310,885273965,868857215,0xb555b50e,886781287,0xb448357b,892122430,0xb535b535,877147515,0xb5353535,897299528,0xb50eb555,0xb3493580,0xb57b3448,895956187,0xb4c4b56d,0xb4953575,0xb56db4c4,888223074,0xb448b57b,0xb504355c,0xb50eb555,0xb416357d,
13696,46464,13696,46464,0xb4953575,883733873,0xb5223546,892122430,0xb50e3555,891467078,0xb57b3448,897561545,0xb5463522,895268100,0xb562b4f1,894317848,0xb56d34c4,897299528,0xb4c4b56d,877147515,0xb57f33c9,897397782,882226549,0xb504355c,0xb57bb448,895628529,894809358,0xb5753495,0xb562b4f1,892089662,897528777,0xb567b4db,
0xb535b535,885273965,892679477,0xb4c4b56d,0xb4f1b562,860435840,868824447,883733873,0xb448b57b,0xb4953575,0xb50e3555,895661297,868857215,0xb518354e,0xb5753495,897070201,885306733,0xb555350e,0xb56db4c4,890123605,891467078,0xb5783479,0xb4f1b562,0xb416357d,894809358,0xb57fb3c9,877180283,0xb5463522,896906389,0xb567b4db,893826338,0xb580b349,
897594752,892712245,13696,892712245,0xb5803580,893302060,0xb4c4356d,896644268,897594752,893826338,0xb5353535,897528777,0xb5803580,894350616,0xb56d34c4,895235332,897594752,894809358,0xb5800000,890123605,0xb5803580,895268100,0xb56db4c4,873870717,897594752,895661297,0xb535b535,0xb4953575,0xb5803580,895988955,0xb4c4b56d,0xb52c353e,
897594752,896382148,46464,0xb56d34c4,0xb5803580,896644268,885306733,0xb580b349,897594752,896906389,892712245,0xb562b4f1,0xb5803580,897102969,896382148,0xb518b54e,897594752,897299528,897581056,0xb448b57b,0xb5803580,897430550,896349380,880391544,897594752,897561545,892679477,891467078,0xb5803580,897626953,885273965,895988955,
0xb5800000,892679477,0xb5800000,892679477,0xb57fb3c9,890778958,0xb562b4f1,880358776,0xb57bb448,888223074,0xb50eb555,0xb4953575,0xb575b495,883701105,0xb3c9b57f,0xb53e352c,0xb56db4c4,877147515,885306733,0xb57b3448,0xb562b4f1,860435840,893826338,0xb571b4ac,0xb555b50e,0xb3c9357f,897299528,0xb522b546,0xb546b522,0xb4793578,896873621,0xb416b57d,
0xb535b535,0xb4c4356d,892679477,885306733,0xb522b546,0xb504355c,882193781,894350616,0xb50eb555,0xb5223546,0xb448357b,897561545,0xb4f1b562,0xb53e352c,0xb5223546,895956187,0xb4c4b56d,0xb555350e,0xb56d34c4,890123605,0xb495b575,0xb56734db,0xb57fb3c9,860435840,0xb448b57b,0xb5753495,0xb555b50e,0xb4f13562,0xb3c9b57f,0xb57d3416,0xb4f1b562,0xb55c3504,
46464,0xb5353535,46464,0xb5353535,877180283,0xb55c3504,890156373,0xb57d3416,885306733,0xb5753495,896382148,0xb562b4f1,890156373,0xb5803349,897266760,0xb4dbb567,892712245,0xb57bb448,892679477,877180283,894809358,0xb567b4db,877147515,893302060,896382148,0xb546b522,0xb4c4356d,897561545,897299528,0xb518b54e,0xb555350e,895235332,
897581056,0xb4c4b56d,0xb5800000,885273965,897266760,0xb416b57d,0xb555b50e,0xb4793578,896349380,868857215,0xb4c4b56d,0xb5463522,894776590,883733873,877180283,0xb5803349,892679477,890156373,892712245,0xb555b50e,890123605,893302060,897299528,0xb4acb571,885273965,895661297,896349380,882226549,877147515,897102969,890123605,894350616,
897581056,0xb535b535,897581056,0xb535b535,896873621,0xb4dbb567,893793570,0xb349b580,894776590,0xb3c9b57f,877147515,891467078,891434310,880391544,0xb4f13562,897430550,885273965,890156373,0xb56d34c4,894776590,868824447,894350616,0xb575b495,880358776,0xb448357b,896906389,0xb50eb555,0xb4f13562,0xb4f13562,897594185,868857215,0xb57134ac,
0xb5353535,896349380,892712245,0xb56db4c4,0xb56234f1,893269292,897561545,0xb4dbb567,0xb57b3448,888223074,894776590,882226549,0xb57fb3c9,873870717,882193781,895268100,0xb56db4c4,0xb448357b,0xb4c4356d,897266760,0xb546b522,0xb504355c,0xb56234f1,890778958,0xb50eb555,0xb5463522,0xb57bb448,0xb3c9357f,0xb495b575,0xb57134ac,0xb522b546,0xb53e352c,
897594752,897581056,13696,897581056,0xb5803580,897594185,0xb4c4356d,895956187,897594752,897528777,0xb5353535,891434310,0xb5803580,897397782,0xb56d34c4,880358776,897594752,897266760,0xb5800000,0xb448357b,0xb5803580,897070201,0xb56db4c4,0xb518354e,897594752,896873621,0xb535b535,0xb56234f1,0xb5803580,896611500,0xb4c4b56d,0xb5803349,
897594752,896349380,46464,0xb56db4c4,0xb5803580,895956187,885306733,0xb52cb53e,897594752,895628529,892712245,0xb495b575,0xb5803580,895235332,896382148,873903485,897594752,894776590,897581056,890156373,0xb5803580,894317848,896349380,895268100,897594752,893793570,892679477,897561545,0xb5803580,893269292,885273965,896611500,
46464,0xb5800000,46464,0xb5800000,868857215,0xb57db416,888255842,0xb55cb504,877180283,0xb575b495,894809358,0xb4f1b562,882226549,0xb567b4db,897561545,860468608,885306733,0xb555b50e,896349380,890156373,888255842,0xb53eb52c,891434310,895988955,890156373,0xb522b546,877147515,897528777,891467078,0xb504b55c,0xb4953575,894317848,
892712245,0xb4c4b56d,0xb5353535,885273965,893826338,0xb479b578,0xb5753495,0xb416357d,894809358,0xb3c9b57f,0xb57bb448,0xb5223546,895661297,860468608,0xb546b522,0xb57134ac,896382148,877180283,0xb4c4b56d,0xb57bb448,896906389,883733873,868857215,0xb53eb52c,897299528,888255842,890156373,0xb495b575,897561545,890811726,895661297,880391544,
13696,897581056,13696,897581056,0xb448357b,897070201,0xb50e3555,894317848,0xb4c4356d,895628529,0xb56d34c4,882193781,0xb50e3555,893269292,0xb57bb448,0xb4ac3571,0xb5353535,890123605,0xb535b535,0xb555350e,0xb555350e,883701105,0xb448b57b,0xb580b349,0xb56d34c4,868824447,885306733,0xb546b522,0xb57b3448,0xb416357d,894809358,0xb479b578,
0xb5800000,0xb4c4356d,897581056,885306733,0xb57bb448,0xb518354e,894776590,895268100,0xb56db4c4,0xb5463522,885273965,897528777,0xb555b50e,0xb56734db,0xb448357b,893269292,0xb535b535,0xb57b3448,0xb5353535,877147515,0xb50eb555,0xb580b349,0xb57b3448,0xb4db3567,0xb4c4b56d,0xb575b495,0xb56db4c4,0xb56234f1,0xb448b57b,0xb55cb504,0xb50eb555,0xb57db416,
46464,0xb5800000,46464,0xb5800000,882226549,0xb571b4ac,891467078,0xb53eb52c,890156373,0xb546b522,897299528,0xb3c9b57f,893826338,0xb504b55c,895628529,890811726,896382148,0xb448b57b,885273965,897299528,897561545,873903485,0xb4953575,895235332,897266760,888255842,0xb555350e,882193781,895628529,893302060,0xb57fb3c9,0xb4db3567,
892679477,896382148,0xb535b535,0xb56d34c4,888223074,897626953,0xb3c9b57f,0xb571b4ac,877147515,896873621,890156373,0xb4f1b562,0xb3c9357f,894317848,896906389,880391544,0xb4c4356d,890123605,896349380,894809358,0xb5223546,880358776,888223074,897397782,0xb555350e,0xb3c9357f,0xb448357b,891434310,0xb5753495,0xb4db3567,0xb5463522,0xb3493580,
897594752,892679477,13696,892679477,0xb5803580,892089662,0xb4c4356d,883701105,897594752,891434310,0xb5353535,0xb3c9357f,0xb5803580,890778958,0xb56d34c4,0xb504355c,897594752,890123605,0xb5800000,0xb555350e,0xb5803580,889468252,0xb56db4c4,0xb57d3416,897594752,888223074,0xb535b535,0xb575b495,0xb5803580,886781287,0xb4c4b56d,0xb53eb52c,
897594752,885273965,46464,0xb4c4b56d,0xb5803580,883701105,885306733,860468608,897594752,882193781,892712245,888255842,0xb5803580,880358776,896382148,894350616,897594752,877147515,897581056,897299528,0xb5803580,873870717,896349380,897070201,897594752,868824447,892679477,893793570,0xb5803580,860435840,885273965,886781287,
897581056,892712245,897581056,892712245,897528777,894350616,895628529,897102969,897266760,895661297,890123605,896873621,896873621,896644268,868824447,892089662,896349380,897299528,0xb4c4356d,877147515,895628529,897626953,0xb5463522,0xb4ac3571,894776590,897528777,0xb57b3448,0xb5463522,893793570,897070201,0xb575b495,0xb57d3416,
892679477,896349380,0xb535b535,0xb56db4c4,891434310,895235332,0xb495b575,0xb518b54e,890123605,893793570,877180283,0xb3c9b57f,888223074,892089662,891467078,886814055,885273965,890123605,896382148,894809358,882193781,886781287,897528777,897626953,877147515,882193781,894776590,895628529,868824447,873870717,888223074,889468252,
46464,0xb535b535,46464,0xb535b535,877180283,0xb504b55c,890156373,0xb416b57d,885306733,0xb495b575,896382148,888255842,890156373,0xb349b580,897266760,895988955,892712245,877180283,892679477,897266760,894809358,886814055,877147515,892089662,896382148,891467078,0xb4c4356d,868824447,897299528,894350616,0xb555350e,0xb504355c,
897581056,896382148,0xb5800000,0xb56d34c4,897266760,897430550,0xb555b50e,0xb578b479,896349380,897528777,0xb4c4b56d,0xb522b546,894776590,896611500,877180283,0xb349b580,892679477,894776590,892712245,890156373,890123605,892089662,897299528,896644268,885273965,888223074,896349380,896873621,877147515,880358776,890123605,890778958,
0xb5800000,0xb5353535,0xb5800000,0xb5353535,0xb575b495,0xb56734db,0xb546b522,0xb5803349,0xb555b50e,0xb57f33c9,0xb448b57b,0xb546b522,0xb522b546,0xb578b479,888255842,0xb416b57d,0xb4c4b56d,0xb555b50e,896382148,890156373,0xb3c9b57f,0xb518b54e,896873621,897102969,877180283,0xb495b575,890123605,895628529,888255842,860468608,0xb3c9357f,883701105,
892712245,885306733,0xb5353535,0xb4c4356d,895661297,892122430,0xb57f33c9,0xb56734db,897299528,895661297,0xb555b50e,0xb575b495,897528777,897430550,0xb495b575,0xb504b55c,896349380,897266760,885306733,877180283,893793570,895235332,895661297,894350616,890123605,891434310,897266760,897528777,882193781,883701105,891434310,892089662,
13696,13696,13696,13696,0xb5353535,0xb53e352c,0xb56d34c4,0xb57134ac,0xb5800000,0xb57fb3c9,0xb535b535,0xb522b546,0xb535b535,0xb518b54e,885306733,889501020,46464,877180283,897581056,897266760,892712245,895268100,885273965,873870717,897581056,896873621,0xb5353535,0xb56234f1,892679477,886781287,0xb56db4c4,0xb53eb52c,
13696,0xb4c4356d,46464,885306733,0xb5353535,0xb57134ac,896382148,897594185,0xb5800000,0xb562b4f1,892679477,882193781,0xb535b535,0xb479b578,0xb4c4356d,0xb54e3518,46464,890156373,0xb5800000,0xb555b50e,892712245,897430550,0xb4c4b56d,880391544,897581056,893793570,892712245,897561545,892679477,860435840,896349380,886781287,
13696,13696,13696,13696,0xb5463522,0xb54e3518,0xb5753495,0xb5783479,0xb57bb448,0xb575b495,0xb50eb555,0xb4f1b562,0xb4f1b562,0xb4acb571,891467078,893302060,885306733,890156373,896349380,894776590,896906389,897626953,0xb3c9357f,0xb4ac3571,894776590,891434310,0xb57b3448,0xb57fb3c9,868824447,0xb4793578,0xb4f1b562,0xb416b57d,
0xb5353535,0xb56d34c4,892712245,896382148,0xb57fb3c9,0xb55cb504,895628529,890778958,0xb50eb555,0xb3c9b57f,0xb448357b,0xb5223546,882226549,893302060,0xb57f33c9,0xb567b4db,896382148,897266760,0xb4c4b56d,877180283,895628529,886781287,893826338,897626953,877147515,0xb4f13562,894776590,882193781,0xb5223546,0xb57d3416,0xb4953575,0xb55c3504,
13696,13696,13696,13696,0xb555350e,0xb55c3504,0xb57b3448,0xb57d3416,0xb56db4c4,0xb562b4f1,0xb4c4b56d,0xb495b575,0xb448b57b,0xb349b580,894809358,895988955,892712245,894809358,892679477,890123605,897266760,895956187,0xb50e3555,0xb53e352c,885273965,868824447,0xb56db4c4,0xb546b522,0xb50e3555,0xb54e3518,877180283,889501020,
0xb5800000,0xb56db4c4,897581056,896349380,0xb50eb555,0xb416b57d,877147515,0xb4793578,885306733,893826338,0xb56d34c4,0xb57fb3c9,897299528,896611500,0xb50eb555,0xb349b580,892679477,877147515,892712245,897299528,0xb448357b,0xb53e352c,894776590,883701105,0xb56d34c4,0xb575b495,0xb4c4356d,0xb56234f1,0xb555b50e,0xb479b578,0xb57bb448,0xb518b54e,
13696,13696,13696,13696,0xb56234f1,0xb56734db,0xb57f33c9,0xb5803349,0xb555b50e,0xb546b522,0xb448b57b,0xb3c9b57f,868857215,880391544,896906389,897430550,896382148,897299528,885273965,877147515,893793570,890778958,0xb56234f1,0xb5783479,0xb448357b,0xb4f13562,0xb50eb555,0xb495b575,0xb5753495,0xb580b349,893826338,896644268,
0xb535b535,0xb4c4b56d,892679477,885273965,882226549,892122430,0xb5223546,0xb56734db,897299528,896873621,0xb555b50e,0xb4f1b562,891434310,873870717,888255842,895268100,0xb4c4356d,0xb555350e,896349380,890123605,0xb57f33c9,0xb55cb504,0xb4953575,0xb54e3518,0xb50eb555,868857215,0xb57bb448,0xb522b546,888255842,896644268,868857215,893302060,
13696,0xb5353535,13696,0xb5353535,0xb5353535,0xb580b349,0xb56d34c4,0xb567b4db,0xb5800000,0xb522b546,0xb535b535,868857215,0xb535b535,873903485,885306733,897102969,46464,894809358,897581056,890123605,892712245,897070201,885273965,0xb518354e,897581056,888223074,0xb5353535,0xb575b495,892679477,0xb4ac3571,0xb56db4c4,0xb349b580,
13696,0xb56d34c4,46464,896382148,0xb5353535,0xb567b4db,896382148,892089662,0xb5800000,0xb495b575,892679477,0xb4f13562,0xb535b535,889501020,0xb4c4356d,0xb57db416,46464,897299528,0xb5800000,0xb448b57b,892712245,894317848,0xb4c4b56d,895268100,897581056,868824447,892712245,893793570,892679477,0xb52c353e,896349380,0xb4ac3571,
0xb5800000,0xb535b535,0xb5800000,0xb535b535,0xb522b546,873903485,0xb495b575,889501020,877180283,895661297,894809358,896873621,895661297,895956187,893793570,0xb3493580,896349380,877147515,0xb4c4356d,0xb57b3448,882193781,0xb52c353e,0xb57fb3c9,0xb4dbb567,0xb50e3555,0xb57fb3c9,0xb448b57b,893826338,0xb57f33c9,0xb504b55c,895661297,894317848,
0xb535b535,885306733,892679477,0xb4c4356d,868857215,897102969,0xb4f13562,0xb57db416,894809358,893793570,0xb57bb448,0xb3c9b57f,896873621,0xb3493580,0xb3c9b57f,896644268,885273965,0xb555350e,896382148,890123605,0xb4f13562,0xb571b4ac,891434310,0xb52c353e,0xb57b3448,0xb495b575,0xb50e3555,0xb562b4f1,0xb546b522,890811726,0xb575b495,880391544,
46464,892712245,46464,892712245,894809358,897070201,897299528,894317848,896349380,882193781,885273965,0xb4f13562,877147515,0xb52c353e,0xb555350e,0xb571b4ac,0xb5353535,0xb57bb448,0xb535b535,877180283,0xb57bb448,0xb4acb571,890156373,897594185,0xb4c4b56d,891467078,896349380,868824447,890156373,897397782,0xb448357b,0xb5783479,
897581056,885273965,0xb5800000,0xb4c4b56d,890123605,0xb518354e,0xb448b57b,895268100,0xb4c4356d,0xb57fb3c9,896382148,891434310,0xb57b3448,0xb4dbb567,890123605,0xb52c353e,0xb535b535,890156373,0xb5353535,0xb555b50e,877180283,897594185,0xb555b50e,886814055,896382148,888223074,885306733,896873621,894776590,0xb504355c,897266760,0xb416357d,
897581056,892679477,897581056,892679477,888223074,0xb4ac3571,868824447,0xb52c353e,0xb50e3555,0xb57f33c9,0xb57b3448,0xb546b522,0xb57fb3c9,0xb504b55c,0xb495b575,890811726,0xb4c4b56d,890156373,896382148,894776590,891467078,897397782,888223074,0xb504355c,897266760,882193781,0xb555350e,0xb562b4f1,882193781,0xb53e352c,0xb522b546,886814055,
0xb5353535,0xb56db4c4,892712245,896349380,0xb575b495,0xb349b580,893793570,0xb4ac3571,0xb448b57b,895661297,0xb50e3555,0xb575b495,893826338,894317848,0xb562b4f1,880391544,896349380,0xb448357b,885306733,897266760,868824447,0xb5783479,896873621,0xb416357d,0xb555350e,0xb522b546,0xb448357b,0xb57fb3c9,0xb562b4f1,886814055,0xb57fb3c9,860468608,
13696,0xb5800000,13696,0xb5800000,0xb5353535,0xb52cb53e,0xb56d34c4,0xb4acb571,0xb5800000,868857215,0xb535b535,893826338,0xb535b535,894350616,885306733,895235332,46464,897266760,897581056,0xb448357b,892712245,889468252,885273965,0xb57d3416,897581056,0xb4953575,0xb5353535,0xb4f1b562,892679477,0xb56734db,0xb56db4c4,892122430,
13696,0xb56db4c4,46464,896349380,0xb5353535,0xb4acb571,896382148,0xb3493580,0xb5800000,888255842,892679477,0xb5753495,0xb535b535,897102969,0xb4c4356d,0xb518b54e,46464,894776590,0xb5800000,890156373,892712245,873870717,0xb4c4b56d,897070201,897581056,0xb5223546,892712245,868824447,892679477,0xb5803349,896349380,0xb56734db,
46464,897581056,46464,897581056,893826338,890778958,896906389,880358776,897266760,0xb4953575,890123605,0xb56234f1,888223074,0xb57134ac,0xb5223546,0xb52cb53e,0xb4c4356d,0xb555b50e,0xb56db4c4,890156373,0xb5753495,0xb349b580,868857215,896611500,0xb555b50e,893826338,897299528,0xb3c9357f,0xb3c9b57f,897070201,888223074,0xb57d3416,
892712245,885273965,0xb5353535,0xb4c4b56d,897528777,0xb504355c,0xb562b4f1,894350616,890123605,0xb57f33c9,877180283,893793570,0xb4953575,0xb52cb53e,897561545,0xb4db3567,0xb56d34c4,877180283,885273965,0xb57bb448,0xb562b4f1,895988955,0xb5463522,0xb349b580,0xb448b57b,895628529,0xb555b50e,896906389,891467078,873870717,882226549,889468252,
13696,0xb5800000,13696,0xb5800000,0xb555350e,0xb504b55c,0xb57b3448,0xb416b57d,0xb56db4c4,888255842,0xb4c4b56d,896906389,0xb448b57b,897626953,894809358,886781287,892712245,890123605,892679477,0xb555350e,897266760,0xb4db3567,0xb50e3555,0xb52cb53e,885273965,0xb57f33c9,0xb56db4c4,891467078,0xb50e3555,0xb518b54e,877180283,895235332,
0xb5800000,885306733,897581056,0xb4c4356d,0xb50eb555,897430550,877147515,0xb578b479,885306733,891434310,0xb56d34c4,868857215,897299528,0xb4ac3571,0xb50eb555,897626953,892679477,0xb57b3448,892712245,877147515,0xb448357b,0xb52cb53e,894776590,0xb57134ac,0xb56d34c4,882226549,0xb4c4356d,0xb4f1b562,0xb555b50e,897102969,0xb57bb448,894350616,
46464,897581056,46464,897581056,895661297,886781287,897561545,860435840,894776590,0xb5223546,877147515,0xb57f33c9,0xb3c9357f,0xb578b479,0xb5753495,0xb416b57d,0xb56d34c4,0xb448b57b,0xb4c4b56d,897299528,0xb546b522,894350616,895661297,880358776,877180283,895628529,890123605,0xb5753495,896906389,0xb3493580,0xb5463522,0xb4acb571,
892679477,0xb56d34c4,0xb535b535,896382148,0xb4953575,0xb53eb52c,891467078,886781287,0xb57b3448,882226549,894776590,0xb56234f1,0xb522b546,897430550,0xb4f13562,0xb504b55c,885306733,890123605,0xb56db4c4,894809358,897561545,0xb504355c,882226549,890778958,890123605,0xb57fb3c9,897266760,0xb5463522,0xb4f13562,0xb4acb571,0xb3c9357f,0xb52cb53e,
13696,0xb535b535,13696,0xb535b535,0xb5353535,860468608,0xb56d34c4,886814055,0xb5800000,893826338,0xb535b535,897528777,0xb535b535,897397782,885306733,880358776,46464,890123605,897581056,0xb555350e,892712245,0xb4793578,885273965,0xb54eb518,897581056,0xb56234f1,0xb5353535,882226549,892679477,0xb571b4ac,0xb56db4c4,897626953,
13696,0xb4c4b56d,46464,885273965,0xb5353535,886814055,896382148,0xb53e352c,0xb5800000,896906389,892679477,0xb562b4f1,0xb535b535,895235332,0xb4c4356d,873903485,46464,877147515,0xb5800000,897299528,892712245,0xb518354e,0xb4c4b56d,889468252,897581056,0xb57f33c9,892712245,0xb5223546,892679477,0xb53eb52c,896349380,0xb571b4ac,
897581056,0xb5353535,897581056,0xb5353535,891434310,0xb57db416,882193781,0xb55cb504,0xb448357b,0xb4f1b562,0xb555350e,882226549,0xb56234f1,886814055,0xb546b522,897594185,0xb56db4c4,897299528,885306733,877147515,0xb495b575,893269292,897528777,0xb56734db,890156373,0xb3c9357f,877147515,0xb522b546,897561545,0xb55c3504,0xb56234f1,890811726,
892679477,0xb56db4c4,0xb535b535,896349380,0xb3c9357f,0xb479b578,888255842,0xb416357d,0xb555350e,891467078,897266760,0xb57f33c9,0xb575b495,897594185,868824447,0xb4acb571,0xb4c4b56d,890123605,0xb56d34c4,894809358,888255842,0xb4ac3571,0xb522b546,893269292,897299528,0xb5753495,890156373,0xb4f13562,893793570,0xb54eb518,896873621,0xb578b479,
46464,892679477,46464,892679477,894809358,0xb4793578,897299528,0xb518354e,896349380,0xb5753495,885273965,0xb562b4f1,877147515,0xb53eb52c,0xb555350e,883733873,0xb5353535,877180283,0xb535b535,897266760,0xb57bb448,896644268,890156373,0xb3493580,0xb4c4b56d,893793570,896349380,0xb57f33c9,890156373,0xb416357d,0xb448357b,0xb479b578,
897581056,0xb56d34c4,0xb5800000,896382148,890123605,0xb54eb518,0xb448b57b,889468252,0xb4c4356d,868857215,896382148,0xb5463522,0xb57b3448,895988955,890123605,0xb53eb52c,0xb535b535,894776590,0xb5353535,890156373,877180283,0xb3493580,0xb555b50e,895956187,896382148,0xb56234f1,885306733,0xb4953575,894776590,0xb55cb504,897266760,0xb57db416,
0xb5800000,892712245,0xb5800000,892712245,0xb4f1b562,896611500,0xb3c9b57f,893269292,890156373,868824447,897299528,0xb5223546,897528777,0xb55c3504,882193781,0xb54eb518,885273965,0xb555b50e,0xb56d34c4,890156373,0xb5223546,873903485,0xb4f1b562,895235332,0xb57bb448,896906389,894809358,0xb4f13562,0xb495b575,892089662,891434310,0xb567b4db,
892712245,0xb4c4356d,0xb5353535,885306733,896873621,0xb5803349,0xb546b522,896611500,877147515,0xb4f1b562,890156373,0xb4953575,0xb5463522,890811726,895628529,0xb578b479,0xb56db4c4,897266760,0xb4c4356d,877180283,0xb3c9b57f,880358776,0xb575b495,897397782,894809358,0xb5463522,877180283,0xb3c9357f,895628529,0xb567b4db,897528777,0xb580b349,
13696,46464,13696,46464,0xb5353535,893302060,0xb56d34c4,896644268,0xb5800000,897528777,0xb535b535,891434310,0xb535b535,890778958,885306733,0xb504355c,46464,0xb448357b,897581056,0xb57bb448,892712245,0xb55c3504,885273965,0xb416b57d,897581056,0xb575b495,0xb5353535,895661297,892679477,0xb4dbb567,0xb56db4c4,893269292,
13696,885306733,46464,0xb4c4356d,0xb5353535,896644268,896382148,0xb580b349,0xb5800000,895628529,892679477,0xb495b575,0xb535b535,880358776,0xb4c4356d,894350616,46464,0xb50e3555,0xb5800000,894776590,892712245,0xb57d3416,0xb4c4b56d,0xb4793578,897581056,0xb546b522,892712245,0xb57f33c9,892679477,0xb349b580,896349380,0xb4dbb567,
13696,46464,13696,46464,0xb5463522,894350616,0xb5753495,897102969,0xb57bb448,896873621,0xb50eb555,888223074,0xb4f1b562,883701105,891467078,0xb53e352c,885306733,0xb50e3555,896349380,0xb555b50e,896906389,0xb5803349,0xb3c9357f,883733873,894776590,0xb522b546,0xb57b3448,897528777,868824447,880391544,0xb4f1b562,873870717,
0xb5353535,896382148,892712245,0xb56d34c4,0xb57fb3c9,895235332,895628529,0xb518b54e,0xb50eb555,868824447,0xb448357b,891467078,882226549,0xb53e352c,0xb57f33c9,895956187,896382148,0xb57bb448,0xb4c4b56d,0xb448357b,895628529,0xb4dbb567,893826338,0xb5803349,877147515,888255842,894776590,0xb495b575,0xb5223546,897430550,0xb4953575,895268100,
13696,46464,13696,46464,0xb555350e,895268100,0xb57b3448,897430550,0xb56db4c4,895628529,0xb4c4b56d,882193781,0xb448b57b,860435840,894809358,0xb56734db,892712245,0xb555350e,892679477,0xb50eb555,897266760,0xb567b4db,0xb50e3555,893302060,885273965,0xb3c9b57f,0xb56db4c4,893793570,0xb50e3555,894350616,877180283,0xb504355c,
0xb5800000,896349380,897581056,0xb56db4c4,0xb50eb555,873870717,877147515,880391544,885306733,0xb5463522,0xb56d34c4,897528777,897299528,0xb571b4ac,0xb50eb555,860435840,892679477,0xb448b57b,892712245,0xb57b3448,0xb448357b,893302060,894776590,0xb4acb571,0xb56d34c4,896873621,0xb4c4356d,895661297,0xb555b50e,880358776,0xb57bb448,890778958,
13696,46464,13696,46464,0xb56234f1,895988955,0xb57f33c9,897626953,0xb555b50e,893793570,0xb448b57b,868824447,868857215,0xb4793578,896906389,0xb57d3416,896382148,0xb57b3448,885273965,0xb448b57b,893793570,0xb518b54e,0xb56234f1,897102969,0xb448357b,888255842,0xb50eb555,882193781,0xb5753495,897594185,893826338,0xb57134ac,
0xb535b535,885273965,892679477,0xb4c4b56d,882226549,0xb52c353e,0xb5223546,895988955,897299528,0xb575b495,0xb555b50e,888223074,891434310,0xb416b57d,888255842,0xb55c3504,0xb4c4356d,894809358,896349380,0xb50eb555,0xb57f33c9,895235332,0xb4953575,894350616,0xb50eb555,0xb3c9357f,0xb57bb448,891434310,888255842,0xb57134ac,868857215,0xb53e352c,
13696,892712245,13696,892712245,0xb5353535,897594185,0xb56d34c4,895956187,0xb5800000,891434310,0xb535b535,0xb3c9357f,0xb535b535,0xb416357d,885306733,0xb5783479,46464,0xb555350e,897581056,0xb50eb555,892712245,0xb578b479,885273965,890811726,897581056,0xb4f1b562,0xb5353535,896873621,892679477,883733873,0xb56db4c4,860435840,
13696,896382148,46464,0xb56d34c4,0xb5353535,895956187,896382148,0xb52cb53e,0xb5800000,882193781,892679477,888255842,0xb535b535,0xb504355c,0xb4c4356d,897397782,46464,0xb57b3448,0xb5800000,877147515,892712245,0xb54eb518,0xb4c4b56d,0xb55c3504,897581056,0xb3c9b57f,892712245,0xb546b522,892679477,892122430,896349380,883733873,
0xb5800000,892679477,0xb5800000,892679477,0xb522b546,0xb416357d,0xb495b575,0xb504355c,877180283,0xb56234f1,894809358,0xb575b495,895661297,0xb567b4db,893793570,860468608,896349380,0xb448b57b,0xb4c4356d,897299528,882193781,892122430,0xb57fb3c9,886781287,0xb50e3555,897528777,0xb448b57b,0xb5463522,0xb57f33c9,889468252,895661297,0xb54eb518,
0xb535b535,0xb4c4356d,892679477,885306733,868857215,0xb5783479,0xb4f13562,897397782,894809358,0xb546b522,0xb57bb448,868824447,896873621,860468608,0xb3c9b57f,0xb57134ac,885273965,894809358,896382148,0xb50eb555,0xb4f13562,896611500,891434310,892122430,0xb57b3448,882193781,0xb50e3555,895628529,0xb546b522,0xb518354e,0xb575b495,0xb4793578,
46464,0xb5353535,46464,0xb5353535,894809358,0xb578b479,897299528,0xb54eb518,896349380,0xb495b575,885273965,888255842,877147515,892122430,0xb555350e,896611500,0xb5353535,897266760,0xb535b535,0xb448357b,0xb57bb448,883701105,890156373,0xb580b349,0xb4c4b56d,0xb5223546,896349380,0xb3c9b57f,890156373,0xb57db416,0xb448357b,897102969,
897581056,0xb4c4b56d,0xb5800000,885273965,890123605,890811726,0xb448b57b,0xb55c3504,0xb4c4356d,897528777,896382148,0xb522b546,0xb57b3448,886781287,890123605,892122430,0xb535b535,0xb50e3555,0xb5353535,894776590,877180283,0xb580b349,0xb555b50e,0xb4db3567,896382148,0xb4f1b562,885306733,0xb575b495,894776590,889501020,897266760,873903485,
897581056,0xb535b535,897581056,0xb535b535,888223074,883733873,868824447,892122430,0xb50e3555,897561545,0xb57b3448,893793570,0xb57fb3c9,889468252,0xb495b575,0xb518354e,0xb4c4b56d,0xb50e3555,896382148,0xb555b50e,891467078,0xb57db416,888223074,889501020,897266760,0xb495b575,0xb555350e,895628529,882193781,893302060,0xb522b546,0xb4db3567,
0xb5353535,896349380,892712245,0xb56db4c4,0xb575b495,860435840,893793570,883733873,0xb448b57b,0xb56234f1,0xb50e3555,896873621,893826338,0xb54eb518,0xb562b4f1,0xb4793578,896349380,877180283,885306733,0xb57bb448,868824447,897102969,896873621,873903485,0xb555350e,891434310,0xb448357b,897528777,0xb562b4f1,0xb4db3567,0xb57fb3c9,0xb3493580,
13696,897581056,13696,897581056,0xb5353535,892089662,0xb56d34c4,883701105,0xb5800000,0xb3c9357f,0xb535b535,0xb5463522,0xb535b535,0xb54e3518,885306733,0xb55cb504,46464,0xb57bb448,897581056,877180283,892712245,0xb504b55c,885273965,897430550,897581056,882226549,0xb5353535,888223074,892679477,895988955,0xb56db4c4,0xb52c353e,
13696,896349380,46464,0xb56db4c4,0xb5353535,883701105,896382148,860468608,0xb5800000,0xb4f13562,892679477,896906389,0xb535b535,0xb5783479,0xb4c4356d,890778958,46464,0xb555b50e,0xb5800000,0xb50e3555,892712245,0xb416b57d,0xb4c4b56d,0xb578b479,897581056,891467078,892712245,0xb3c9b57f,892679477,897626953,896349380,895988955,
46464,0xb5800000,46464,0xb5800000,893826338,0xb518b54e,896906389,0xb479b578,897266760,882226549,890123605,895661297,888223074,896644268,0xb5223546,892089662,0xb4c4356d,894776590,0xb56db4c4,0xb50e3555,0xb5753495,860435840,868857215,0xb571b4ac,0xb555b50e,0xb5463522,897299528,868857215,0xb3c9b57f,0xb578b479,888223074,897430550,
892712245,0xb4c4b56d,0xb5353535,885273965,897528777,889501020,0xb562b4f1,0xb54e3518,890123605,897561545,877180283,0xb546b522,0xb4953575,892089662,897561545,886814055,0xb56d34c4,0xb448357b,885273965,897266760,0xb562b4f1,0xb56734db,0xb5463522,860435840,0xb448b57b,0xb562b4f1,0xb555b50e,0xb5753495,891467078,0xb416b57d,882226549,0xb504b55c,
13696,897581056,13696,897581056,0xb555350e,889468252,0xb57b3448,873870717,0xb56db4c4,0xb4f13562,0xb4c4b56d,0xb5753495,0xb448b57b,0xb5803349,894809358,0xb4dbb567,892712245,0xb50eb555,892679477,894809358,897266760,886814055,0xb50e3555,892089662,885273965,897561545,0xb56db4c4,0xb5223546,0xb50e3555,890778958,877180283,0xb55cb504,
0xb5800000,0xb4c4356d,897581056,885306733,0xb50eb555,0xb57d3416,877147515,897070201,885306733,0xb522b546,0xb56d34c4,0xb3c9357f,897299528,883733873,0xb50eb555,0xb5803349,892679477,897299528,892712245,0xb448b57b,0xb448357b,892089662,894776590,896644268,0xb56d34c4,0xb4953575,0xb4c4356d,888223074,0xb555b50e,0xb5783479,0xb57bb448,0xb54e3518,
46464,0xb5800000,46464,0xb5800000,895661297,0xb4dbb567,897561545,0xb349b580,894776590,891467078,877147515,897561545,0xb3c9357f,897070201,0xb5753495,873870717,0xb56d34c4,877147515,0xb4c4b56d,0xb57b3448,0xb546b522,0xb54e3518,895661297,0xb479b578,877180283,0xb562b4f1,890123605,896906389,896906389,860468608,0xb5463522,883701105,
892679477,896382148,0xb535b535,0xb56d34c4,0xb4953575,893269292,891467078,0xb4dbb567,0xb57b3448,0xb4953575,894776590,895661297,0xb522b546,0xb57d3416,0xb4f13562,889468252,885306733,0xb50eb555,0xb56db4c4,0xb555350e,897561545,889501020,882226549,0xb518b54e,890123605,897528777,897266760,893826338,0xb4f13562,883701105,0xb3c9357f,892089662,
13696,892679477,13696,892679477,0xb5353535,0xb3493580,0xb56d34c4,0xb4db3567,0xb5800000,0xb5463522,0xb535b535,0xb57fb3c9,0xb535b535,0xb57db416,885306733,0xb479b578,46464,0xb50eb555,897581056,894809358,892712245,880391544,885273965,894317848,897581056,895661297,0xb5353535,0xb4953575,892679477,896611500,0xb56db4c4,0xb5803349,
13696,885273965,46464,0xb4c4b56d,0xb5353535,0xb4db3567,896382148,893302060,0xb5800000,0xb5753495,892679477,895628529,0xb535b535,0xb55cb504,0xb4c4356d,0xb416357d,46464,0xb448b57b,0xb5800000,0xb57b3448,892712245,890811726,0xb4c4b56d,0xb504b55c,897581056,897561545,892712245,891467078,892679477,893269292,896349380,896611500,
897581056,892712245,897581056,892712245,891434310,897397782,882193781,895235332,0xb448357b,888223074,0xb555350e,0xb4953575,0xb56234f1,0xb4db3567,0xb546b522,0xb580b349,0xb56db4c4,0xb57b3448,885306733,0xb448b57b,0xb495b575,0xb53eb52c,897528777,895988955,890156373,868857215,877147515,891434310,897561545,895268100,0xb56234f1,0xb518354e,
892679477,896349380,0xb535b535,0xb56db4c4,0xb3c9357f,880358776,888255842,873903485,0xb555350e,0xb5223546,897266760,897561545,0xb575b495,0xb580b349,868824447,883701105,0xb4c4b56d,0xb50eb555,0xb56d34c4,0xb555350e,888255842,883733873,0xb522b546,0xb53eb52c,897299528,896906389,890156373,888255842,893793570,894317848,896873621,897070201,
46464,0xb535b535,46464,0xb535b535,894809358,880391544,897299528,890811726,896349380,896906389,885273965,895628529,877147515,893269292,0xb555350e,0xb4ac3571,0xb5353535,0xb448357b,0xb535b535,0xb57bb448,0xb57bb448,0xb57134ac,890156373,860468608,0xb4c4b56d,0xb546b522,896349380,897561545,890156373,873903485,0xb448357b,880358776,
897581056,896382148,0xb5800000,0xb56d34c4,890123605,894317848,0xb448b57b,0xb504b55c,0xb4c4356d,0xb3c9357f,896382148,893826338,0xb57b3448,0xb56734db,890123605,893269292,0xb535b535,0xb555b50e,0xb5353535,0xb50e3555,877180283,860468608,0xb555b50e,0xb567b4db,896382148,895661297,885306733,882226549,894776590,895235332,897266760,897397782,
0xb5800000,0xb5353535,0xb5800000,0xb5353535,0xb4f1b562,0xb571b4ac,0xb3c9b57f,0xb53eb52c,890156373,0xb3c9b57f,897299528,891467078,897528777,895268100,882193781,894317848,885273965,894776590,0xb56d34c4,0xb50e3555,0xb5223546,0xb416357d,0xb4f1b562,0xb55cb504,0xb57bb448,0xb5753495,894809358,888255842,0xb495b575,0xb52cb53e,891434310,895956187,
892712245,885306733,0xb5353535,0xb4c4356d,896873621,897626953,0xb546b522,0xb571b4ac,877147515,888223074,890156373,882226549,0xb5463522,0xb518354e,895628529,897070201,0xb56db4c4,0xb57bb448,0xb4c4356d,0xb448357b,0xb3c9b57f,0xb479b578,0xb575b495,0xb57db416,894809358,893826338,877180283,868857215,895628529,895956187,897528777,897594185,
13696,13696,13696,13696,0xb5800000,0xb580b349,0xb56db4c4,0xb567b4db,46464,868857215,892712245,893826338,897581056,897397782,885273965,880358776,13696,0xb448357b,0xb5800000,0xb57bb448,0xb5800000,0xb578b479,885306733,890811726,46464,882226549,892679477,888223074,897581056,896611500,0xb56d34c4,0xb5803349,
13696,0xb4c4356d,46464,885306733,0xb5800000,0xb567b4db,896349380,892089662,46464,888255842,0xb5353535,0xb5753495,897581056,895235332,0xb4c4b56d,873903485,13696,0xb50e3555,897581056,894776590,0xb5800000,0xb54eb518,0xb4c4356d,0xb55c3504,46464,891467078,0xb535b535,0xb3c9b57f,897581056,893269292,896382148,896611500,
13696,13696,13696,13696,0xb57fb3c9,0xb57db416,0xb562b4f1,0xb55cb504,877180283,882226549,894809358,895661297,896873621,895956187,868824447,0xb3493580,0xb4c4356d,0xb50e3555,0xb56db4c4,0xb555b50e,0xb562b4f1,0xb53eb52c,893826338,895988955,890156373,893826338,877147515,0xb3c9357f,893793570,889468252,0xb575b495,0xb54eb518,
0xb5353535,0xb56d34c4,892712245,896382148,0xb522b546,0xb479b578,882193781,0xb416357d,894809358,897561545,0xb57bb448,0xb546b522,888223074,0xb3493580,891467078,896644268,0xb56d34c4,0xb57bb448,885273965,0xb448357b,0xb495b575,883733873,0xb57fb3c9,0xb53eb52c,897299528,895628529,890156373,896906389,868824447,0xb518354e,888223074,0xb4793578,
13696,13696,13696,13696,0xb57bb448,0xb578b479,0xb555b50e,0xb54eb518,885306733,888255842,896382148,896906389,894776590,893269292,0xb448357b,0xb4ac3571,0xb5353535,0xb555350e,0xb535b535,0xb50eb555,0xb50eb555,0xb4acb571,897299528,897594185,896382148,897561545,0xb4c4356d,0xb5223546,877147515,0xb416357d,0xb50eb555,0xb479b578,
0xb5800000,0xb56db4c4,897581056,896349380,877180283,890811726,0xb50e3555,0xb55c3504,896349380,891434310,0xb4c4b56d,868857215,0xb50e3555,0xb56734db,897266760,893269292,0xb535b535,0xb448b57b,0xb5353535,0xb57b3448,894809358,897594185,0xb448b57b,886814055,885273965,0xb4953575,896349380,888223074,0xb57b3448,0xb55cb504,0xb555350e,0xb57db416,
13696,13696,13696,13696,0xb575b495,0xb571b4ac,0xb546b522,0xb53eb52c,890156373,891467078,897299528,897561545,891434310,889468252,0xb4f13562,0xb518354e,0xb56d34c4,0xb57b3448,0xb4c4b56d,0xb448b57b,0xb3c9b57f,873903485,896873621,895235332,897266760,895628529,0xb555350e,0xb5753495,0xb4f13562,0xb53e352c,868857215,886814055,
0xb535b535,0xb4c4b56d,892679477,885273965,895661297,897626953,0xb57f33c9,0xb571b4ac,877147515,0xb4953575,890156373,895661297,0xb57fb3c9,0xb54eb518,882193781,0xb4793578,885306733,894809358,0xb56db4c4,0xb50eb555,893793570,880358776,895661297,897397782,0xb555350e,0xb57fb3c9,0xb448357b,0xb5463522,0xb495b575,886814055,0xb522b546,860468608,
13696,0xb5353535,13696,0xb5353535,0xb5800000,0xb52cb53e,0xb56db4c4,0xb4acb571,46464,893826338,892712245,897528777,897581056,890778958,885273965,0xb504355c,13696,0xb555350e,0xb5800000,0xb50eb555,0xb5800000,0xb504b55c,885306733,897430550,46464,895661297,892679477,0xb4953575,897581056,886781287,0xb56d34c4,0xb53eb52c,
13696,0xb56d34c4,46464,896382148,0xb5800000,0xb4acb571,896349380,0xb3493580,46464,896906389,0xb5353535,0xb562b4f1,897581056,880358776,0xb4c4b56d,894350616,13696,0xb57b3448,897581056,877147515,0xb5800000,0xb416b57d,0xb4c4356d,0xb578b479,46464,897561545,0xb535b535,891467078,897581056,860435840,896382148,886781287,
0xb5800000,0xb535b535,0xb5800000,0xb535b535,868857215,894350616,888255842,897102969,897266760,888223074,890123605,0xb4953575,0xb4953575,0xb57134ac,0xb57f33c9,0xb52cb53e,0xb56db4c4,0xb448b57b,885306733,897299528,888255842,897626953,891434310,0xb4ac3571,894776590,0xb3c9357f,0xb57b3448,0xb522b546,0xb5223546,0xb578b479,882226549,897430550,
0xb535b535,885306733,892679477,0xb4c4356d,893826338,895235332,0xb5753495,0xb518b54e,890123605,0xb5223546,877180283,897561545,0xb56234f1,0xb52cb53e,893793570,0xb4db3567,0xb4c4b56d,894809358,0xb56d34c4,0xb50eb555,896906389,886781287,868857215,897626953,877147515,0xb5753495,894776590,0xb4f13562,0xb57f33c9,0xb416b57d,0xb56234f1,0xb504b55c,
46464,892712245,46464,892712245,897266760,889468252,894776590,873870717,0xb4c4356d,0xb5753495,0xb56d34c4,0xb562b4f1,0xb555b50e,0xb349b580,877180283,895988955,892712245,897266760,892679477,0xb448357b,890123605,0xb4db3567,0xb57b3448,0xb52cb53e,0xb56d34c4,0xb546b522,885306733,897561545,0xb448b57b,894350616,890123605,0xb504355c,
897581056,885273965,0xb5800000,0xb4c4b56d,0xb448357b,0xb57d3416,890156373,897070201,0xb56db4c4,868857215,885273965,0xb5463522,890156373,896611500,0xb57bb448,0xb349b580,892679477,0xb50e3555,892712245,894776590,0xb555350e,0xb52cb53e,877147515,0xb57134ac,0xb4c4b56d,895661297,0xb56db4c4,882226549,897299528,880358776,894809358,890778958,
897581056,892679477,897581056,892679477,0xb4953575,0xb56734db,0xb5223546,0xb5803349,0xb555b50e,0xb3c9b57f,0xb448b57b,891467078,893826338,897070201,895628529,873870717,885273965,0xb50e3555,0xb56d34c4,0xb555b50e,0xb57f33c9,0xb518b54e,882226549,897102969,877180283,896906389,890123605,0xb4f13562,895628529,0xb3493580,0xb57fb3c9,0xb4acb571,
0xb5353535,0xb56db4c4,892712245,896349380,0xb4f1b562,892122430,868824447,0xb56734db,897299528,888223074,0xb555b50e,882226549,0xb3c9357f,0xb57d3416,896906389,889468252,0xb56db4c4,877180283,0xb4c4356d,0xb57bb448,891467078,895235332,0xb4f1b562,894350616,890123605,0xb5463522,897266760,0xb3c9357f,0xb5753495,0xb4acb571,0xb5463522,0xb52cb53e,
13696,0xb5800000,13696,0xb5800000,0xb5800000,860468608,0xb56db4c4,886814055,46464,897528777,892712245,891434310,897581056,0xb416357d,885273965,0xb5783479,13696,0xb57bb448,0xb5800000,877180283,0xb5800000,880391544,885306733,894317848,46464,896873621,892679477,0xb56234f1,897581056,0xb4ac3571,0xb56d34c4,0xb349b580,
13696,0xb56db4c4,46464,896349380,0xb5800000,886814055,896349380,0xb53e352c,46464,895628529,0xb5353535,0xb495b575,897581056,0xb504355c,0xb4c4b56d,897397782,13696,0xb555b50e,897581056,0xb50e3555,0xb5800000,890811726,0xb4c4356d,0xb504b55c,46464,893793570,0xb535b535,897561545,897581056,0xb52c353e,896382148,0xb4ac3571,
46464,897581056,46464,897581056,897528777,0xb416357d,895628529,0xb504355c,0xb448357b,0xb575b495,0xb555350e,0xb4f1b562,0xb575b495,886814055,0xb3c9b57f,897594185,885306733,894776590,896349380,0xb50e3555,895628529,0xb52c353e,0xb5463522,0xb4dbb567,0xb50e3555,0xb522b546,0xb448b57b,897528777,0xb546b522,895268100,896873621,0xb518354e,
892712245,885273965,0xb5353535,0xb4c4b56d,891434310,0xb5783479,0xb495b575,897397782,0xb555350e,0xb3c9b57f,897266760,0xb5223546,0xb4f1b562,897594185,0xb5223546,0xb4acb571,896382148,0xb448357b,0xb4c4b56d,897266760,882193781,0xb571b4ac,897528777,0xb52c353e,0xb57b3448,888255842,0xb50e3555,0xb495b575,0xb3c9b57f,894317848,0xb4f1b562,897070201,
13696,0xb5800000,13696,0xb5800000,0xb57bb448,880391544,0xb555b50e,890811726,885306733,895628529,896382148,882193781,894776590,0xb52c353e,0xb448357b,0xb571b4ac,0xb5353535,0xb50eb555,0xb535b535,894809358,0xb50eb555,896644268,897299528,0xb3493580,896382148,868824447,0xb4c4356d,0xb546b522,877147515,0xb57db416,0xb50eb555,897102969,
0xb5800000,885306733,897581056,0xb4c4356d,877180283,894317848,0xb50e3555,0xb504b55c,896349380,0xb5463522,0xb4c4b56d,897528777,0xb50e3555,0xb4dbb567,897266760,0xb52c353e,0xb535b535,897299528,0xb5353535,0xb448b57b,894809358,0xb3493580,0xb448b57b,895956187,885273965,0xb575b495,896349380,0xb56234f1,0xb57b3448,889501020,0xb555350e,873903485,
46464,897581056,46464,897581056,896873621,0xb4ac3571,893793570,0xb52c353e,0xb50e3555,0xb546b522,0xb57b3448,0xb3c9b57f,0xb522b546,895268100,888255842,894317848,896382148,877147515,885273965,0xb57b3448,868824447,0xb57db416,0xb575b495,889501020,0xb57bb448,888255842,894809358,882193781,888255842,892089662,0xb3c9357f,0xb567b4db,
892679477,0xb56d34c4,0xb535b535,896382148,0xb56234f1,0xb349b580,897561545,0xb4ac3571,0xb448b57b,896873621,0xb50e3555,0xb4f1b562,897528777,0xb518354e,0xb495b575,897070201,0xb4c4356d,0xb50eb555,896349380,0xb555350e,0xb546b522,897102969,0xb56234f1,873903485,894809358,0xb3c9357f,877180283,891434310,882193781,0xb567b4db,891434310,0xb580b349,
13696,0xb535b535,13696,0xb535b535,0xb5800000,893302060,0xb56db4c4,896644268,46464,891434310,892712245,0xb3c9357f,897581056,0xb54e3518,885273965,0xb55cb504,13696,0xb50eb555,0xb5800000,894809358,0xb5800000,895268100,885306733,873870717,46464,888223074,892679477,0xb575b495,897581056,0xb56734db,0xb56d34c4,892122430,
13696,0xb4c4b56d,46464,885273965,0xb5800000,896644268,896349380,0xb580b349,46464,882193781,0xb5353535,888255842,897581056,0xb5783479,0xb4c4b56d,890778958,13696,0xb448b57b,897581056,0xb57b3448,0xb5800000,897430550,0xb4c4356d,880391544,46464,868824447,0xb535b535,893793570,897581056,0xb5803349,896382148,0xb56734db,
897581056,0xb5353535,897581056,0xb5353535,0xb3c9357f,0xb518b54e,0xb4f13562,0xb479b578,0xb57bb448,895661297,0xb50eb555,896873621,882226549,883701105,897561545,0xb53e352c,896349380,0xb57b3448,0xb4c4356d,0xb448b57b,0xb4f13562,0xb349b580,0xb522b546,896611500,0xb555b50e,897528777,897299528,0xb5463522,891467078,0xb4793578,0xb4953575,0xb416b57d,
892679477,0xb56db4c4,0xb535b535,896349380,0xb5463522,889501020,896906389,0xb54e3518,0xb50eb555,893793570,0xb448357b,0xb3c9b57f,895661297,0xb53e352c,0xb546b522,895956187,885273965,0xb50eb555,896382148,0xb555350e,0xb5753495,895988955,0xb3c9357f,0xb349b580,0xb448b57b,882193781,0xb555b50e,895628529,897561545,0xb57d3416,895661297,0xb55c3504,
46464,892679477,46464,892679477,897266760,0xb55c3504,894776590,0xb57d3416,0xb4c4356d,0xb495b575,0xb56d34c4,888255842,0xb555b50e,897626953,877180283,886781287,892712245,0xb448357b,892679477,0xb57bb448,890123605,0xb567b4db,0xb57b3448,893302060,0xb56d34c4,891467078,885306733,868824447,0xb448b57b,890778958,890123605,0xb55cb504,
897581056,0xb56d34c4,0xb5800000,896382148,0xb448357b,0xb416b57d,890156373,0xb4793578,0xb56db4c4,897528777,885273965,0xb522b546,890156373,0xb4ac3571,0xb57bb448,897626953,892679477,0xb555b50e,892712245,0xb50e3555,0xb555350e,893302060,877147515,0xb4acb571,0xb4c4b56d,888223074,0xb56db4c4,896873621,897299528,0xb5783479,894809358,0xb54e3518,
0xb5800000,892712245,0xb5800000,892712245,882226549,886781287,891467078,860435840,894776590,0xb57f33c9,877147515,0xb546b522,0xb5463522,880391544,0xb562b4f1,897430550,0xb4c4b56d,894776590,896382148,0xb50e3555,897561545,0xb54e3518,0xb4953575,0xb479b578,0xb448357b,0xb495b575,0xb50eb555,895628529,0xb562b4f1,897594185,897528777,0xb57134ac,
892712245,0xb4c4356d,0xb5353535,885306733,888223074,0xb53eb52c,0xb3c9b57f,886781287,0xb57b3448,895661297,894776590,0xb575b495,868857215,873870717,0xb5753495,895268100,896349380,0xb57bb448,885306733,0xb448357b,0xb5223546,889501020,888223074,0xb518b54e,0xb50eb555,891434310,0xb57bb448,897528777,896906389,0xb57134ac,893826338,0xb53e352c,
13696,46464,13696,46464,0xb5800000,897594185,0xb56db4c4,895956187,46464,0xb3c9357f,892712245,0xb5463522,897581056,0xb57db416,885273965,0xb479b578,13696,877180283,0xb5800000,897266760,0xb5800000,897070201,885306733,0xb518354e,46464,0xb4953575,892679477,0xb4f1b562,897581056,0xb571b4ac,0xb56d34c4,897626953,
13696,885306733,46464,0xb4c4356d,0xb5800000,895956187,896349380,0xb52cb53e,46464,0xb4f13562,0xb5353535,896906389,897581056,0xb55cb504,0xb4c4b56d,0xb416357d,13696,890156373,897581056,0xb555b50e,0xb5800000,894317848,0xb4c4356d,895268100,46464,0xb5223546,0xb535b535,868824447,897581056,0xb53eb52c,896382148,0xb571b4ac,
13696,46464,13696,46464,0xb57fb3c9,897397782,0xb562b4f1,895235332,877180283,0xb4953575,894809358,0xb56234f1,896873621,0xb567b4db,868824447,860468608,0xb4c4356d,890156373,0xb56db4c4,894776590,0xb562b4f1,893269292,893826338,0xb56734db,890156373,0xb5463522,877147515,868857215,893793570,0xb504b55c,0xb575b495,894317848,
0xb5353535,896382148,892712245,0xb56d34c4,0xb522b546,880358776,882193781,873903485,894809358,0xb57f33c9,0xb57bb448,893793570,888223074,860468608,891467078,0xb57134ac,0xb56d34c4,897266760,885273965,877180283,0xb495b575,0xb4ac3571,0xb57fb3c9,893269292,897299528,0xb562b4f1,890156373,0xb5753495,868824447,890811726,888223074,880391544,
13696,46464,13696,46464,0xb57bb448,897070201,0xb555b50e,894317848,885306733,0xb4f13562,896382148,0xb5753495,894776590,0xb53eb52c,0xb448357b,883733873,0xb5353535,894809358,0xb535b535,890123605,0xb50eb555,883701105,897299528,0xb580b349,896382148,0xb57f33c9,0xb4c4356d,891467078,877147515,873903485,0xb50eb555,880358776,
0xb5800000,896349380,897581056,0xb56db4c4,877180283,0xb518354e,0xb50e3555,895268100,896349380,0xb522b546,0xb4c4b56d,0xb3c9357f,0xb50e3555,895988955,897266760,0xb53eb52c,0xb535b535,877147515,0xb5353535,897299528,894809358,0xb580b349,0xb448b57b,0xb4db3567,885273965,882226549,896349380,0xb4f1b562,0xb57b3448,895235332,0xb555350e,897397782,
13696,46464,13696,46464,0xb575b495,896611500,0xb546b522,893269292,890156373,0xb5223546,897299528,0xb57f33c9,891434310,0xb504b55c,0xb4f13562,890811726,0xb56d34c4,897299528,0xb4c4b56d,877147515,0xb3c9b57f,0xb416357d,896873621,0xb55cb504,897266760,0xb562b4f1,0xb555350e,896906389,0xb4f13562,893302060,868857215,0xb4db3567,
0xb535b535,885273965,892679477,0xb4c4b56d,895661297,0xb5803349,0xb57f33c9,896611500,877147515,882226549,890156373,0xb56234f1,0xb57fb3c9,894317848,882193781,880391544,885306733,0xb555350e,0xb56db4c4,890123605,893793570,0xb479b578,895661297,0xb57db416,0xb555350e,897528777,0xb448357b,893826338,0xb495b575,0xb4db3567,0xb522b546,0xb3493580,
13696,892712245,13696,892712245,0xb5800000,892089662,0xb56db4c4,883701105,46464,0xb5463522,892712245,0xb57fb3c9,897581056,0xb518b54e,885273965,889501020,13696,894809358,0xb5800000,890123605,0xb5800000,889468252,885306733,0xb57d3416,46464,0xb56234f1,892679477,882226549,897581056,0xb4dbb567,0xb56d34c4,893269292,
13696,896382148,46464,0xb56d34c4,0xb5800000,883701105,896349380,860468608,46464,0xb5753495,0xb5353535,895628529,897581056,0xb479b578,0xb4c4b56d,0xb54e3518,13696,897299528,897581056,0xb448b57b,0xb5800000,873870717,0xb4c4356d,897070201,46464,0xb57f33c9,0xb535b535,0xb5223546,897581056,0xb349b580,896382148,0xb4dbb567,
0xb5800000,892679477,0xb5800000,892679477,868857215,0xb54e3518,888255842,0xb5783479,897266760,0xb4f1b562,890123605,882226549,0xb4953575,896644268,0xb57f33c9,892089662,0xb56db4c4,877147515,885306733,0xb57b3448,888255842,0xb5803349,891434310,883733873,894776590,868857215,0xb57b3448,891434310,0xb5223546,897070201,882226549,0xb57d3416,
0xb535b535,0xb4c4356d,892679477,885306733,893826338,0xb55cb504,0xb5753495,890778958,890123605,891467078,877180283,0xb57f33c9,0xb56234f1,892089662,893793570,886814055,0xb4c4b56d,0xb555350e,0xb56d34c4,890123605,896906389,0xb4dbb567,868857215,0xb5803349,877147515,896906389,894776590,888255842,0xb57f33c9,873870717,0xb56234f1,889468252,
46464,0xb5353535,46464,0xb5353535,897266760,0xb504b55c,894776590,0xb416b57d,0xb4c4356d,896906389,0xb56d34c4,895628529,0xb555b50e,860435840,877180283,0xb56734db,892712245,0xb57bb448,892679477,877180283,890123605,886814055,0xb57b3448,892089662,0xb56d34c4,893793570,885306733,0xb57f33c9,0xb448b57b,0xb54e3518,890123605,889501020,
897581056,0xb4c4b56d,0xb5800000,885273965,0xb448357b,897430550,890156373,0xb578b479,0xb56db4c4,0xb3c9357f,885273965,893826338,890156373,0xb571b4ac,0xb57bb448,860435840,892679477,890156373,892712245,0xb555b50e,0xb555350e,892089662,877147515,896644268,0xb4c4b56d,0xb56234f1,0xb56db4c4,0xb4953575,897299528,0xb479b578,894809358,0xb518b54e,
897581056,0xb535b535,897581056,0xb535b535,0xb4953575,895988955,0xb5223546,897626953,0xb555b50e,868824447,0xb448b57b,0xb5223546,893826338,0xb578b479,895628529,0xb416b57d,885273965,890156373,0xb56d34c4,894776590,0xb57f33c9,890778958,882226549,0xb5783479,877180283,0xb5753495,890123605,888255842,895628529,860468608,0xb57fb3c9,883701105,
0xb5353535,896349380,892712245,0xb56db4c4,0xb4f1b562,0xb52c353e,868824447,895988955,897299528,0xb4f1b562,0xb555b50e,0xb4953575,0xb3c9357f,897430550,896906389,0xb504b55c,0xb56db4c4,0xb448357b,0xb4c4356d,897266760,891467078,0xb55cb504,0xb4f1b562,0xb54e3518,890123605,893826338,897266760,868857215,0xb5753495,883701105,0xb5463522,892089662,
13696,897581056,13696,897581056,0xb5800000,0xb3493580,0xb56db4c4,0xb4db3567,46464,0xb57fb3c9,892712245,0xb522b546,897581056,873903485,885273965,897102969,13696,897266760,0xb5800000,0xb448357b,0xb5800000,0xb4793578,885306733,0xb54eb518,46464,0xb575b495,892679477,895661297,897581056,883733873,0xb56d34c4,860435840,
13696,896349380,46464,0xb56db4c4,0xb5800000,0xb4db3567,896349380,893302060,46464,0xb562b4f1,0xb5353535,882193781,897581056,889501020,0xb4c4b56d,0xb57db416,13696,894776590,897581056,890156373,0xb5800000,0xb518354e,0xb4c4356d,889468252,46464,0xb546b522,0xb535b535,0xb57f33c9,897581056,892122430,896382148,883733873,
46464,0xb5800000,46464,0xb5800000,897528777,873903485,895628529,889501020,0xb448357b,896873621,0xb555350e,888223074,0xb575b495,0xb4db3567,0xb3c9b57f,0xb580b349,885306733,0xb555b50e,896349380,890156373,895628529,892122430,0xb5463522,886781287,0xb50e3555,891434310,0xb448b57b,0xb57fb3c9,0xb546b522,0xb55c3504,896873621,890811726,
892712245,0xb4c4b56d,0xb5353535,885273965,891434310,897102969,0xb495b575,0xb57db416,0xb555350e,868824447,897266760,891467078,0xb4f1b562,0xb580b349,0xb5223546,883701105,896382148,877180283,0xb4c4b56d,0xb57bb448,882193781,896611500,897528777,892122430,0xb57b3448,0xb4f13562,0xb50e3555,882193781,0xb3c9b57f,0xb54eb518,0xb4f1b562,0xb578b479,
13696,897581056,13696,897581056,0xb57bb448,0xb4793578,0xb555b50e,0xb518354e,885306733,0xb562b4f1,896382148,0xb495b575,894776590,892122430,0xb448357b,896611500,0xb5353535,890123605,0xb535b535,0xb555350e,0xb50eb555,0xb57134ac,897299528,860468608,896382148,0xb3c9b57f,0xb4c4356d,893793570,877147515,897397782,0xb50eb555,0xb5783479,
0xb5800000,0xb4c4356d,897581056,885306733,877180283,0xb54eb518,0xb50e3555,889468252,896349380,893826338,0xb4c4b56d,0xb57fb3c9,0xb50e3555,886781287,897266760,892122430,0xb535b535,0xb57b3448,0xb5353535,877147515,894809358,860468608,0xb448b57b,0xb567b4db,885273965,896873621,896349380,895661297,0xb57b3448,0xb504355c,0xb555350e,0xb416357d,
46464,0xb5800000,46464,0xb5800000,896873621,883733873,893793570,892122430,0xb50e3555,893793570,0xb57b3448,868824447,0xb522b546,0xb55c3504,888255842,0xb54eb518,896382148,0xb448b57b,885273965,897299528,868824447,897397782,0xb575b495,0xb504355c,0xb57bb448,0xb4f13562,894809358,0xb495b575,888255842,0xb52cb53e,0xb3c9357f,895956187,
892679477,896382148,0xb535b535,0xb56d34c4,0xb56234f1,860435840,897561545,883733873,0xb448b57b,0xb575b495,0xb50e3555,888223074,897528777,890811726,0xb495b575,0xb578b479,0xb4c4356d,890123605,896349380,894809358,0xb546b522,0xb5783479,0xb56234f1,0xb416357d,894809358,868857215,877180283,0xb522b546,882193781,895956187,891434310,897594185,
13696,892679477,13696,892679477,0xb5800000,0xb53e352c,0xb56db4c4,0xb57134ac,46464,0xb522b546,892712245,868857215,897581056,894350616,885273965,895235332,13696,890123605,0xb5800000,0xb555350e,0xb5800000,0xb55c3504,885306733,0xb416b57d,46464,0xb4f1b562,892679477,896873621,897581056,895988955,0xb56d34c4,0xb52c353e,
13696,885273965,46464,0xb4c4b56d,0xb5800000,0xb57134ac,896349380,897594185,46464,0xb495b575,0xb5353535,0xb4f13562,897581056,897102969,0xb4c4b56d,0xb518b54e,13696,877147515,897581056,897299528,0xb5800000,0xb57d3416,0xb4c4356d,0xb4793578,46464,0xb3c9b57f,0xb535b535,0xb546b522,897581056,897626953,896382148,895988955,
897581056,892712245,897581056,892712245,0xb3c9357f,890778958,0xb4f13562,880358776,0xb57bb448,0xb56234f1,0xb50eb555,0xb575b495,882226549,0xb4acb571,897561545,893302060,896349380,897299528,0xb4c4356d,877147515,0xb4f13562,860435840,0xb522b546,0xb571b4ac,0xb555b50e,0xb57fb3c9,897299528,893826338,891467078,880391544,0xb4953575,873870717,
892679477,896349380,0xb535b535,0xb56db4c4,0xb5463522,0xb504355c,896906389,894350616,0xb50eb555,0xb546b522,0xb448357b,868824447,895661297,893302060,0xb546b522,0xb567b4db,885273965,890123605,896382148,894809358,0xb5753495,0xb56734db,0xb3c9357f,860435840,0xb448b57b,0xb495b575,0xb555b50e,0xb562b4f1,897561545,897430550,895661297,895268100,
46464,0xb535b535,46464,0xb535b535,897266760,895268100,894776590,897430550,0xb4c4356d,882193781,0xb56d34c4,0xb4f13562,0xb555b50e,0xb5803349,877180283,0xb4dbb567,892712245,877180283,892679477,897266760,890123605,895956187,0xb57b3448,0xb53e352c,0xb56d34c4,0xb5223546,885306733,0xb3c9b57f,0xb448b57b,0xb518b54e,890123605,895235332,
897581056,896382148,0xb5800000,0xb56d34c4,0xb448357b,873870717,890156373,880391544,0xb56db4c4,0xb57fb3c9,885273965,891434310,890156373,883733873,0xb57bb448,0xb5803349,892679477,894776590,892712245,890156373,0xb555350e,0xb53e352c,877147515,883701105,0xb4c4b56d,0xb4f1b562,0xb56db4c4,0xb575b495,897299528,897102969,894809358,894350616,
0xb5800000,0xb5353535,0xb5800000,0xb5353535,882226549,0xb4dbb567,891467078,0xb349b580,894776590,897561545,877147515,893793570,0xb5463522,0xb4793578,0xb562b4f1,0xb57d3416,0xb4c4b56d,0xb555b50e,896382148,890156373,897561545,894350616,0xb4953575,880358776,0xb448357b,882193781,0xb50eb555,0xb562b4f1,0xb562b4f1,0xb580b349,897528777,896644268,
892712245,885306733,0xb5353535,0xb4c4356d,888223074,893269292,0xb3c9b57f,0xb4dbb567,0xb57b3448,0xb56234f1,894776590,896873621,868857215,0xb416b57d,0xb5753495,0xb55c3504,896349380,897266760,885306733,877180283,0xb5223546,0xb504355c,888223074,890778958,0xb50eb555,0xb522b546,0xb57bb448,0xb57fb3c9,896906389,896644268,893826338,893302060,
13696,13696,13696,13696,0xb535b535,0xb52cb53e,0xb4c4b56d,0xb4acb571,897581056,897528777,892679477,891434310,0xb5353535,0xb54e3518,0xb56db4c4,0xb55cb504,46464,877180283,897581056,897266760,892679477,889468252,0xb56d34c4,0xb57d3416,0xb5800000,0xb575b495,892712245,895661297,892712245,895988955,0xb4c4356d,0xb52c353e,
13696,0xb4c4356d,46464,885306733,0xb535b535,0xb4acb571,885273965,0xb3493580,897581056,895628529,0xb535b535,0xb495b575,0xb5353535,0xb5783479,896349380,890778958,46464,890156373,0xb5800000,0xb555b50e,892679477,873870717,896382148,897070201,0xb5800000,0xb546b522,0xb5353535,0xb57f33c9,892712245,897626953,885306733,895988955,
13696,13696,13696,13696,0xb522b546,0xb518b54e,0xb495b575,0xb479b578,897266760,896873621,890123605,888223074,0xb56234f1,0xb57134ac,0xb546b522,0xb52cb53e,885306733,890156373,896349380,894776590,882193781,860435840,0xb57fb3c9,0xb571b4ac,0xb555b50e,0xb522b546,897299528,897528777,897561545,897070201,0xb56234f1,0xb57d3416,
0xb5353535,0xb56d34c4,892712245,896382148,868857215,889501020,0xb4f13562,0xb54e3518,890123605,868824447,877180283,891467078,0xb575b495,0xb52cb53e,868824447,0xb4db3567,896382148,897266760,0xb4c4b56d,877180283,0xb4f13562,0xb56734db,891434310,860435840,0xb448b57b,888255842,0xb555b50e,0xb495b575,893793570,873870717,896873621,889468252,
13696,13696,13696,13696,0xb50eb555,0xb504b55c,0xb448b57b,0xb416b57d,896349380,895628529,885273965,882193781,0xb57b3448,0xb5803349,0xb50eb555,0xb4dbb567,892712245,894809358,892679477,890123605,0xb448357b,0xb4db3567,0xb555b50e,0xb52cb53e,0xb4c4b56d,0xb3c9b57f,896349380,893793570,894776590,890778958,0xb57bb448,0xb55cb504,
0xb5800000,0xb56db4c4,897581056,896349380,894809358,897430550,0xb57b3448,0xb578b479,0xb4c4356d,0xb5463522,896382148,897528777,0xb448b57b,883733873,0xb555350e,0xb5803349,892679477,877147515,892712245,897299528,0xb57bb448,0xb52cb53e,0xb50e3555,0xb57134ac,896382148,896873621,885306733,895661297,0xb50e3555,0xb5783479,0xb448357b,0xb54e3518,
13696,13696,13696,13696,0xb4f1b562,0xb4dbb567,0xb3c9b57f,0xb349b580,894776590,893793570,877147515,868824447,0xb57fb3c9,0xb578b479,0xb495b575,0xb416b57d,896382148,897299528,885273965,877147515,0xb5223546,0xb54e3518,0xb4f1b562,0xb479b578,877180283,888255842,890123605,882193781,882193781,0xb3493580,0xb522b546,0xb4acb571,
0xb535b535,0xb4c4b56d,892679477,885273965,896873621,893269292,0xb546b522,0xb4dbb567,0xb57b3448,0xb575b495,894776590,888223074,893826338,897430550,0xb562b4f1,0xb504b55c,0xb4c4356d,0xb555350e,896349380,890123605,0xb3c9b57f,889501020,0xb575b495,0xb518b54e,890123605,0xb3c9357f,897266760,891434310,0xb562b4f1,0xb4acb571,0xb57fb3c9,0xb52cb53e,
13696,0xb5353535,13696,0xb5353535,0xb535b535,860468608,0xb4c4b56d,886814055,897581056,891434310,892679477,0xb3c9357f,0xb5353535,0xb57db416,0xb56db4c4,0xb479b578,46464,894809358,897581056,890123605,892679477,0xb4793578,0xb56d34c4,0xb54eb518,0xb5800000,0xb4f1b562,892712245,896873621,892712245,896611500,0xb4c4356d,0xb5803349,
13696,0xb56d34c4,46464,896382148,0xb535b535,886814055,885273965,0xb53e352c,897581056,882193781,0xb535b535,888255842,0xb5353535,0xb55cb504,896349380,0xb416357d,46464,897299528,0xb5800000,0xb448b57b,892679477,0xb518354e,896382148,889468252,0xb5800000,0xb3c9b57f,0xb5353535,0xb546b522,892712245,893269292,885306733,896611500,
0xb5800000,0xb535b535,0xb5800000,0xb535b535,893826338,897397782,896906389,895235332,0xb448357b,0xb56234f1,0xb555350e,0xb575b495,0xb4f1b562,886814055,891467078,897594185,896349380,877147515,0xb4c4356d,0xb57b3448,0xb5753495,0xb53eb52c,868857215,895988955,890156373,897528777,877147515,0xb5463522,868824447,0xb55c3504,0xb4f1b562,890811726,
0xb535b535,885306733,892679477,0xb4c4356d,897528777,880358776,0xb562b4f1,873903485,0xb555350e,0xb546b522,897266760,868824447,882226549,897594185,0xb57f33c9,0xb4acb571,885273965,0xb555350e,896382148,890123605,0xb562b4f1,883733873,0xb5463522,0xb53eb52c,897299528,882193781,890156373,895628529,0xb5223546,0xb54eb518,0xb4953575,0xb578b479,
46464,892712245,46464,892712245,890123605,0xb4793578,877147515,0xb518354e,0xb56db4c4,0xb495b575,0xb4c4b56d,888255842,897299528,893269292,890123605,0xb4ac3571,0xb5353535,0xb57bb448,0xb535b535,877180283,877180283,896644268,894776590,0xb3493580,885273965,0xb5223546,0xb56db4c4,0xb3c9b57f,0xb555b50e,873903485,897266760,880358776,
897581056,885273965,0xb5800000,0xb4c4b56d,0xb555350e,0xb54eb518,897299528,889468252,885306733,897528777,0xb56d34c4,0xb522b546,877147515,0xb56734db,894809358,893269292,0xb535b535,890156373,0xb5353535,0xb555b50e,897266760,0xb3493580,890156373,895956187,0xb56d34c4,0xb4f1b562,0xb4c4356d,0xb575b495,890156373,895235332,877180283,897397782,
897581056,892679477,897581056,892679477,0xb56234f1,0xb571b4ac,0xb57f33c9,0xb53eb52c,890156373,897561545,897299528,893793570,0xb3c9357f,0xb55c3504,0xb5753495,0xb54eb518,0xb4c4b56d,890156373,896382148,894776590,893793570,0xb416357d,0xb56234f1,0xb55cb504,0xb57bb448,0xb495b575,894809358,895628529,896906389,892089662,0xb5463522,0xb567b4db,
0xb5353535,0xb56db4c4,892712245,896349380,882226549,897626953,0xb5223546,0xb571b4ac,877147515,0xb56234f1,890156373,896873621,0xb522b546,890811726,0xb4f13562,0xb578b479,896349380,0xb448357b,885306733,897266760,0xb57f33c9,0xb479b578,0xb4953575,0xb57db416,894809358,891434310,877180283,897528777,0xb4f13562,0xb567b4db,0xb3c9357f,0xb580b349,
13696,0xb5800000,13696,0xb5800000,0xb535b535,893302060,0xb4c4b56d,896644268,897581056,0xb3c9357f,892679477,0xb5463522,0xb5353535,0xb518b54e,0xb56db4c4,889501020,46464,897266760,897581056,0xb448357b,892679477,0xb55c3504,0xb56d34c4,0xb416b57d,0xb5800000,882226549,892712245,888223074,892712245,886781287,0xb4c4356d,0xb53eb52c,
13696,0xb56db4c4,46464,896349380,0xb535b535,896644268,885273965,0xb580b349,897581056,0xb4f13562,0xb535b535,896906389,0xb5353535,0xb479b578,896349380,0xb54e3518,46464,894776590,0xb5800000,890156373,892679477,0xb57d3416,896382148,0xb4793578,0xb5800000,891467078,0xb5353535,0xb3c9b57f,892712245,860435840,885306733,886781287,
46464,897581056,46464,897581056,891434310,0xb54e3518,882193781,0xb5783479,0xb57bb448,882226549,0xb50eb555,895661297,895661297,883701105,893793570,0xb53e352c,0xb4c4356d,0xb555b50e,0xb56db4c4,890156373,0xb495b575,897626953,897528777,0xb4ac3571,894776590,0xb5463522,0xb57b3448,868857215,0xb57f33c9,880391544,895661297,873870717,
892712245,885273965,0xb5353535,0xb4c4b56d,0xb3c9357f,0xb55cb504,888255842,890778958,0xb50eb555,897561545,0xb448357b,0xb546b522,896873621,0xb53e352c,0xb3c9b57f,895956187,0xb56d34c4,877180283,885273965,0xb57bb448,888255842,886781287,0xb522b546,897626953,877147515,0xb562b4f1,894776590,0xb5753495,0xb546b522,897430550,0xb575b495,895268100,
13696,0xb5800000,13696,0xb5800000,0xb50eb555,895268100,0xb448b57b,897430550,896349380,0xb4f13562,885273965,0xb5753495,0xb57b3448,0xb349b580,0xb50eb555,895988955,892712245,890123605,892679477,0xb555350e,0xb448357b,0xb567b4db,0xb555b50e,893302060,0xb4c4b56d,897561545,896349380,0xb5223546,894776590,0xb54e3518,0xb57bb448,889501020,
0xb5800000,885306733,897581056,0xb4c4356d,894809358,873870717,0xb57b3448,880391544,0xb4c4356d,0xb522b546,896382148,0xb3c9357f,0xb448b57b,896611500,0xb555350e,0xb349b580,892679477,0xb57b3448,892712245,877147515,0xb57bb448,893302060,0xb50e3555,0xb4acb571,896382148,0xb4953575,885306733,888223074,0xb50e3555,0xb479b578,0xb448357b,0xb518b54e,
46464,897581056,46464,897581056,888223074,0xb56734db,868824447,0xb5803349,0xb555b50e,891467078,0xb448b57b,897561545,897528777,0xb4793578,882193781,0xb57d3416,0xb56d34c4,0xb448b57b,0xb4c4b56d,897299528,891467078,890778958,888223074,0xb5783479,0xb448357b,0xb562b4f1,0xb50eb555,896906389,0xb495b575,897594185,891434310,0xb57134ac,
892679477,0xb56d34c4,0xb535b535,896382148,0xb575b495,892122430,893793570,0xb56734db,897299528,0xb4953575,0xb555b50e,895661297,0xb5463522,0xb416b57d,895628529,0xb55c3504,885306733,890123605,0xb56db4c4,894809358,868824447,0xb55cb504,896873621,0xb54e3518,0xb50eb555,897528777,0xb57bb448,893826338,895628529,0xb57134ac,897528777,0xb53e352c,
13696,0xb535b535,13696,0xb535b535,0xb535b535,897594185,0xb4c4b56d,895956187,897581056,0xb5463522,892679477,0xb57fb3c9,0xb5353535,873903485,0xb56db4c4,897102969,46464,890123605,897581056,0xb555350e,892679477,0xb578b479,0xb56d34c4,890811726,0xb5800000,895661297,892712245,0xb4953575,892712245,0xb4ac3571,0xb4c4356d,0xb349b580,
13696,0xb4c4b56d,46464,885273965,0xb535b535,895956187,885273965,0xb52cb53e,897581056,0xb5753495,0xb535b535,895628529,0xb5353535,889501020,896349380,0xb57db416,46464,877147515,0xb5800000,897299528,892679477,0xb54eb518,896382148,0xb55c3504,0xb5800000,897561545,0xb5353535,891467078,892712245,0xb52c353e,885306733,0xb4ac3571,
897581056,0xb5353535,897581056,0xb5353535,0xb5463522,873903485,0xb5753495,889501020,877180283,888223074,894809358,0xb4953575,888223074,0xb567b4db,0xb5223546,860468608,0xb56db4c4,897299528,885306733,877147515,896906389,0xb52c353e,0xb3c9357f,0xb4dbb567,0xb50e3555,868857215,0xb448b57b,891434310,0xb3c9b57f,889468252,888223074,0xb54eb518,
892679477,0xb56db4c4,0xb535b535,896349380,0xb57fb3c9,897102969,895628529,0xb57db416,894809358,0xb5223546,0xb57bb448,897561545,0xb4953575,860468608,897561545,0xb57134ac,0xb4c4b56d,890123605,0xb56d34c4,894809358,895628529,0xb571b4ac,893826338,0xb52c353e,0xb57b3448,896906389,0xb50e3555,888255842,891467078,0xb518354e,882226549,0xb4793578,
46464,892679477,46464,892679477,890123605,0xb578b479,877147515,0xb54eb518,0xb56db4c4,896906389,0xb4c4b56d,895628529,897299528,0xb52c353e,890123605,0xb571b4ac,0xb5353535,877180283,0xb535b535,897266760,877180283,883701105,894776590,0xb580b349,885273965,0xb546b522,0xb56db4c4,897561545,0xb555b50e,897397782,897266760,0xb5783479,
897581056,0xb56d34c4,0xb5800000,896382148,0xb555350e,890811726,897299528,0xb55c3504,885306733,0xb3c9357f,0xb56d34c4,893826338,877147515,0xb4dbb567,894809358,0xb52c353e,0xb535b535,894776590,0xb5353535,890156373,897266760,0xb580b349,890156373,0xb4db3567,0xb56d34c4,895661297,0xb4c4356d,882226549,890156373,0xb504355c,877180283,0xb416357d,
0xb5800000,892712245,0xb5800000,892712245,895661297,0xb4ac3571,897561545,0xb52c353e,0xb50e3555,0xb3c9b57f,0xb57b3448,891467078,868857215,889468252,896906389,0xb518354e,885273965,0xb555b50e,0xb56d34c4,890156373,0xb546b522,897397782,895661297,0xb504355c,897266760,0xb5753495,0xb555350e,888255842,0xb5753495,893302060,893826338,0xb4db3567,
892712245,0xb4c4356d,0xb5353535,885306733,0xb4953575,0xb349b580,891467078,0xb4ac3571,0xb448b57b,888223074,0xb50e3555,882226549,891434310,0xb54eb518,888255842,0xb4793578,0xb56db4c4,897266760,0xb4c4356d,877180283,897561545,0xb5783479,882226549,0xb416357d,0xb555350e,893826338,0xb448357b,868857215,888255842,0xb4db3567,868857215,0xb3493580,
13696,46464,13696,46464,0xb535b535,892089662,0xb4c4b56d,883701105,897581056,0xb57fb3c9,892679477,0xb522b546,0xb5353535,894350616,0xb56db4c4,895235332,46464,0xb448357b,897581056,0xb57bb448,892679477,0xb504b55c,0xb56d34c4,897430550,0xb5800000,896873621,892712245,0xb56234f1,892712245,0xb56734db,0xb4c4356d,892122430,
13696,885306733,46464,0xb4c4356d,0xb535b535,883701105,885273965,860468608,897581056,0xb562b4f1,0xb535b535,882193781,0xb5353535,897102969,896349380,0xb518b54e,46464,0xb50e3555,0xb5800000,894776590,892679477,0xb416b57d,896382148,0xb578b479,0xb5800000,893793570,0xb5353535,897561545,892712245,0xb5803349,885306733,0xb56734db,
13696,46464,13696,46464,0xb522b546,890778958,0xb495b575,880358776,897266760,0xb575b495,890123605,0xb4f1b562,0xb56234f1,896644268,0xb546b522,892089662,885306733,0xb50e3555,896349380,0xb555b50e,882193781,0xb349b580,0xb57fb3c9,896611500,0xb555b50e,891434310,897299528,0xb57fb3c9,897561545,0xb578b479,0xb56234f1,897430550,
0xb5353535,896382148,892712245,0xb56d34c4,868857215,0xb504355c,0xb4f13562,894350616,890123605,0xb3c9b57f,877180283,0xb5223546,0xb575b495,892089662,868824447,886814055,896382148,0xb57bb448,0xb4c4b56d,0xb448357b,0xb4f13562,895988955,891434310,0xb349b580,0xb448b57b,0xb4f13562,0xb555b50e,882193781,893793570,0xb416b57d,896873621,0xb504b55c,
13696,46464,13696,46464,0xb50eb555,889468252,0xb448b57b,873870717,896349380,0xb562b4f1,885273965,0xb495b575,0xb57b3448,897626953,0xb50eb555,886781287,892712245,0xb555350e,892679477,0xb50eb555,0xb448357b,886814055,0xb555b50e,892089662,0xb4c4b56d,868824447,896349380,0xb546b522,894776590,0xb518b54e,0xb57bb448,895235332,
0xb5800000,896349380,897581056,0xb56db4c4,894809358,0xb57d3416,0xb57b3448,897070201,0xb4c4356d,893826338,896382148,0xb57fb3c9,0xb448b57b,0xb4ac3571,0xb555350e,897626953,892679477,0xb448b57b,892712245,0xb57b3448,0xb57bb448,892089662,0xb50e3555,896644268,896382148,0xb575b495,885306733,0xb56234f1,0xb50e3555,897102969,0xb448357b,894350616,
13696,46464,13696,46464,0xb4f1b562,886781287,0xb3c9b57f,860435840,894776590,0xb546b522,877147515,0xb3c9b57f,0xb57fb3c9,897070201,0xb495b575,873870717,896382148,0xb57b3448,885273965,0xb448b57b,0xb5223546,894350616,0xb4f1b562,880358776,877180283,0xb4f13562,890123605,0xb495b575,882193781,860468608,0xb522b546,883701105,
0xb535b535,885273965,892679477,0xb4c4b56d,896873621,0xb53eb52c,0xb546b522,886781287,0xb57b3448,896873621,894776590,0xb4f1b562,893826338,0xb57d3416,0xb562b4f1,889468252,0xb4c4356d,894809358,896349380,0xb50eb555,0xb3c9b57f,0xb504355c,0xb575b495,890778958,890123605,868857215,897266760,0xb522b546,0xb562b4f1,883701105,0xb57fb3c9,892089662,
13696,892712245,13696,892712245,0xb535b535,0xb3493580,0xb4c4b56d,0xb4db3567,897581056,0xb522b546,892679477,868857215,0xb5353535,897397782,0xb56db4c4,880358776,46464,0xb555350e,897581056,0xb50eb555,892679477,880391544,0xb56d34c4,894317848,0xb5800000,888223074,892712245,0xb575b495,892712245,0xb571b4ac,0xb4c4356d,897626953,
13696,896382148,46464,0xb56d34c4,0xb535b535,0xb4db3567,885273965,893302060,897581056,0xb495b575,0xb535b535,0xb4f13562,0xb5353535,895235332,896349380,873903485,46464,0xb57b3448,0xb5800000,877147515,892679477,890811726,896382148,0xb504b55c,0xb5800000,868824447,0xb5353535,893793570,892712245,0xb53eb52c,885306733,0xb571b4ac,
0xb5800000,892679477,0xb5800000,892679477,893826338,0xb57db416,896906389,0xb55cb504,0xb448357b,895661297,0xb555350e,896873621,0xb4f1b562,0xb4db3567,891467078,0xb580b349,896349380,0xb448b57b,0xb4c4356d,897299528,0xb5753495,893269292,868857215,0xb56734db,890156373,0xb57fb3c9,877147515,893826338,868824447,895268100,0xb4f1b562,0xb518354e,
0xb535b535,0xb4c4356d,892679477,885306733,897528777,0xb479b578,0xb562b4f1,0xb416357d,0xb555350e,893793570,897266760,0xb3c9b57f,882226549,0xb580b349,0xb57f33c9,883701105,885273965,894809358,896382148,0xb50eb555,0xb562b4f1,0xb4ac3571,0xb5463522,893269292,897299528,0xb495b575,890156373,0xb562b4f1,0xb5223546,894317848,0xb4953575,897070201,
46464,0xb5353535,46464,0xb5353535,890123605,880391544,877147515,890811726,0xb56db4c4,882193781,0xb4c4b56d,0xb4f13562,897299528,0xb53eb52c,890123605,883733873,0xb5353535,897266760,0xb535b535,0xb448357b,877180283,0xb57134ac,894776590,860468608,885273965,891467078,0xb56db4c4,868824447,0xb555b50e,0xb416357d,897266760,0xb479b578,
897581056,0xb4c4b56d,0xb5800000,885273965,0xb555350e,894317848,897299528,0xb504b55c,885306733,0xb57fb3c9,0xb56d34c4,891434310,877147515,895988955,894809358,0xb53eb52c,0xb535b535,0xb50e3555,0xb5353535,894776590,897266760,860468608,890156373,0xb567b4db,0xb56d34c4,888223074,0xb4c4356d,896873621,890156373,0xb55cb504,877180283,0xb57db416,
897581056,0xb535b535,897581056,0xb535b535,0xb56234f1,896611500,0xb57f33c9,893269292,890156373,0xb57f33c9,897299528,0xb546b522,0xb3c9357f,895268100,0xb5753495,894317848,0xb4c4b56d,0xb50e3555,896382148,0xb555b50e,893793570,873903485,0xb56234f1,895235332,0xb57bb448,882193781,894809358,0xb562b4f1,896906389,0xb52cb53e,0xb5463522,895956187,
0xb5353535,896349380,892712245,0xb56db4c4,882226549,0xb5803349,0xb5223546,896611500,877147515,895661297,890156373,0xb575b495,0xb522b546,0xb518354e,0xb4f13562,897070201,896349380,877180283,885306733,0xb57bb448,0xb57f33c9,880358776,0xb4953575,897397782,894809358,0xb522b546,877180283,0xb57fb3c9,0xb4f13562,895956187,0xb3c9357f,897594185,
13696,897581056,13696,897581056,0xb535b535,0xb53e352c,0xb4c4b56d,0xb57134ac,897581056,868857215,892679477,893826338,0xb5353535,890778958,0xb56db4c4,0xb504355c,46464,0xb57bb448,897581056,877180283,892679477,895268100,0xb56d34c4,873870717,0xb5800000,0xb4953575,892712245,0xb4f1b562,892712245,0xb4dbb567,0xb4c4356d,893269292,
13696,896349380,46464,0xb56db4c4,0xb535b535,0xb57134ac,885273965,897594185,897581056,888255842,0xb535b535,0xb5753495,0xb5353535,880358776,896349380,894350616,46464,0xb555b50e,0xb5800000,0xb50e3555,892679477,897430550,896382148,880391544,0xb5800000,0xb5223546,0xb5353535,868824447,892712245,0xb349b580,885306733,0xb4dbb567,
46464,0xb5800000,46464,0xb5800000,891434310,894350616,882193781,897102969,0xb57bb448,0xb4953575,0xb50eb555,0xb56234f1,895661297,0xb4acb571,893793570,893302060,0xb4c4356d,894776590,0xb56db4c4,0xb50e3555,0xb495b575,0xb5803349,897528777,883733873,894776590,893826338,0xb57b3448,0xb3c9357f,0xb57f33c9,0xb4793578,895661297,0xb416b57d,
892712245,0xb4c4b56d,0xb5353535,885273965,0xb3c9357f,895235332,888255842,0xb518b54e,0xb50eb555,0xb57f33c9,0xb448357b,893793570,896873621,893302060,0xb3c9b57f,0xb567b4db,0xb56d34c4,0xb448357b,885273965,897266760,888255842,0xb4dbb567,0xb522b546,0xb5803349,877147515,895628529,894776590,896906389,0xb546b522,0xb57d3416,0xb575b495,0xb55c3504,
13696,897581056,13696,897581056,0xb50eb555,0xb55c3504,0xb448b57b,0xb57d3416,896349380,888255842,885273965,896906389,0xb57b3448,860435840,0xb50eb555,0xb56734db,892712245,0xb50eb555,892679477,894809358,0xb448357b,895956187,0xb555b50e,0xb53e352c,0xb4c4b56d,0xb57f33c9,896349380,891467078,894776590,894350616,0xb57bb448,0xb504355c,
0xb5800000,0xb4c4356d,897581056,885306733,894809358,0xb416b57d,0xb57b3448,0xb4793578,0xb4c4356d,891434310,896382148,868857215,0xb448b57b,0xb571b4ac,0xb555350e,860435840,892679477,897299528,892712245,0xb448b57b,0xb57bb448,0xb53e352c,0xb50e3555,883701105,896382148,882226549,885306733,0xb4f1b562,0xb50e3555,880358776,0xb448357b,890778958,
46464,0xb5800000,46464,0xb5800000,888223074,895988955,868824447,897626953,0xb555b50e,0xb5223546,0xb448b57b,0xb57f33c9,897528777,880391544,882193781,897430550,0xb56d34c4,877147515,0xb4c4b56d,0xb57b3448,891467078,0xb518b54e,888223074,897102969,0xb448357b,895628529,0xb50eb555,0xb5753495,0xb495b575,0xb580b349,891434310,896644268,
892679477,896382148,0xb535b535,0xb56d34c4,0xb575b495,0xb52c353e,893793570,895988955,897299528,882226549,0xb555b50e,0xb56234f1,0xb5463522,873870717,895628529,895268100,885306733,0xb50eb555,0xb56db4c4,0xb555350e,868824447,895235332,896873621,894350616,0xb50eb555,0xb57fb3c9,0xb57bb448,0xb5463522,895628529,896644268,897528777,893302060,
13696,892679477,13696,892679477,0xb535b535,0xb580b349,0xb4c4b56d,0xb567b4db,897581056,893826338,892679477,897528777,0xb5353535,0xb416357d,0xb56db4c4,0xb5783479,46464,0xb50eb555,897581056,894809358,892679477,897070201,0xb56d34c4,0xb518354e,0xb5800000,0xb56234f1,892712245,882226549,892712245,883733873,0xb4c4356d,860435840,
13696,885273965,46464,0xb4c4b56d,0xb535b535,0xb567b4db,885273965,892089662,897581056,896906389,0xb535b535,0xb562b4f1,0xb5353535,0xb504355c,896349380,897397782,46464,0xb448b57b,0xb5800000,0xb57b3448,892679477,894317848,896382148,895268100,0xb5800000,0xb57f33c9,0xb5353535,0xb5223546,892712245,892122430,885306733,883733873,
897581056,892712245,897581056,892712245,0xb5463522,0xb416357d,0xb5753495,0xb504355c,877180283,0xb4f1b562,894809358,882226549,888223074,895956187,0xb5223546,0xb3493580,0xb56db4c4,0xb57b3448,885306733,0xb448b57b,896906389,892122430,0xb3c9357f,886781287,0xb50e3555,0xb3c9357f,0xb448b57b,0xb522b546,0xb3c9b57f,0xb504b55c,888223074,894317848,
892679477,896349380,0xb535b535,0xb56db4c4,0xb57fb3c9,0xb5783479,895628529,897397782,894809358,891467078,0xb57bb448,0xb57f33c9,0xb4953575,0xb3493580,897561545,896644268,0xb4c4b56d,0xb50eb555,0xb56d34c4,0xb555350e,895628529,896611500,893826338,892122430,0xb57b3448,0xb5753495,0xb50e3555,0xb4f13562,891467078,890811726,882226549,880391544,
46464,0xb535b535,46464,0xb535b535,890123605,897070201,877147515,894317848,0xb56db4c4,0xb5753495,0xb4c4b56d,0xb562b4f1,897299528,892122430,890123605,896611500,0xb5353535,0xb448357b,0xb535b535,0xb57bb448,877180283,0xb4acb571,894776590,897594185,885273965,893793570,0xb56db4c4,0xb57f33c9,0xb555b50e,0xb57db416,897266760,897102969,
897581056,896382148,0xb5800000,0xb56d34c4,0xb555350e,0xb518354e,897299528,895268100,885306733,868857215,0xb56d34c4,0xb5463522,877147515,886781287,894809358,892122430,0xb535b535,0xb555b50e,0xb5353535,0xb50e3555,897266760,897594185,890156373,886814055,0xb56d34c4,0xb56234f1,0xb4c4356d,0xb4953575,890156373,889501020,877180283,873903485,
0xb5800000,0xb5353535,0xb5800000,0xb5353535,895661297,883733873,897561545,892122430,0xb50e3555,868824447,0xb57b3448,0xb5223546,868857215,0xb504b55c,896906389,890811726,885273965,894776590,0xb56d34c4,0xb50e3555,0xb546b522,0xb57db416,895661297,889501020,897266760,896906389,0xb555350e,0xb4f13562,0xb5753495,0xb53e352c,893826338,886814055,
892712245,885306733,0xb5353535,0xb4c4356d,0xb4953575,860435840,891467078,883733873,0xb448b57b,0xb4f1b562,0xb50e3555,0xb4953575,891434310,894317848,888255842,880391544,0xb56db4c4,0xb57bb448,0xb4c4356d,0xb448357b,897561545,897102969,882226549,873903485,0xb555350e,0xb5463522,0xb448357b,0xb3c9357f,888255842,886814055,868857215,860468608,
0,7296,14336,21632,16,7312,14352,21648,32,7328,14368,21664,48,7344,14384,21680,64,7360,14400,21696,80,7376,14416,21712,96,7392,14432,21728,112,7408,14448,21744,
128,7168,14464,21504,144,7184,14480,21520,160,7200,14496,21536,176,7216,14512,21552,192,7232,14528,21568,208,7248,14544,21584,224,7264,14560,21600,240,7280,14576,21616,
2080,9376,16416,23712,2096,9392,16432,23728,2048,9344,16384,23680,2064,9360,16400,23696,2144,9440,16480,23776,2160,9456,16496,23792,2112,9408,16448,23744,2128,9424,16464,23760,
2208,9248,16544,23584,2224,9264,16560,23600,2176,9216,16512,23552,2192,9232,16528,23568,2272,9312,16608,23648,2288,9328,16624,23664,2240,9280,16576,23616,2256,9296,16592,23632,
4160,11456,18496,25792,4176,11472,18512,25808,4192,11488,18528,25824,4208,11504,18544,25840,4096,11392,18432,25728,4112,11408,18448,25744,4128,11424,18464,25760,4144,11440,18480,25776,
4288,11328,18624,25664,4304,11344,18640,25680,4320,11360,18656,25696,4336,11376,18672,25712,4224,11264,18560,25600,4240,11280,18576,25616,4256,11296,18592,25632,4272,11312,18608,25648,
6240,13536,20576,27872,6256,13552,20592,27888,6208,13504,20544,27840,6224,13520,20560,27856,6176,13472,20512,27808,6192,13488,20528,27824,6144,13440,20480,27776,6160,13456,20496,27792,
6368,13408,20704,27744,6384,13424,20720,27760,6336,13376,20672,27712,6352,13392,20688,27728,6304,13344,20640,27680,6320,13360,20656,27696,6272,13312,20608,27648,6288,13328,20624,27664,
16384,0,0,0,16400,16,128,4,16416,32,256,8,16432,48,384,12,16448,64,512,16,16464,80,640,20,16480,96,768,24,16496,112,896,28,
16512,2048,1024,32,16528,2064,1152,36,16544,2080,1280,40,16560,2096,1408,44,16576,2112,1536,48,16592,2128,1664,52,16608,2144,1792,56,16624,2160,1920,60,
16640,4128,2048,32768,16656,4144,2176,32772,16672,4096,2304,32776,16688,4112,2432,32780,16704,4192,2560,32784,16720,4208,2688,32788,16736,4160,2816,32792,16752,4176,2944,32796,
16768,6176,3072,32800,16784,6192,3200,32804,16800,6144,3328,32808,16816,6160,3456,32812,16832,6240,3584,32816,16848,6256,3712,32820,16864,6208,3840,32824,16880,6224,3968,32828,
16896,8256,0,65536,16912,8272,128,65540,16928,8288,256,65544,16944,8304,384,65548,16960,8192,512,65552,16976,8208,640,65556,16992,8224,768,65560,17008,8240,896,65564,
17024,10304,1024,65568,17040,10320,1152,65572,17056,10336,1280,65576,17072,10352,1408,65580,17088,10240,1536,65584,17104,10256,1664,65588,17120,10272,1792,65592,17136,10288,1920,65596,
17152,12384,2048,98304,17168,12400,2176,98308,17184,12352,2304,98312,17200,12368,2432,98316,17216,12320,2560,98320,17232,12336,2688,98324,17248,12288,2816,98328,17264,12304,2944,98332,
17280,14432,3072,98336,17296,14448,3200,98340,17312,14400,3328,98344,17328,14416,3456,98348,17344,14368,3584,98352,17360,14384,3712,98356,17376,14336,3840,98360,17392,14352,3968,98364,
65536,65568,65600,65632,65544,65576,65608,65640,65552,65584,65616,65648,65560,65592,65624,65656,65824,65792,65888,65856,65832,65800,65896,65864,65840,65808,65904,65872,65848,65816,65912,65880,
66112,66144,66048,66080,66120,66152,66056,66088,66128,66160,66064,66096,66136,66168,66072,66104,66400,66368,66336,66304,66408,66376,66344,66312,66416,66384,66352,66320,66424,66392,66360,66328,
66688,66720,66752,66784,66696,66728,66760,66792,66704,66736,66768,66800,66712,66744,66776,66808,66976,66944,67040,67008,66984,66952,67048,67016,66992,66960,67056,67024,67000,66968,67064,67032,
67264,67296,67200,67232,67272,67304,67208,67240,67280,67312,67216,67248,67288,67320,67224,67256,67552,67520,67488,67456,67560,67528,67496,67464,67568,67536,67504,67472,67576,67544,67512,67480,
67584,67616,67648,67680,67592,67624,67656,67688,67600,67632,67664,67696,67608,67640,67672,67704,67872,67840,67936,67904,67880,67848,67944,67912,67888,67856,67952,67920,67896,67864,67960,67928,
68160,68192,68096,68128,68168,68200,68104,68136,68176,68208,68112,68144,68184,68216,68120,68152,68448,68416,68384,68352,68456,68424,68392,68360,68464,68432,68400,68368,68472,68440,68408,68376,
68736,68768,68800,68832,68744,68776,68808,68840,68752,68784,68816,68848,68760,68792,68824,68856,69024,68992,69088,69056,69032,69000,69096,69064,69040,69008,69104,69072,69048,69016,69112,69080,
69312,69344,69248,69280,69320,69352,69256,69288,69328,69360,69264,69296,69336,69368,69272,69304,69600,69568,69536,69504,69608,69576,69544,69512,69616,69584,69552,69520,69624,69592,69560,69528,
65664,65696,65728,65760,65672,65704,65736,65768,65680,65712,65744,65776,65688,65720,65752,65784,65952,65920,66016,65984,65960,65928,66024,65992,65968,65936,66032,66000,65976,65944,66040,66008,
66240,66272,66176,66208,66248,66280,66184,66216,66256,66288,66192,66224,66264,66296,66200,66232,66528,66496,66464,66432,66536,66504,66472,66440,66544,66512,66480,66448,66552,66520,66488,66456,
66560,66592,66624,66656,66568,66600,66632,66664,66576,66608,66640,66672,66584,66616,66648,66680,66848,66816,66912,66880,66856,66824,66920,66888,66864,66832,66928,66896,66872,66840,66936,66904,
67136,67168,67072,67104,67144,67176,67080,67112,67152,67184,67088,67120,67160,67192,67096,67128,67424,67392,67360,67328,67432,67400,67368,67336,67440,67408,67376,67344,67448,67416,67384,67352,
67712,67744,67776,67808,67720,67752,67784,67816,67728,67760,67792,67824,67736,67768,67800,67832,68000,67968,68064,68032,68008,67976,68072,68040,68016,67984,68080,68048,68024,67992,68088,68056,
68288,68320,68224,68256,68296,68328,68232,68264,68304,68336,68240,68272,68312,68344,68248,68280,68576,68544,68512,68480,68584,68552,68520,68488,68592,68560,68528,68496,68600,68568,68536,68504,
68608,68640,68672,68704,68616,68648,68680,68712,68624,68656,68688,68720,68632,68664,68696,68728,68896,68864,68960,68928,68904,68872,68968,68936,68912,68880,68976,68944,68920,68888,68984,68952,
69184,69216,69120,69152,69192,69224,69128,69160,69200,69232,69136,69168,69208,69240,69144,69176,69472,69440,69408,69376,69480,69448,69416,69384,69488,69456,69424,69392,69496,69464,69432,69400,
16384,16416,16448,16480,16392,16424,16456,16488,16400,16432,16464,16496,16408,16440,16472,16504,16512,16544,16576,16608,16520,16552,16584,16616,16528,16560,16592,16624,16536,16568,16600,16632,
16672,16640,16736,16704,16680,16648,16744,16712,16688,16656,16752,16720,16696,16664,16760,16728,16800,16768,16864,16832,16808,16776,16872,16840,16816,16784,16880,16848,16824,16792,16888,16856,
16960,16992,16896,16928,16968,17000,16904,16936,16976,17008,16912,16944,16984,17016,16920,16952,17088,17120,17024,17056,17096,17128,17032,17064,17104,17136,17040,17072,17112,17144,17048,17080,
17248,17216,17184,17152,17256,17224,17192,17160,17264,17232,17200,17168,17272,17240,17208,17176,17376,17344,17312,17280,17384,17352,17320,17288,17392,17360,17328,17296,17400,17368,17336,17304,
17408,17440,17472,17504,17416,17448,17480,17512,17424,17456,17488,17520,17432,17464,17496,17528,17536,17568,17600,17632,17544,17576,17608,17640,17552,17584,17616,17648,17560,17592,17624,17656,
17696,17664,17760,17728,17704,17672,17768,17736,17712,17680,17776,17744,17720,17688,17784,17752,17824,17792,17888,17856,17832,17800,17896,17864,17840,17808,17904,17872,17848,17816,17912,17880,
17984,18016,17920,17952,17992,18024,17928,17960,18000,18032,17936,17968,18008,18040,17944,17976,18112,18144,18048,18080,18120,18152,18056,18088,18128,18160,18064,18096,18136,18168,18072,18104,
18272,18240,18208,18176,18280,18248,18216,18184,18288,18256,18224,18192,18296,18264,18232,18200,18400,18368,18336,18304,18408,18376,18344,18312,18416,18384,18352,18320,18424,18392,18360,18328,
0,0,65536,65536,16,16,65552,65680,32,32,65568,65824,48,48,65584,65968,64,64,65600,66112,80,80,65616,66256,96,96,65632,66400,112,112,65648,66544,
128,32784,65664,66560,144,32768,65680,66704,160,32816,65696,66848,176,32800,65712,66992,192,32848,65728,67136,208,32832,65744,67280,224,32880,65760,67424,240,32864,65776,67568,
256,65568,65792,65544,272,65584,65808,65688,288,65536,65824,65832,304,65552,65840,65976,320,65632,65856,66120,336,65648,65872,66264,352,65600,65888,66408,368,65616,65904,66552,
384,98352,65920,66568,400,98336,65936,66712,416,98320,65952,66856,432,98304,65968,67000,448,98416,65984,67144,464,98400,66000,67288,480,98384,66016,67432,496,98368,66032,67576,
512,131136,66048,65552,528,131152,66064,65664,544,131168,66080,65840,560,131184,66096,65952,576,131072,66112,66128,592,131088,66128,66240,608,131104,66144,66416,624,131120,66160,66528,
640,163920,66176,66576,656,163904,66192,66688,672,163952,66208,66864,688,163936,66224,66976,704,163856,66240,67152,720,163840,66256,67264,736,163888,66272,67440,752,163872,66288,67552,
768,196704,66304,65560,784,196720,66320,65672,800,196672,66336,65848,816,196688,66352,65960,832,196640,66368,66136,848,196656,66384,66248,864,196608,66400,66424,880,196624,66416,66536,
896,229488,66432,66584,912,229472,66448,66696,928,229456,66464,66872,944,229440,66480,66984,960,229424,66496,67160,976,229408,66512,67272,992,229392,66528,67448,1008,229376,66544,67560,
32768,40064,47104,54400,32784,40080,47120,54416,32800,40096,47136,54432,32816,40112,47152,54448,32832,40128,47168,54464,32848,40144,47184,54480,32864,40160,47200,54496,32880,40176,47216,54512,
32896,39936,47232,54272,32912,39952,47248,54288,32928,39968,47264,54304,32944,39984,47280,54320,32960,40000,47296,54336,32976,40016,47312,54352,32992,40032,47328,54368,33008,40048,47344,54384,
34848,42144,49184,56480,34864,42160,49200,56496,34816,42112,49152,56448,34832,42128,49168,56464,34912,42208,49248,56544,34928,42224,49264,56560,34880,42176,49216,56512,34896,42192,49232,56528,
34976,42016,49312,56352,34992,42032,49328,56368,34944,41984,49280,56320,34960,42000,49296,56336,35040,42080,49376,56416,35056,42096,49392,56432,35008,42048,49344,56384,35024,42064,49360,56400,
36928,44224,51264,58560,36944,44240,51280,58576,36960,44256,51296,58592,36976,44272,51312,58608,36864,44160,51200,58496,36880,44176,51216,58512,36896,44192,51232,58528,36912,44208,51248,58544,
37056,44096,51392,58432,37072,44112,51408,58448,37088,44128,51424,58464,37104,44144,51440,58480,36992,44032,51328,58368,37008,44048,51344,58384,37024,44064,51360,58400,37040,44080,51376,58416,
39008,46304,53344,60640,39024,46320,53360,60656,38976,46272,53312,60608,38992,46288,53328,60624,38944,46240,53280,60576,38960,46256,53296,60592,38912,46208,53248,60544,38928,46224,53264,60560,
39136,46176,53472,60512,39152,46192,53488,60528,39104,46144,53440,60480,39120,46160,53456,60496,39072,46112,53408,60448,39088,46128,53424,60464,39040,46080,53376,60416,39056,46096,53392,60432,
16384,0,0,0,16400,16,128,4,16416,32,256,8,16432,48,384,12,16448,64,512,16,16464,80,640,20,16480,96,768,24,16496,112,896,28,
16512,2048,1024,32,16528,2064,1152,36,16544,2080,1280,40,16560,2096,1408,44,16576,2112,1536,48,16592,2128,1664,52,16608,2144,1792,56,16624,2160,1920,60,
16640,4128,2048,32768,16656,4144,2176,32772,16672,4096,2304,32776,16688,4112,2432,32780,16704,4192,2560,32784,16720,4208,2688,32788,16736,4160,2816,32792,16752,4176,2944,32796,
16768,6176,3072,32800,16784,6192,3200,32804,16800,6144,3328,32808,16816,6160,3456,32812,16832,6240,3584,32816,16848,6256,3712,32820,16864,6208,3840,32824,16880,6224,3968,32828,
16896,8256,0,65536,16912,8272,128,65540,16928,8288,256,65544,16944,8304,384,65548,16960,8192,512,65552,16976,8208,640,65556,16992,8224,768,65560,17008,8240,896,65564,
17024,10304,1024,65568,17040,10320,1152,65572,17056,10336,1280,65576,17072,10352,1408,65580,17088,10240,1536,65584,17104,10256,1664,65588,17120,10272,1792,65592,17136,10288,1920,65596,
17152,12384,2048,98304,17168,12400,2176,98308,17184,12352,2304,98312,17200,12368,2432,98316,17216,12320,2560,98320,17232,12336,2688,98324,17248,12288,2816,98328,17264,12304,2944,98332,
17280,14432,3072,98336,17296,14448,3200,98340,17312,14400,3328,98344,17328,14416,3456,98348,17344,14368,3584,98352,17360,14384,3712,98356,17376,14336,3840,98360,17392,14352,3968,98364,
65536,65568,65600,65632,65544,65576,65608,65640,65552,65584,65616,65648,65560,65592,65624,65656,65824,65792,65888,65856,65832,65800,65896,65864,65840,65808,65904,65872,65848,65816,65912,65880,
66112,66144,66048,66080,66120,66152,66056,66088,66128,66160,66064,66096,66136,66168,66072,66104,66400,66368,66336,66304,66408,66376,66344,66312,66416,66384,66352,66320,66424,66392,66360,66328,
66688,66720,66752,66784,66696,66728,66760,66792,66704,66736,66768,66800,66712,66744,66776,66808,66976,66944,67040,67008,66984,66952,67048,67016,66992,66960,67056,67024,67000,66968,67064,67032,
67264,67296,67200,67232,67272,67304,67208,67240,67280,67312,67216,67248,67288,67320,67224,67256,67552,67520,67488,67456,67560,67528,67496,67464,67568,67536,67504,67472,67576,67544,67512,67480,
67584,67616,67648,67680,67592,67624,67656,67688,67600,67632,67664,67696,67608,67640,67672,67704,67872,67840,67936,67904,67880,67848,67944,67912,67888,67856,67952,67920,67896,67864,67960,67928,
68160,68192,68096,68128,68168,68200,68104,68136,68176,68208,68112,68144,68184,68216,68120,68152,68448,68416,68384,68352,68456,68424,68392,68360,68464,68432,68400,68368,68472,68440,68408,68376,
68736,68768,68800,68832,68744,68776,68808,68840,68752,68784,68816,68848,68760,68792,68824,68856,69024,68992,69088,69056,69032,69000,69096,69064,69040,69008,69104,69072,69048,69016,69112,69080,
69312,69344,69248,69280,69320,69352,69256,69288,69328,69360,69264,69296,69336,69368,69272,69304,69600,69568,69536,69504,69608,69576,69544,69512,69616,69584,69552,69520,69624,69592,69560,69528,
65664,65696,65728,65760,65672,65704,65736,65768,65680,65712,65744,65776,65688,65720,65752,65784,65952,65920,66016,65984,65960,65928,66024,65992,65968,65936,66032,66000,65976,65944,66040,66008,
66240,66272,66176,66208,66248,66280,66184,66216,66256,66288,66192,66224,66264,66296,66200,66232,66528,66496,66464,66432,66536,66504,66472,66440,66544,66512,66480,66448,66552,66520,66488,66456,
66560,66592,66624,66656,66568,66600,66632,66664,66576,66608,66640,66672,66584,66616,66648,66680,66848,66816,66912,66880,66856,66824,66920,66888,66864,66832,66928,66896,66872,66840,66936,66904,
67136,67168,67072,67104,67144,67176,67080,67112,67152,67184,67088,67120,67160,67192,67096,67128,67424,67392,67360,67328,67432,67400,67368,67336,67440,67408,67376,67344,67448,67416,67384,67352,
67712,67744,67776,67808,67720,67752,67784,67816,67728,67760,67792,67824,67736,67768,67800,67832,68000,67968,68064,68032,68008,67976,68072,68040,68016,67984,68080,68048,68024,67992,68088,68056,
68288,68320,68224,68256,68296,68328,68232,68264,68304,68336,68240,68272,68312,68344,68248,68280,68576,68544,68512,68480,68584,68552,68520,68488,68592,68560,68528,68496,68600,68568,68536,68504,
68608,68640,68672,68704,68616,68648,68680,68712,68624,68656,68688,68720,68632,68664,68696,68728,68896,68864,68960,68928,68904,68872,68968,68936,68912,68880,68976,68944,68920,68888,68984,68952,
69184,69216,69120,69152,69192,69224,69128,69160,69200,69232,69136,69168,69208,69240,69144,69176,69472,69440,69408,69376,69480,69448,69416,69384,69488,69456,69424,69392,69496,69464,69432,69400,
16384,16416,16448,16480,16392,16424,16456,16488,16400,16432,16464,16496,16408,16440,16472,16504,16512,16544,16576,16608,16520,16552,16584,16616,16528,16560,16592,16624,16536,16568,16600,16632,
16672,16640,16736,16704,16680,16648,16744,16712,16688,16656,16752,16720,16696,16664,16760,16728,16800,16768,16864,16832,16808,16776,16872,16840,16816,16784,16880,16848,16824,16792,16888,16856,
16960,16992,16896,16928,16968,17000,16904,16936,16976,17008,16912,16944,16984,17016,16920,16952,17088,17120,17024,17056,17096,17128,17032,17064,17104,17136,17040,17072,17112,17144,17048,17080,
17248,17216,17184,17152,17256,17224,17192,17160,17264,17232,17200,17168,17272,17240,17208,17176,17376,17344,17312,17280,17384,17352,17320,17288,17392,17360,17328,17296,17400,17368,17336,17304,
17408,17440,17472,17504,17416,17448,17480,17512,17424,17456,17488,17520,17432,17464,17496,17528,17536,17568,17600,17632,17544,17576,17608,17640,17552,17584,17616,17648,17560,17592,17624,17656,
17696,17664,17760,17728,17704,17672,17768,17736,17712,17680,17776,17744,17720,17688,17784,17752,17824,17792,17888,17856,17832,17800,17896,17864,17840,17808,17904,17872,17848,17816,17912,17880,
17984,18016,17920,17952,17992,18024,17928,17960,18000,18032,17936,17968,18008,18040,17944,17976,18112,18144,18048,18080,18120,18152,18056,18088,18128,18160,18064,18096,18136,18168,18072,18104,
18272,18240,18208,18176,18280,18248,18216,18184,18288,18256,18224,18192,18296,18264,18232,18200,18400,18368,18336,18304,18408,18376,18344,18312,18416,18384,18352,18320,18424,18392,18360,18328,
0,0,69632,69632,16,16,69648,69776,32,32,69664,69920,48,48,69680,70064,64,64,69696,70208,80,80,69712,70352,96,96,69728,70496,112,112,69744,70640,
128,32784,69760,70656,144,32768,69776,70800,160,32816,69792,70944,176,32800,69808,71088,192,32848,69824,71232,208,32832,69840,71376,224,32880,69856,71520,240,32864,69872,71664,
256,65568,69888,69640,272,65584,69904,69784,288,65536,69920,69928,304,65552,69936,70072,320,65632,69952,70216,336,65648,69968,70360,352,65600,69984,70504,368,65616,70000,70648,
384,98352,70016,70664,400,98336,70032,70808,416,98320,70048,70952,432,98304,70064,71096,448,98416,70080,71240,464,98400,70096,71384,480,98384,70112,71528,496,98368,70128,71672,
512,131136,70144,69648,528,131152,70160,69760,544,131168,70176,69936,560,131184,70192,70048,576,131072,70208,70224,592,131088,70224,70336,608,131104,70240,70512,624,131120,70256,70624,
640,163920,70272,70672,656,163904,70288,70784,672,163952,70304,70960,688,163936,70320,71072,704,163856,70336,71248,720,163840,70352,71360,736,163888,70368,71536,752,163872,70384,71648,
768,196704,70400,69656,784,196720,70416,69768,800,196672,70432,69944,816,196688,70448,70056,832,196640,70464,70232,848,196656,70480,70344,864,196608,70496,70520,880,196624,70512,70632,
896,229488,70528,70680,912,229472,70544,70792,928,229456,70560,70968,944,229440,70576,71080,960,229424,70592,71256,976,229408,70608,71368,992,229392,70624,71544,1008,229376,70640,71656,
131072,138368,145408,152704,131088,138384,145424,152720,131104,138400,145440,152736,131120,138416,145456,152752,131136,138432,145472,152768,131152,138448,145488,152784,131168,138464,145504,152800,131184,138480,145520,152816,
131200,138240,145536,152576,131216,138256,145552,152592,131232,138272,145568,152608,131248,138288,145584,152624,131264,138304,145600,152640,131280,138320,145616,152656,131296,138336,145632,152672,131312,138352,145648,152688,
133152,140448,147488,154784,133168,140464,147504,154800,133120,140416,147456,154752,133136,140432,147472,154768,133216,140512,147552,154848,133232,140528,147568,154864,133184,140480,147520,154816,133200,140496,147536,154832,
133280,140320,147616,154656,133296,140336,147632,154672,133248,140288,147584,154624,133264,140304,147600,154640,133344,140384,147680,154720,133360,140400,147696,154736,133312,140352,147648,154688,133328,140368,147664,154704,
135232,142528,149568,156864,135248,142544,149584,156880,135264,142560,149600,156896,135280,142576,149616,156912,135168,142464,149504,156800,135184,142480,149520,156816,135200,142496,149536,156832,135216,142512,149552,156848,
135360,142400,149696,156736,135376,142416,149712,156752,135392,142432,149728,156768,135408,142448,149744,156784,135296,142336,149632,156672,135312,142352,149648,156688,135328,142368,149664,156704,135344,142384,149680,156720,
137312,144608,151648,158944,137328,144624,151664,158960,137280,144576,151616,158912,137296,144592,151632,158928,137248,144544,151584,158880,137264,144560,151600,158896,137216,144512,151552,158848,137232,144528,151568,158864,
137440,144480,151776,158816,137456,144496,151792,158832,137408,144448,151744,158784,137424,144464,151760,158800,137376,144416,151712,158752,137392,144432,151728,158768,137344,144384,151680,158720,137360,144400,151696,158736,
16384,0,0,0,16400,16,128,4,16416,32,256,8,16432,48,384,12,16448,64,512,16,16464,80,640,20,16480,96,768,24,16496,112,896,28,
16512,2048,1024,32,16528,2064,1152,36,16544,2080,1280,40,16560,2096,1408,44,16576,2112,1536,48,16592,2128,1664,52,16608,2144,1792,56,16624,2160,1920,60,
16640,4128,2048,32768,16656,4144,2176,32772,16672,4096,2304,32776,16688,4112,2432,32780,16704,4192,2560,32784,16720,4208,2688,32788,16736,4160,2816,32792,16752,4176,2944,32796,
16768,6176,3072,32800,16784,6192,3200,32804,16800,6144,3328,32808,16816,6160,3456,32812,16832,6240,3584,32816,16848,6256,3712,32820,16864,6208,3840,32824,16880,6224,3968,32828,
16896,8256,0,65536,16912,8272,128,65540,16928,8288,256,65544,16944,8304,384,65548,16960,8192,512,65552,16976,8208,640,65556,16992,8224,768,65560,17008,8240,896,65564,
17024,10304,1024,65568,17040,10320,1152,65572,17056,10336,1280,65576,17072,10352,1408,65580,17088,10240,1536,65584,17104,10256,1664,65588,17120,10272,1792,65592,17136,10288,1920,65596,
17152,12384,2048,98304,17168,12400,2176,98308,17184,12352,2304,98312,17200,12368,2432,98316,17216,12320,2560,98320,17232,12336,2688,98324,17248,12288,2816,98328,17264,12304,2944,98332,
17280,14432,3072,98336,17296,14448,3200,98340,17312,14400,3328,98344,17328,14416,3456,98348,17344,14368,3584,98352,17360,14384,3712,98356,17376,14336,3840,98360,17392,14352,3968,98364,
65536,65568,65600,65632,65544,65576,65608,65640,65552,65584,65616,65648,65560,65592,65624,65656,65824,65792,65888,65856,65832,65800,65896,65864,65840,65808,65904,65872,65848,65816,65912,65880,
66112,66144,66048,66080,66120,66152,66056,66088,66128,66160,66064,66096,66136,66168,66072,66104,66400,66368,66336,66304,66408,66376,66344,66312,66416,66384,66352,66320,66424,66392,66360,66328,
66688,66720,66752,66784,66696,66728,66760,66792,66704,66736,66768,66800,66712,66744,66776,66808,66976,66944,67040,67008,66984,66952,67048,67016,66992,66960,67056,67024,67000,66968,67064,67032,
67264,67296,67200,67232,67272,67304,67208,67240,67280,67312,67216,67248,67288,67320,67224,67256,67552,67520,67488,67456,67560,67528,67496,67464,67568,67536,67504,67472,67576,67544,67512,67480,
67584,67616,67648,67680,67592,67624,67656,67688,67600,67632,67664,67696,67608,67640,67672,67704,67872,67840,67936,67904,67880,67848,67944,67912,67888,67856,67952,67920,67896,67864,67960,67928,
68160,68192,68096,68128,68168,68200,68104,68136,68176,68208,68112,68144,68184,68216,68120,68152,68448,68416,68384,68352,68456,68424,68392,68360,68464,68432,68400,68368,68472,68440,68408,68376,
68736,68768,68800,68832,68744,68776,68808,68840,68752,68784,68816,68848,68760,68792,68824,68856,69024,68992,69088,69056,69032,69000,69096,69064,69040,69008,69104,69072,69048,69016,69112,69080,
69312,69344,69248,69280,69320,69352,69256,69288,69328,69360,69264,69296,69336,69368,69272,69304,69600,69568,69536,69504,69608,69576,69544,69512,69616,69584,69552,69520,69624,69592,69560,69528,
65664,65696,65728,65760,65672,65704,65736,65768,65680,65712,65744,65776,65688,65720,65752,65784,65952,65920,66016,65984,65960,65928,66024,65992,65968,65936,66032,66000,65976,65944,66040,66008,
66240,66272,66176,66208,66248,66280,66184,66216,66256,66288,66192,66224,66264,66296,66200,66232,66528,66496,66464,66432,66536,66504,66472,66440,66544,66512,66480,66448,66552,66520,66488,66456,
66560,66592,66624,66656,66568,66600,66632,66664,66576,66608,66640,66672,66584,66616,66648,66680,66848,66816,66912,66880,66856,66824,66920,66888,66864,66832,66928,66896,66872,66840,66936,66904,
67136,67168,67072,67104,67144,67176,67080,67112,67152,67184,67088,67120,67160,67192,67096,67128,67424,67392,67360,67328,67432,67400,67368,67336,67440,67408,67376,67344,67448,67416,67384,67352,
67712,67744,67776,67808,67720,67752,67784,67816,67728,67760,67792,67824,67736,67768,67800,67832,68000,67968,68064,68032,68008,67976,68072,68040,68016,67984,68080,68048,68024,67992,68088,68056,
68288,68320,68224,68256,68296,68328,68232,68264,68304,68336,68240,68272,68312,68344,68248,68280,68576,68544,68512,68480,68584,68552,68520,68488,68592,68560,68528,68496,68600,68568,68536,68504,
68608,68640,68672,68704,68616,68648,68680,68712,68624,68656,68688,68720,68632,68664,68696,68728,68896,68864,68960,68928,68904,68872,68968,68936,68912,68880,68976,68944,68920,68888,68984,68952,
69184,69216,69120,69152,69192,69224,69128,69160,69200,69232,69136,69168,69208,69240,69144,69176,69472,69440,69408,69376,69480,69448,69416,69384,69488,69456,69424,69392,69496,69464,69432,69400,
16384,16416,16448,16480,16392,16424,16456,16488,16400,16432,16464,16496,16408,16440,16472,16504,16512,16544,16576,16608,16520,16552,16584,16616,16528,16560,16592,16624,16536,16568,16600,16632,
16672,16640,16736,16704,16680,16648,16744,16712,16688,16656,16752,16720,16696,16664,16760,16728,16800,16768,16864,16832,16808,16776,16872,16840,16816,16784,16880,16848,16824,16792,16888,16856,
16960,16992,16896,16928,16968,17000,16904,16936,16976,17008,16912,16944,16984,17016,16920,16952,17088,17120,17024,17056,17096,17128,17032,17064,17104,17136,17040,17072,17112,17144,17048,17080,
17248,17216,17184,17152,17256,17224,17192,17160,17264,17232,17200,17168,17272,17240,17208,17176,17376,17344,17312,17280,17384,17352,17320,17288,17392,17360,17328,17296,17400,17368,17336,17304,
17408,17440,17472,17504,17416,17448,17480,17512,17424,17456,17488,17520,17432,17464,17496,17528,17536,17568,17600,17632,17544,17576,17608,17640,17552,17584,17616,17648,17560,17592,17624,17656,
17696,17664,17760,17728,17704,17672,17768,17736,17712,17680,17776,17744,17720,17688,17784,17752,17824,17792,17888,17856,17832,17800,17896,17864,17840,17808,17904,17872,17848,17816,17912,17880,
17984,18016,17920,17952,17992,18024,17928,17960,18000,18032,17936,17968,18008,18040,17944,17976,18112,18144,18048,18080,18120,18152,18056,18088,18128,18160,18064,18096,18136,18168,18072,18104,
18272,18240,18208,18176,18280,18248,18216,18184,18288,18256,18224,18192,18296,18264,18232,18200,18400,18368,18336,18304,18408,18376,18344,18312,18416,18384,18352,18320,18424,18392,18360,18328,
0,0,73728,73728,16,16,73744,73872,32,32,73760,74016,48,48,73776,74160,64,64,73792,74304,80,80,73808,74448,96,96,73824,74592,112,112,73840,74736,
128,32784,73856,74752,144,32768,73872,74896,160,32816,73888,75040,176,32800,73904,75184,192,32848,73920,75328,208,32832,73936,75472,224,32880,73952,75616,240,32864,73968,75760,
256,65568,73984,73736,272,65584,74000,73880,288,65536,74016,74024,304,65552,74032,74168,320,65632,74048,74312,336,65648,74064,74456,352,65600,74080,74600,368,65616,74096,74744,
384,98352,74112,74760,400,98336,74128,74904,416,98320,74144,75048,432,98304,74160,75192,448,98416,74176,75336,464,98400,74192,75480,480,98384,74208,75624,496,98368,74224,75768,
512,131136,74240,73744,528,131152,74256,73856,544,131168,74272,74032,560,131184,74288,74144,576,131072,74304,74320,592,131088,74320,74432,608,131104,74336,74608,624,131120,74352,74720,
640,163920,74368,74768,656,163904,74384,74880,672,163952,74400,75056,688,163936,74416,75168,704,163856,74432,75344,720,163840,74448,75456,736,163888,74464,75632,752,163872,74480,75744,
768,196704,74496,73752,784,196720,74512,73864,800,196672,74528,74040,816,196688,74544,74152,832,196640,74560,74328,848,196656,74576,74440,864,196608,74592,74616,880,196624,74608,74728,
896,229488,74624,74776,912,229472,74640,74888,928,229456,74656,75064,944,229440,74672,75176,960,229424,74688,75352,976,229408,74704,75464,992,229392,74720,75640,1008,229376,74736,75752,
163840,171136,178176,185472,163856,171152,178192,185488,163872,171168,178208,185504,163888,171184,178224,185520,163904,171200,178240,185536,163920,171216,178256,185552,163936,171232,178272,185568,163952,171248,178288,185584,
163968,171008,178304,185344,163984,171024,178320,185360,164000,171040,178336,185376,164016,171056,178352,185392,164032,171072,178368,185408,164048,171088,178384,185424,164064,171104,178400,185440,164080,171120,178416,185456,
165920,173216,180256,187552,165936,173232,180272,187568,165888,173184,180224,187520,165904,173200,180240,187536,165984,173280,180320,187616,166000,173296,180336,187632,165952,173248,180288,187584,165968,173264,180304,187600,
166048,173088,180384,187424,166064,173104,180400,187440,166016,173056,180352,187392,166032,173072,180368,187408,166112,173152,180448,187488,166128,173168,180464,187504,166080,173120,180416,187456,166096,173136,180432,187472,
168000,175296,182336,189632,168016,175312,182352,189648,168032,175328,182368,189664,168048,175344,182384,189680,167936,175232,182272,189568,167952,175248,182288,189584,167968,175264,182304,189600,167984,175280,182320,189616,
168128,175168,182464,189504,168144,175184,182480,189520,168160,175200,182496,189536,168176,175216,182512,189552,168064,175104,182400,189440,168080,175120,182416,189456,168096,175136,182432,189472,168112,175152,182448,189488,
170080,177376,184416,191712,170096,177392,184432,191728,170048,177344,184384,191680,170064,177360,184400,191696,170016,177312,184352,191648,170032,177328,184368,191664,169984,177280,184320,191616,170000,177296,184336,191632,
170208,177248,184544,191584,170224,177264,184560,191600,170176,177216,184512,191552,170192,177232,184528,191568,170144,177184,184480,191520,170160,177200,184496,191536,170112,177152,184448,191488,170128,177168,184464,191504,
16384,0,0,0,16400,16,128,4,16416,32,256,8,16432,48,384,12,16448,64,512,16,16464,80,640,20,16480,96,768,24,16496,112,896,28,
16512,2048,1024,32,16528,2064,1152,36,16544,2080,1280,40,16560,2096,1408,44,16576,2112,1536,48,16592,2128,1664,52,16608,2144,1792,56,16624,2160,1920,60,
16640,4128,2048,32768,16656,4144,2176,32772,16672,4096,2304,32776,16688,4112,2432,32780,16704,4192,2560,32784,16720,4208,2688,32788,16736,4160,2816,32792,16752,4176,2944,32796,
16768,6176,3072,32800,16784,6192,3200,32804,16800,6144,3328,32808,16816,6160,3456,32812,16832,6240,3584,32816,16848,6256,3712,32820,16864,6208,3840,32824,16880,6224,3968,32828,
16896,8256,0,65536,16912,8272,128,65540,16928,8288,256,65544,16944,8304,384,65548,16960,8192,512,65552,16976,8208,640,65556,16992,8224,768,65560,17008,8240,896,65564,
17024,10304,1024,65568,17040,10320,1152,65572,17056,10336,1280,65576,17072,10352,1408,65580,17088,10240,1536,65584,17104,10256,1664,65588,17120,10272,1792,65592,17136,10288,1920,65596,
17152,12384,2048,98304,17168,12400,2176,98308,17184,12352,2304,98312,17200,12368,2432,98316,17216,12320,2560,98320,17232,12336,2688,98324,17248,12288,2816,98328,17264,12304,2944,98332,
17280,14432,3072,98336,17296,14448,3200,98340,17312,14400,3328,98344,17328,14416,3456,98348,17344,14368,3584,98352,17360,14384,3712,98356,17376,14336,3840,98360,17392,14352,3968,98364,
65536,65568,65600,65632,65544,65576,65608,65640,65552,65584,65616,65648,65560,65592,65624,65656,65824,65792,65888,65856,65832,65800,65896,65864,65840,65808,65904,65872,65848,65816,65912,65880,
66112,66144,66048,66080,66120,66152,66056,66088,66128,66160,66064,66096,66136,66168,66072,66104,66400,66368,66336,66304,66408,66376,66344,66312,66416,66384,66352,66320,66424,66392,66360,66328,
66688,66720,66752,66784,66696,66728,66760,66792,66704,66736,66768,66800,66712,66744,66776,66808,66976,66944,67040,67008,66984,66952,67048,67016,66992,66960,67056,67024,67000,66968,67064,67032,
67264,67296,67200,67232,67272,67304,67208,67240,67280,67312,67216,67248,67288,67320,67224,67256,67552,67520,67488,67456,67560,67528,67496,67464,67568,67536,67504,67472,67576,67544,67512,67480,
67584,67616,67648,67680,67592,67624,67656,67688,67600,67632,67664,67696,67608,67640,67672,67704,67872,67840,67936,67904,67880,67848,67944,67912,67888,67856,67952,67920,67896,67864,67960,67928,
68160,68192,68096,68128,68168,68200,68104,68136,68176,68208,68112,68144,68184,68216,68120,68152,68448,68416,68384,68352,68456,68424,68392,68360,68464,68432,68400,68368,68472,68440,68408,68376,
68736,68768,68800,68832,68744,68776,68808,68840,68752,68784,68816,68848,68760,68792,68824,68856,69024,68992,69088,69056,69032,69000,69096,69064,69040,69008,69104,69072,69048,69016,69112,69080,
69312,69344,69248,69280,69320,69352,69256,69288,69328,69360,69264,69296,69336,69368,69272,69304,69600,69568,69536,69504,69608,69576,69544,69512,69616,69584,69552,69520,69624,69592,69560,69528,
65664,65696,65728,65760,65672,65704,65736,65768,65680,65712,65744,65776,65688,65720,65752,65784,65952,65920,66016,65984,65960,65928,66024,65992,65968,65936,66032,66000,65976,65944,66040,66008,
66240,66272,66176,66208,66248,66280,66184,66216,66256,66288,66192,66224,66264,66296,66200,66232,66528,66496,66464,66432,66536,66504,66472,66440,66544,66512,66480,66448,66552,66520,66488,66456,
66560,66592,66624,66656,66568,66600,66632,66664,66576,66608,66640,66672,66584,66616,66648,66680,66848,66816,66912,66880,66856,66824,66920,66888,66864,66832,66928,66896,66872,66840,66936,66904,
67136,67168,67072,67104,67144,67176,67080,67112,67152,67184,67088,67120,67160,67192,67096,67128,67424,67392,67360,67328,67432,67400,67368,67336,67440,67408,67376,67344,67448,67416,67384,67352,
67712,67744,67776,67808,67720,67752,67784,67816,67728,67760,67792,67824,67736,67768,67800,67832,68000,67968,68064,68032,68008,67976,68072,68040,68016,67984,68080,68048,68024,67992,68088,68056,
68288,68320,68224,68256,68296,68328,68232,68264,68304,68336,68240,68272,68312,68344,68248,68280,68576,68544,68512,68480,68584,68552,68520,68488,68592,68560,68528,68496,68600,68568,68536,68504,
68608,68640,68672,68704,68616,68648,68680,68712,68624,68656,68688,68720,68632,68664,68696,68728,68896,68864,68960,68928,68904,68872,68968,68936,68912,68880,68976,68944,68920,68888,68984,68952,
69184,69216,69120,69152,69192,69224,69128,69160,69200,69232,69136,69168,69208,69240,69144,69176,69472,69440,69408,69376,69480,69448,69416,69384,69488,69456,69424,69392,69496,69464,69432,69400,
16384,16416,16448,16480,16392,16424,16456,16488,16400,16432,16464,16496,16408,16440,16472,16504,16512,16544,16576,16608,16520,16552,16584,16616,16528,16560,16592,16624,16536,16568,16600,16632,
16672,16640,16736,16704,16680,16648,16744,16712,16688,16656,16752,16720,16696,16664,16760,16728,16800,16768,16864,16832,16808,16776,16872,16840,16816,16784,16880,16848,16824,16792,16888,16856,
16960,16992,16896,16928,16968,17000,16904,16936,16976,17008,16912,16944,16984,17016,16920,16952,17088,17120,17024,17056,17096,17128,17032,17064,17104,17136,17040,17072,17112,17144,17048,17080,
17248,17216,17184,17152,17256,17224,17192,17160,17264,17232,17200,17168,17272,17240,17208,17176,17376,17344,17312,17280,17384,17352,17320,17288,17392,17360,17328,17296,17400,17368,17336,17304,
17408,17440,17472,17504,17416,17448,17480,17512,17424,17456,17488,17520,17432,17464,17496,17528,17536,17568,17600,17632,17544,17576,17608,17640,17552,17584,17616,17648,17560,17592,17624,17656,
17696,17664,17760,17728,17704,17672,17768,17736,17712,17680,17776,17744,17720,17688,17784,17752,17824,17792,17888,17856,17832,17800,17896,17864,17840,17808,17904,17872,17848,17816,17912,17880,
17984,18016,17920,17952,17992,18024,17928,17960,18000,18032,17936,17968,18008,18040,17944,17976,18112,18144,18048,18080,18120,18152,18056,18088,18128,18160,18064,18096,18136,18168,18072,18104,
18272,18240,18208,18176,18280,18248,18216,18184,18288,18256,18224,18192,18296,18264,18232,18200,18400,18368,18336,18304,18408,18376,18344,18312,18416,18384,18352,18320,18424,18392,18360,18328,
0,0,77824,77824,16,16,77840,77968,32,32,77856,78112,48,48,77872,78256,64,64,77888,78400,80,80,77904,78544,96,96,77920,78688,112,112,77936,78832,
128,32784,77952,78848,144,32768,77968,78992,160,32816,77984,79136,176,32800,78000,79280,192,32848,78016,79424,208,32832,78032,79568,224,32880,78048,79712,240,32864,78064,79856,
256,65568,78080,77832,272,65584,78096,77976,288,65536,78112,78120,304,65552,78128,78264,320,65632,78144,78408,336,65648,78160,78552,352,65600,78176,78696,368,65616,78192,78840,
384,98352,78208,78856,400,98336,78224,79000,416,98320,78240,79144,432,98304,78256,79288,448,98416,78272,79432,464,98400,78288,79576,480,98384,78304,79720,496,98368,78320,79864,
512,131136,78336,77840,528,131152,78352,77952,544,131168,78368,78128,560,131184,78384,78240,576,131072,78400,78416,592,131088,78416,78528,608,131104,78432,78704,624,131120,78448,78816,
640,163920,78464,78864,656,163904,78480,78976,672,163952,78496,79152,688,163936,78512,79264,704,163856,78528,79440,720,163840,78544,79552,736,163888,78560,79728,752,163872,78576,79840,
768,196704,78592,77848,784,196720,78608,77960,800,196672,78624,78136,816,196688,78640,78248,832,196640,78656,78424,848,196656,78672,78536,864,196608,78688,78712,880,196624,78704,78824,
896,229488,78720,78872,912,229472,78736,78984,928,229456,78752,79160,944,229440,78768,79272,960,229424,78784,79448,976,229408,78800,79560,992,229392,78816,79736,1008,229376,78832,79848,
};

__global__ __launch_bounds__(256,1) void fused(const u16* x,const u16* wr,const u16* wi,const u16* wg,const u16* wu,const u16* wd,const u16* gw,char* ws,u16* out){
  extern __shared__ char smem[];
  const unsigned b=blockIdx.x, xcd=b&7, cu=b>>3, tid=threadIdx.x, w=rfl(tid>>6);
  unsigned l=tid&63;

  const u64 ka_=(u64)__builtin_amdgcn_kernarg_segment_ptr(); u32x4 kb_; kb_[0]=(unsigned)ka_; kb_[1]=(unsigned)(ka_>>32); kb_[2]=0; kb_[3]=b;
  asm volatile(
"s_cmp_lt_u32 s19, 32\n"
"s_cbranch_scc1 .Lfft0\n"
".Lg1beg:\n"
"s_load_dwordx2 s[40:41], s[16:17], 0x0\n"
"s_load_dwordx4 s[44:47], s[16:17], 0x18\n"
"s_load_dwordx4 s[32:35], s[16:17], 0x30\n"
"s_getpc_b64 s[94:95]\n"
"v_and_b32 v4, 15, v0\n"
"v_lshrrev_b32 v5, 6, v0\n"
"v_lshl_or_b32 v4, v5, 4, v4\n"
"v_min_u32 v4, 63, v4\n"
"v_lshlrev_b32 v4, 7, v4\n"
"global_load_dword a191, v4, s[94:95]\n"
"v_readfirstlane_b32 s92, v0\n"
"v_and_b32 v131, 63, v0\n"
"s_lshr_b32 s92, s92, 6\n"
"s_lshl_b32 s98, s92, 10\n"
"s_sub_u32 s52, s19, 32\n"
"s_bfe_u32 s85, s52, 0x20003\n"
"s_lshr_b32 s84, s52, 5\n"
"s_and_b32 s53, s19, 1\n"
"s_lshl_b32 s53, s53, 2\n"
"s_add_u32 s90, s53, s85\n"
"s_bfe_u32 s53, s19, 0x20001\n"
"s_mul_i32 s53, s53, 7\n"
"s_add_u32 s91, s53, s84\n"
"s_lshl_b32 s86, s90, 12\n"
"s_lshl_b32 s53, s92, 18\n"
"s_add_u32 s86, s86, s53\n"
"s_and_b32 s53, s91, 1\n"
"s_lshl_b32 s53, s53, 11\n"
"s_add_u32 s97, s86, s53\n"
"s_lshr_b32 s53, s91, 1\n"
"s_lshl_b32 s53, s53, 7\n"
"s_add_u32 s97, s97, s53\n"
"v_lshrrev_b32 v4, 3, v131\n"
"v_and_b32 v5, 7, v131\n"
"v_lshrrev_b32 v6, 4, v131\n"
"v_lshlrev_b32 v4, 15, v4\n"
"v_and_b32 v7, 1, v6\n"
"v_lshlrev_b32 v7, 2, v7\n"
"v_xor_b32 v7, v7, v5\n"
"v_lshl_add_u32 v130, v7, 4, v4\n"
"s_mov_b32 s42, 4194304\n"
"s_mov_b32 s43, 0x20000\n"
"s_and_b32 s53, s92, 1\n"
"s_lshl_b32 s53, s53, 2\n"
"v_or_b32 v7, s53, v6\n"
"v_xor_b32 v7, v7, v5\n"
"v_lshl_add_u32 v2, v7, 4, v4\n"
"s_lshr_b32 s53, s92, 1\n"
"s_lshl_b32 s53, s53, 2\n"
"v_or_b32 v7, s53, v6\n"
"v_lshlrev_b32 v7, 1, v7\n"
"v_and_b32 v5, 15, v131\n"
"v_xor_b32 v7, v7, v5\n"
"v_mul_u32_u24 v6, 7168, v6\n"
"v_lshl_add_u32 v3, v7, 4, v6\n"
"s_mul_i32 s87, s92, 28672\n"
"s_lshl_b32 s53, s91, 8\n"
"s_add_u32 s87, s87, s53\n"
"v_lshlrev_b32 v131, 4, v131\n"
"s_add_u32 s52, s85, 0\n"
"s_and_b32 s52, s52, 3\n"
"s_lshl_b32 s53, s52, 12\n"
"s_add_u32 s76, s53, s98\n"
"s_mul_i32 s52, s52, 114688\n"
"s_add_u32 s72, s52, s87\n"
"s_add_u32 s52, s84, 0\n"
"s_and_b32 s52, s52, 7\n"
"s_lshl_b32 s53, s52, 12\n"
"s_add_u32 s64, s53, s98\n"
"s_and_b32 s53, s52, 3\n"
"s_lshl_b32 s53, s53, 20\n"
"s_add_u32 s53, s53, s86\n"
"s_lshr_b32 s52, s52, 2\n"
"s_lshl_b32 s52, s52, 11\n"
"s_add_u32 s56, s52, s53\n"
"s_add_u32 s52, s84, 1\n"
"s_and_b32 s52, s52, 7\n"
"s_lshl_b32 s53, s52, 12\n"
"s_add_u32 s65, s53, s98\n"
"s_and_b32 s53, s52, 3\n"
"s_lshl_b32 s53, s53, 20\n"
"s_add_u32 s53, s53, s86\n"
"s_lshr_b32 s52, s52, 2\n"
"s_lshl_b32 s52, s52, 11\n"
"s_add_u32 s57, s52, s53\n"
"s_waitcnt lgkmcnt(0)\n"
"s_add_u32 m0, s98, 131072\n"
"s_add_u32 s99, s97, 0\n"
"buffer_load_dwordx4 v130, s[40:43], s99 offen lds\n"
"s_add_u32 m0, s98, 135168\n"
"s_add_u32 s99, s97, 1048576\n"
"buffer_load_dwordx4 v130, s[40:43], s99 offen lds\n"
"s_add_u32 m0, s98, 139264\n"
"s_add_u32 s99, s97, 2097152\n"
"buffer_load_dwordx4 v130, s[40:43], s99 offen lds\n"
"s_add_u32 m0, s98, 143360\n"
"s_add_u32 s99, s97, 3145728\n"
"buffer_load_dwordx4 v130, s[40:43], s99 offen lds\n"
"s_mov_b64 s[48:49], s[46:47]\n"
"s_mov_b32 s46, 7340032\n"
"s_mov_b32 s47, 0x20000\n"
"s_mov_b32 s50, 7340032\n"
"s_mov_b32 s51, 0x20000\n"
"s_add_u32 m0, s76, 65536\n"
"s_nop 0\n"
"buffer_load_dwordx4 v3, s[44:47], s72 offen lds\n"
"s_add_u32 m0, s76, 81920\n"
"s_nop 0\n"
"buffer_load_dwordx4 v3, s[48:51], s72 offen lds\n"
"s_mov_b32 m0, s64\n"
"s_nop 0\n"
"buffer_load_dwordx4 v2, s[40:43], s56 offen lds\n"
"s_mov_b32 m0, s65\n"
"s_nop 0\n"
"buffer_load_dwordx4 v2, s[40:43], s57 offen lds\n"
"s_mov_b64 s[28:29], s[34:35]\n"
"s_mov_b32 s30, 589824\n"
"s_mov_b32 s31, 0x20000\n"
"s_mov_b32 s34, 2048\n"
"s_mov_b32 s35, 0x20000\n"
"s_lshr_b32 s88, s91, 1\n"
"s_lshl_b32 s96, s88, 7\n"
"s_add_u32 s24, s28, 1048576\n"
"s_addc_u32 s25, s29, 0\n"
"s_mov_b32 s26, 4194304\n"
"s_mov_b32 s27, 0x20000\n"
"s_load_dwordx2 s[36:37], s[28:29], 0x0\n"
"v_and_b32 v251, 0x300, v131\n"
"v_lshrrev_b32 v251, 4, v251\n"
"buffer_load_dwordx4 v[112:115], v251, s[32:35], s96 offen offset:0\n"
"buffer_load_dwordx4 v[116:119], v251, s[32:35], s96 offen offset:64\n"
"s_sub_u32 s83, s19, 32\n"
"s_lshr_b32 s99, s83, 3\n"
"s_lshl_b32 s99, s99, 2\n"
"s_add_u32 s99, s99, s92\n"
"s_getpc_b64 s[94:95]\n"
".Lpfg1:\n"
"s_lshl_b32 s99, s99, 8\n"
"s_min_u32 s99, s99, .Lg2end-.Lpfg1-132\n"
"v_min_u32 v14, 16, v131\n"
"v_lshlrev_b32 v14, 3, v14\n"
"v_add_u32 v14, s99, v14\n"
"global_load_dword a190, v14, s[94:95]\n"
"s_add_u32 s52, s84, 2\n"
"s_and_b32 s52, s52, 7\n"
"s_lshl_b32 s53, s52, 12\n"
"s_add_u32 s66, s53, s98\n"
"s_and_b32 s53, s52, 3\n"
"s_lshl_b32 s53, s53, 20\n"
"s_add_u32 s53, s53, s86\n"
"s_lshr_b32 s52, s52, 2\n"
"s_lshl_b32 s52, s52, 11\n"
"s_add_u32 s58, s52, s53\n"
"s_add_u32 s52, s84, 3\n"
"s_and_b32 s52, s52, 7\n"
"s_lshl_b32 s53, s52, 12\n"
"s_add_u32 s67, s53, s98\n"
"s_and_b32 s53, s52, 3\n"
"s_lshl_b32 s53, s53, 20\n"
"s_add_u32 s53, s53, s86\n"
"s_lshr_b32 s52, s52, 2\n"
"s_lshl_b32 s52, s52, 11\n"
"s_add_u32 s59, s52, s53\n"
"s_add_u32 s52, s84, 4\n"
"s_and_b32 s52, s52, 7\n"
"s_lshl_b32 s53, s52, 12\n"
"s_add_u32 s68, s53, s98\n"
"s_and_b32 s53, s52, 3\n"
"s_lshl_b32 s53, s53, 20\n"
"s_add_u32 s53, s53, s86\n"
"s_lshr_b32 s52, s52, 2\n"
"s_lshl_b32 s52, s52, 11\n"
"s_add_u32 s60, s52, s53\n"
"s_add_u32 s52, s84, 5\n"
"s_and_b32 s52, s52, 7\n"
"s_lshl_b32 s53, s52, 12\n"
"s_add_u32 s69, s53, s98\n"
"s_and_b32 s53, s52, 3\n"
"s_lshl_b32 s53, s53, 20\n"
"s_add_u32 s53, s53, s86\n"
"s_lshr_b32 s52, s52, 2\n"
"s_lshl_b32 s52, s52, 11\n"
"s_add_u32 s61, s52, s53\n"
"s_add_u32 s52, s84, 6\n"
"s_and_b32 s52, s52, 7\n"
"s_lshl_b32 s53, s52, 12\n"
"s_add_u32 s70, s53, s98\n"
"s_and_b32 s53, s52, 3\n"
"s_lshl_b32 s53, s53, 20\n"
"s_add_u32 s53, s53, s86\n"
"s_lshr_b32 s52, s52, 2\n"
"s_lshl_b32 s52, s52, 11\n"
"s_add_u32 s62, s52, s53\n"
"s_add_u32 s52, s84, 7\n"
"s_and_b32 s52, s52, 7\n"
"s_lshl_b32 s53, s52, 12\n"
"s_add_u32 s71, s53, s98\n"
"s_and_b32 s53, s52, 3\n"
"s_lshl_b32 s53, s53, 20\n"
"s_add_u32 s53, s53, s86\n"
"s_lshr_b32 s52, s52, 2\n"
"s_lshl_b32 s52, s52, 11\n"
"s_add_u32 s63, s52, s53\n"
"s_add_u32 s52, s85, 1\n"
"s_and_b32 s52, s52, 3\n"
"s_lshl_b32 s53, s52, 12\n"
"s_add_u32 s77, s53, s98\n"
"s_mul_i32 s52, s52, 114688\n"
"s_add_u32 s73, s52, s87\n"
"s_add_u32 s52, s85, 2\n"
"s_and_b32 s52, s52, 3\n"
"s_lshl_b32 s53, s52, 12\n"
"s_add_u32 s78, s53, s98\n"
"s_mul_i32 s52, s52, 114688\n"
"s_add_u32 s74, s52, s87\n"
"s_add_u32 s52, s85, 3\n"
"s_and_b32 s52, s52, 3\n"
"s_lshl_b32 s53, s52, 12\n"
"s_add_u32 s79, s53, s98\n"
"s_mul_i32 s52, s52, 114688\n"
"s_add_u32 s75, s52, s87\n"
"s_and_b32 s52, s91, 1\n"
"s_lshl_b32 s53, s90, 1\n"
"s_add_u32 s52, s52, s53\n"
"s_lshl_b32 s38, s52, 12\n"
"s_add_u32 s38, s38, s96\n"
"s_lshl_b32 s54, s98, 10\n"
"s_add_u32 s38, s38, s54\n"
"s_lshl_b32 s39, s52, 9\n"
"s_lshl_b32 s54, s96, 6\n"
"s_add_u32 s39, s39, s54\n"
"s_lshr_b32 s54, s98, 3\n"
"s_add_u32 s39, s39, s54\n"
"s_add_u32 s39, s39, 65536\n"
"s_lshr_b32 s52, s92, 1\n"
"s_add_u32 s53, s53, s52\n"
"s_and_b32 s52, s92, 1\n"
"s_lshl_b32 s54, s91, 1\n"
"s_mul_i32 s80, s53, 56\n"
"s_add_u32 s54, s54, s52\n"
"s_add_u32 s80, s80, s54\n"
"s_lshl_b32 s80, s80, 14\n"
"s_lshl_b32 s81, s19, 7\n"
"s_add_u32 s81, s81, 4096\n"
"s_add_u32 s55, s54, 4\n"
"s_cmp_gt_u32 s91, 13\n"
"s_cselect_b32 s54, s55, s54\n"
"s_lshl_b32 s53, s53, 6\n"
"s_add_u32 s82, s53, s54\n"
"s_lshl_b32 s82, s82, 7\n"
"s_add_u32 s82, s82, 327680\n"
"s_sub_u32 s83, s19, 32\n"
"s_lshl_b32 s83, s83, 2\n"
"s_add_u32 s83, s83, s92\n"
"v_lshrrev_b32 v168, 4, v131\n"
"v_and_b32 v170, 31, v168\n"
"s_lshr_b32 s99, s98, 5\n"
"v_add_u32 v176, s99, v170\n"
"v_cmp_eq_u32 vcc, 1, v176\n"
"v_lshrrev_b32 v177, 1, v176\n"
"v_and_b32 v178, 1, v176\n"
"v_lshrrev_b32 v169, 5, v168\n"
"v_cndmask_b32_e64 v177, v177, 64, vcc\n"
"v_cndmask_b32_e64 v178, v178, 0, vcc\n"
"v_cvt_f32_u32 v179, v169\n"
"v_cvt_f32_u32 v177, v177\n"
"v_cvt_f32_u32 v178, v178\n"
"v_mul_f32 v128, 0x3c000000, v177\n"
"v_mul_f32 v178, 0x3e800000, v178\n"
"v_mul_f32 v179, 0x41000000, v179\n"
"v_fma_f32 v129, v179, v128, v178\n"
"v_bfe_u32 v125, v168, 1, 1\n"
"s_mov_b32 s95, 0x80008000\n"
"v_mul_lo_u32 v125, v125, s95\n"
"v_mov_b32 v180, v129\n"
"v_fmamk_f32 v181, v128, 0x3f800000, v129\n"
"v_fmamk_f32 v182, v128, 0x40000000, v129\n"
"v_fmamk_f32 v183, v128, 0x40400000, v129\n"
"v_fmamk_f32 v184, v128, 0x40800000, v129\n"
"v_fmamk_f32 v185, v128, 0x40a00000, v129\n"
"v_fmamk_f32 v186, v128, 0x40c00000, v129\n"
"v_fmamk_f32 v187, v128, 0x40e00000, v129\n"
"v_fmamk_f32 v188, v128, 0x41800000, v129\n"
"v_fmamk_f32 v189, v128, 0x41880000, v129\n"
"v_fmamk_f32 v190, v128, 0x41900000, v129\n"
"v_fmamk_f32 v191, v128, 0x41980000, v129\n"
"v_fmamk_f32 v192, v128, 0x41a00000, v129\n"
"v_fmamk_f32 v193, v128, 0x41a80000, v129\n"
"v_fmamk_f32 v194, v128, 0x41b00000, v129\n"
"v_fmamk_f32 v195, v128, 0x41b80000, v129\n"
"s_lshl_b32 s99, s98, 2\n"
"v_and_b32 v172, 15, v168\n"
"v_cos_f32 v180, v180\n"
"v_cos_f32 v181, v181\n"
"v_cos_f32 v182, v182\n"
"v_cos_f32 v183, v183\n"
"v_cos_f32 v184, v184\n"
"v_cos_f32 v185, v185\n"
"v_cos_f32 v186, v186\n"
"v_cos_f32 v187, v187\n"
"v_cos_f32 v188, v188\n"
"v_cos_f32 v189, v189\n"
"v_cos_f32 v190, v190\n"
"v_cos_f32 v191, v191\n"
"v_cos_f32 v192, v192\n"
"v_cos_f32 v193, v193\n"
"v_cos_f32 v194, v194\n"
"v_cos_f32 v195, v195\n"
"s_add_u32 s95, s99, 0x20000\n"
"v_lshrrev_b32 v171, 4, v168\n"
"v_lshrrev_b32 v173, 2, v172\n"
"v_bfe_u32 v174, v168, 1, 1\n"
"v_bfe_u32 v175, v168, 4, 1\n"
"s_add_u32 s94, s99, 0x24000\n"
"v_and_b32 v204, 3, v168\n"
"v_lshl_or_b32 v174, v175, 1, v174\n"
"v_lshrrev_b32 v197, 1, v173\n"
"s_lshr_b32 s93, s92, 1\n"
"v_lshl_add_u32 v203, v171, 3, v173\n"
"v_lshlrev_b32 v204, 3, v204\n"
"v_cvt_pk_bf16_f32 v136, v180, v181\n"
"v_cvt_pk_bf16_f32 v137, v182, v183\n"
"v_cvt_pk_bf16_f32 v138, v184, v185\n"
"v_cvt_pk_bf16_f32 v139, v186, v187\n"
"v_cvt_pk_bf16_f32 v140, v188, v189\n"
"v_cvt_pk_bf16_f32 v141, v190, v191\n"
"v_cvt_pk_bf16_f32 v142, v192, v193\n"
"v_cvt_pk_bf16_f32 v143, v194, v195\n"
"v_and_b32 v175, 1, v168\n"
"v_lshlrev_b32 v197, 2, v197\n"
"v_bfe_u32 v198, v168, 1, 1\n"
"v_and_b32 v199, 7, v168\n"
"s_lshl_b32 s93, s93, 14\n"
"v_lshl_add_u32 v203, v203, 8, v204\n"
"v_fmamk_f32 v180, v128, 0x42000000, v129\n"
"v_fmamk_f32 v181, v128, 0x42040000, v129\n"
"v_fmamk_f32 v182, v128, 0x42080000, v129\n"
"v_fmamk_f32 v183, v128, 0x420c0000, v129\n"
"v_fmamk_f32 v184, v128, 0x42100000, v129\n"
"v_fmamk_f32 v185, v128, 0x42140000, v129\n"
"v_fmamk_f32 v186, v128, 0x42180000, v129\n"
"v_fmamk_f32 v187, v128, 0x421c0000, v129\n"
"v_fmamk_f32 v188, v128, 0x42400000, v129\n"
"v_fmamk_f32 v189, v128, 0x42440000, v129\n"
"v_fmamk_f32 v190, v128, 0x42480000, v129\n"
"v_fmamk_f32 v191, v128, 0x424c0000, v129\n"
"v_fmamk_f32 v192, v128, 0x42500000, v129\n"
"v_fmamk_f32 v193, v128, 0x42540000, v129\n"
"v_fmamk_f32 v194, v128, 0x42580000, v129\n"
"v_fmamk_f32 v195, v128, 0x425c0000, v129\n"
"v_lshlrev_b32 v175, 3, v175\n"
"v_xor_b32 v197, v197, v174\n"
"v_lshlrev_b32 v198, 2, v198\n"
"v_lshl_add_u32 v200, v199, 4, s94\n"
"v_lshrrev_b32 v201, 3, v168\n"
"v_lshrrev_b32 v202, 1, v172\n"
"v_and_b32 v204, 1, v171\n"
"s_and_b32 s89, s92, 1\n"
"v_cos_f32 v180, v180\n"
"v_cos_f32 v181, v181\n"
"v_cos_f32 v182, v182\n"
"v_cos_f32 v183, v183\n"
"v_cos_f32 v184, v184\n"
"v_cos_f32 v185, v185\n"
"v_cos_f32 v186, v186\n"
"v_cos_f32 v187, v187\n"
"v_cos_f32 v188, v188\n"
"v_cos_f32 v189, v189\n"
"v_cos_f32 v190, v190\n"
"v_cos_f32 v191, v191\n"
"v_cos_f32 v192, v192\n"
"v_cos_f32 v193, v193\n"
"v_cos_f32 v194, v194\n"
"v_cos_f32 v195, v195\n"
"v_lshl_add_u32 v196, v169, 3, v173\n"
"v_lshl_add_u32 v197, v197, 4, v175\n"
"v_xor_b32 v198, v198, v171\n"
"v_xor_b32 v199, v199, v201\n"
"v_xor_b32 v202, v202, v171\n"
"v_lshl_or_b32 v204, v204, 2, v173\n"
"s_lshl_b32 s89, s89, 2\n"
"v_lshl_add_u32 v197, v196, 7, v197\n"
"v_lshl_add_u32 v198, v198, 4, s95\n"
"v_lshlrev_b32 v199, 4, v199\n"
"v_and_b32 v201, 1, v201\n"
"v_lshl_add_u32 v202, v202, 4, s93\n"
"v_xor_b32 v204, s89, v204\n"
"v_cvt_pk_bf16_f32 v144, v180, v181\n"
"v_cvt_pk_bf16_f32 v145, v182, v183\n"
"v_cvt_pk_bf16_f32 v146, v184, v185\n"
"v_cvt_pk_bf16_f32 v147, v186, v187\n"
"v_cvt_pk_bf16_f32 v148, v188, v189\n"
"v_cvt_pk_bf16_f32 v149, v190, v191\n"
"v_cvt_pk_bf16_f32 v150, v192, v193\n"
"v_cvt_pk_bf16_f32 v151, v194, v195\n"
"v_add_u32 v120, 0x20000, v197\n"
"v_lshl_add_u32 v122, v172, 7, v198\n"
"v_lshl_add_u32 v200, v169, 3, v200\n"
"v_lshl_add_u32 v199, v201, 11, v199\n"
"v_lshl_add_u32 v4, v172, 7, v202\n"
"v_add_u32 v203, 0x10000, v203\n"
"v_xor_b32 v205, 0, v204\n"
"v_xor_b32 v206, 1, v204\n"
"v_xor_b32 v207, 2, v204\n"
"v_xor_b32 v208, 3, v204\n"
"v_xor_b32 v152, v125, v136\n"
"v_xor_b32 v153, v125, v137\n"
"v_xor_b32 v154, v125, v138\n"
"v_xor_b32 v155, v125, v139\n"
"v_xor_b32 v156, v125, v140\n"
"v_xor_b32 v157, v125, v141\n"
"v_xor_b32 v158, v125, v142\n"
"v_xor_b32 v159, v125, v143\n"
"v_xor_b32 v160, v125, v144\n"
"v_xor_b32 v161, v125, v145\n"
"v_xor_b32 v162, v125, v146\n"
"v_xor_b32 v163, v125, v147\n"
"v_xor_b32 v164, v125, v148\n"
"v_xor_b32 v165, v125, v149\n"
"v_xor_b32 v166, v125, v150\n"
"v_xor_b32 v167, v125, v151\n"
"v_xor_b32 v121, 64, v120\n"
"v_xor_b32 v123, 64, v122\n"
"v_lshl_add_u32 v248, v170, 7, v200\n"
"v_add_u32 v249, s94, v131\n"
"v_lshl_add_u32 v250, v171, 16, v199\n"
"v_xor_b32 v5, 64, v4\n"
"v_mov_b32 v10, v131\n"
"v_lshl_add_u32 v6, v205, 5, v203\n"
"v_lshl_add_u32 v7, v206, 5, v203\n"
"v_lshl_add_u32 v8, v207, 5, v203\n"
"v_lshl_add_u32 v9, v208, 5, v203\n"
"s_nop 1\n"
"s_waitcnt vmcnt(7) lgkmcnt(0)\n"
"s_barrier\n"
"s_mul_i32 s99, s37, 0x7f4a7c15\n"
"s_mul_i32 s97, s36, 0x9e3779b9\n"
"s_add_u32 s99, s99, s97\n"
"s_mul_hi_u32 s97, s36, 0x7f4a7c15\n"
"s_add_u32 s99, s99, s97\n"
"s_mul_i32 s36, s36, 0x7f4a7c15\n"
"s_add_u32 s36, s36, 0xd192ed03\n"
"s_addc_u32 s37, s99, 0xd1b54a32\n"
"s_add_u32 m0, s77, 65536\n"
"s_nop 0\n"
"buffer_load_dwordx4 v3, s[44:47], s73 offen lds\n"
"s_add_u32 m0, s77, 81920\n"
"s_nop 0\n"
"buffer_load_dwordx4 v3, s[48:51], s73 offen lds\n"
"s_mov_b32 m0, s66\n"
"ds_read_b64_tr_b16 a[64:65], v120 offset:0\n"
"ds_read_b64_tr_b16 a[66:67], v120 offset:512\n"
"ds_read_b64_tr_b16 a[68:69], v121 offset:0\n"
"ds_read_b64_tr_b16 a[70:71], v121 offset:512\n"
"ds_read_b64_tr_b16 a[72:73], v120 offset:2048\n"
"ds_read_b64_tr_b16 a[74:75], v120 offset:2560\n"
"ds_read_b64_tr_b16 a[76:77], v121 offset:2048\n"
"ds_read_b64_tr_b16 a[78:79], v121 offset:2560\n"
"ds_read_b64_tr_b16 a[80:81], v120 offset:4096\n"
"buffer_load_dwordx4 v2, s[40:43], s58 offen lds\n"
"s_mov_b32 m0, s67\n"
"ds_read_b64_tr_b16 a[82:83], v120 offset:4608\n"
"ds_read_b64_tr_b16 a[84:85], v121 offset:4096\n"
"ds_read_b64_tr_b16 a[86:87], v121 offset:4608\n"
"ds_read_b64_tr_b16 a[88:89], v120 offset:6144\n"
"ds_read_b64_tr_b16 a[90:91], v120 offset:6656\n"
"ds_read_b64_tr_b16 a[92:93], v121 offset:6144\n"
"ds_read_b64_tr_b16 a[94:95], v121 offset:6656\n"
"ds_read_b64_tr_b16 a[96:97], v120 offset:8192\n"
"v_xor_b32 v72, 0, v248\n"
"v_xor_b32 v73, 16, v248\n"
"v_xor_b32 v74, 32, v248\n"
"v_xor_b32 v75, 48, v248\n"
"buffer_load_dwordx4 v2, s[40:43], s59 offen lds\n"
"s_add_u32 m0, s78, 65536\n"
"ds_read_b64_tr_b16 a[98:99], v120 offset:8704\n"
"ds_read_b64_tr_b16 a[100:101], v121 offset:8192\n"
"ds_read_b64_tr_b16 a[102:103], v121 offset:8704\n"
"ds_read_b64_tr_b16 a[104:105], v120 offset:10240\n"
"ds_read_b64_tr_b16 a[106:107], v120 offset:10752\n"
"ds_read_b64_tr_b16 a[108:109], v121 offset:10240\n"
"ds_read_b64_tr_b16 a[110:111], v121 offset:10752\n"
"s_waitcnt lgkmcnt(15)\n"
"v_mfma_f32_32x32x16_bf16 v[16:31], a[64:67], v[136:139], 0\n"
"ds_read_b64_tr_b16 a[112:113], v120 offset:12288\n"
"ds_read_b64_tr_b16 a[114:115], v120 offset:12800\n"
"ds_read_b64_tr_b16 a[116:117], v121 offset:12288\n"
"buffer_load_dwordx4 v3, s[44:47], s74 offen lds\n"
"s_add_u32 m0, s78, 81920\n"
"v_mfma_f32_32x32x16_bf16 v[32:47], a[68:71], v[136:139], 0\n"
"ds_read_b64_tr_b16 a[118:119], v121 offset:12800\n"
"ds_read_b64_tr_b16 a[120:121], v120 offset:14336\n"
"ds_read_b64_tr_b16 a[122:123], v120 offset:14848\n"
"ds_read_b64_tr_b16 a[124:125], v121 offset:14336\n"
"ds_read_b64_tr_b16 a[126:127], v121 offset:14848\n"
"ds_read_b128 v[96:99], v122 offset:0\n"
"v_mfma_f32_32x32x16_bf16 v[16:31], a[72:75], v[140:143], v[16:31]\n"
"v_xor_b32 v76, 64, v248\n"
"v_xor_b32 v77, 80, v248\n"
"buffer_load_dwordx4 v3, s[48:51], s74 offen lds\n"
"s_mov_b32 m0, s68\n"
"v_mfma_f32_32x32x16_bf16 v[32:47], a[76:79], v[140:143], v[32:47]\n"
"ds_read_b128 v[104:107], v122 offset:2048\n"
"v_xor_b32 v78, 96, v248\n"
"v_xor_b32 v79, 112, v248\n"
"ds_read_b128 v[100:103], v123 offset:0\n"
"s_waitcnt lgkmcnt(15)\n"
"v_mfma_f32_32x32x16_bf16 v[16:31], a[80:83], v[144:147], v[16:31]\n"
"buffer_load_dwordx4 v2, s[40:43], s60 offen lds\n"
"s_mov_b32 m0, s69\n"
"v_mfma_f32_32x32x16_bf16 v[32:47], a[84:87], v[144:147], v[32:47]\n"
"ds_read_b128 v[108:111], v123 offset:2048\n"
"v_mfma_f32_32x32x16_bf16 v[16:31], a[88:91], v[148:151], v[16:31]\n"
"buffer_load_dwordx4 v2, s[40:43], s61 offen lds\n"
"s_add_u32 m0, s79, 65536\n"
"v_mfma_f32_32x32x16_bf16 v[32:47], a[92:95], v[148:151], v[32:47]\n"
"v_mfma_f32_32x32x16_bf16 v[16:31], a[96:99], v[152:155], v[16:31]\n"
"buffer_load_dwordx4 v3, s[44:47], s75 offen lds\n"
"s_add_u32 m0, s79, 81920\n"
"v_mfma_f32_32x32x16_bf16 v[32:47], a[100:103], v[152:155], v[32:47]\n"
"s_waitcnt lgkmcnt(14)\n"
"v_mfma_f32_32x32x16_bf16 v[16:31], a[104:107], v[156:159], v[16:31]\n"
"buffer_load_dwordx4 v3, s[48:51], s75 offen lds\n"
"s_mov_b32 m0, s70\n"
"s_waitcnt lgkmcnt(12)\n"
"v_mfma_f32_32x32x16_bf16 v[32:47], a[108:111], v[156:159], v[32:47]\n"
"s_waitcnt lgkmcnt(10)\n"
"v_mfma_f32_32x32x16_bf16 v[16:31], a[112:115], v[160:163], v[16:31]\n"
"buffer_load_dwordx4 v2, s[40:43], s62 offen lds\n"
"s_mov_b32 m0, s71\n"
"s_waitcnt lgkmcnt(8)\n"
"v_mfma_f32_32x32x16_bf16 v[32:47], a[116:119], v[160:163], v[32:47]\n"
"s_waitcnt lgkmcnt(6)\n"
"v_mfma_f32_32x32x16_bf16 v[16:31], a[120:123], v[164:167], v[16:31]\n"
"buffer_load_dwordx4 v2, s[40:43], s63 offen lds\n"
"s_add_u32 m0, s76, 98304\n"
"s_add_u32 s99, s72, 458752\n"
"s_waitcnt lgkmcnt(4)\n"
"v_mfma_f32_32x32x16_bf16 v[32:47], a[124:127], v[164:167], v[32:47]\n"
"s_nop 6\n"
"v_cvt_pk_bf16_f32 v56, v16, v17\n"
"v_cvt_pk_bf16_f32 v57, v18, v19\n"
"v_cvt_pk_bf16_f32 v58, v20, v21\n"
"s_waitcnt vmcnt(14) lgkmcnt(3)\n"
"v_mfma_f32_16x16x32_bf16 v[240:243], v[96:99], v[112:115], 0\n"
"ds_write_b64 v72, v[56:57]\n"
"buffer_load_dwordx4 v3, s[44:47], s99 offen lds\n"
"s_add_u32 m0, s76, 114688\n"
"s_add_u32 s99, s72, 458752\n"
"s_waitcnt lgkmcnt(3)\n"
"v_mfma_f32_16x16x32_bf16 v[244:247], v[104:107], v[112:115], 0\n"
"v_cvt_pk_bf16_f32 v59, v22, v23\n"
"v_cvt_pk_bf16_f32 v60, v24, v25\n"
"s_waitcnt vmcnt(14) lgkmcnt(2)\n"
"v_mfma_f32_16x16x32_bf16 v[240:243], v[100:103], v[116:119], v[240:243]\n"
"ds_write_b64 v73, v[58:59]\n"
"s_waitcnt lgkmcnt(2)\n"
"v_mfma_f32_16x16x32_bf16 v[244:247], v[108:111], v[116:119], v[244:247]\n"
"buffer_load_dwordx4 v3, s[48:51], s99 offen lds\n"
"s_add_u32 m0, s64, 32768\n"
"s_add_u32 s99, s56, 128\n"
"v_cvt_pk_bf16_f32 v61, v26, v27\n"
"v_cvt_pk_bf16_f32 v62, v28, v29\n"
"v_cvt_pk_bf16_f32 v63, v30, v31\n"
"v_cvt_pk_bf16_f32 v64, v32, v33\n"
"v_cvt_pk_bf16_f32 v65, v34, v35\n"
"v_cvt_pk_bf16_f32 v66, v36, v37\n"
"v_cvt_pk_bf16_f32 v67, v38, v39\n"
"v_cvt_pk_bf16_f32 v68, v40, v41\n"
"v_cvt_pk_bf16_f32 v69, v42, v43\n"
"v_cvt_pk_bf16_f32 v70, v44, v45\n"
"v_cvt_pk_bf16_f32 v71, v46, v47\n"
"buffer_load_dwordx4 v2, s[40:43], s99 offen lds\n"
"s_add_u32 m0, s65, 32768\n"
"s_add_u32 s99, s57, 128\n"
"ds_write_b64 v74, v[60:61]\n"
"ds_write_b64 v75, v[62:63]\n"
"ds_write_b64 v76, v[64:65]\n"
"ds_write_b64 v77, v[66:67]\n"
"buffer_load_dwordx4 v2, s[40:43], s99 offen lds\n"
"s_add_u32 m0, s77, 98304\n"
"s_add_u32 s99, s73, 458752\n"
"ds_write_b64 v78, v[68:69]\n"
"ds_write_b64 v79, v[70:71]\n"
"buffer_load_dwordx4 v3, s[44:47], s99 offen lds\n"
"s_add_u32 m0, s77, 114688\n"
"s_add_u32 s99, s73, 458752\n"
"ds_read_b128 v[224:227], v249 offset:0\n"
"ds_read_b128 v[228:231], v249 offset:1024\n"
"ds_read_b128 v[232:235], v249 offset:2048\n"
"buffer_load_dwordx4 v3, s[48:51], s99 offen lds\n"
"s_add_u32 m0, s66, 32768\n"
"s_add_u32 s99, s58, 128\n"
"ds_read_b128 v[236:239], v249 offset:3072\n"
"buffer_load_dwordx4 v2, s[40:43], s99 offen lds\n"
"s_add_u32 m0, s67, 32768\n"
"s_add_u32 s99, s59, 128\n"
"buffer_load_dwordx4 v2, s[40:43], s99 offen lds\n"
"s_mov_b32 s96, 0x8000\n"
"s_mov_b32 s89, 0\n"
"s_mov_b32 s84, 0\n"
"s_mov_b32 s90, 0xbfb8aa3b\n"
"s_mov_b32 s91, 0xbfb8aa3b\n"
"s_mov_b32 s92, 1.0\n"
"s_mov_b32 s93, 1.0\n"
"v_mov_b32 v12, s36\n"
"v_mov_b32 v13, s37\n"
"s_waitcnt lgkmcnt(0)\n"
"s_add_u32 s94, s38, 0\n"
"buffer_store_dwordx4 v[224:227], v250, s[24:27], s94 offen sc1\n"
"s_add_u32 s94, s38, 262144\n"
"buffer_store_dwordx4 v[228:231], v250, s[24:27], s94 offen sc1\n"
"s_add_u32 s94, s38, 524288\n"
"buffer_store_dwordx4 v[232:235], v250, s[24:27], s94 offen sc1\n"
"s_add_u32 s94, s38, 786432\n"
"buffer_store_dwordx4 v[236:239], v250, s[24:27], s94 offen sc1\n"
"s_mov_b32 exec_lo, 0x10001\n"
"s_mov_b32 exec_hi, 0x10001\n"
"buffer_store_dwordx4 v[240:243], v251, s[28:31], s39 offen offset:0 sc1\n"
"s_mov_b64 exec, -1\n"
"s_mov_b32 exec_lo, 0x10001\n"
"s_mov_b32 exec_hi, 0x10001\n"
"buffer_store_dwordx4 v[244:247], v251, s[28:31], s39 offen offset:64 sc1\n"
"s_mov_b64 exec, -1\n"
"s_waitcnt vmcnt(14) lgkmcnt(0)\n"
"s_barrier\n"
"ds_read_b64_tr_b16 a[0:1], v6 offset:0\n"
"ds_read_b64_tr_b16 a[2:3], v6 offset:1024\n"
"ds_read_b64_tr_b16 a[4:5], v7 offset:0\n"
"ds_read_b64_tr_b16 a[6:7], v7 offset:1024\n"
"ds_read_b128 a[32:35], v4 offset:0\n"
"ds_read_b128 a[36:39], v4 offset:2048\n"
"ds_read_b128 a[40:43], v4 offset:4096\n"
"ds_read_b128 a[44:47], v4 offset:6144\n"
"ds_read_b128 a[48:51], v4 offset:8192\n"
"ds_read_b128 a[52:55], v4 offset:10240\n"
"ds_read_b128 a[56:59], v4 offset:12288\n"
"ds_read_b128 a[60:63], v4 offset:14336\n"
"ds_read_b64_tr_b16 a[8:9], v8 offset:0\n"
"ds_read_b64_tr_b16 a[10:11], v8 offset:1024\n"
"ds_read_b64_tr_b16 a[12:13], v9 offset:0\n"
"ds_read_b64_tr_b16 a[14:15], v9 offset:1024\n"
"ds_read_b64_tr_b16 a[16:17], v6 offset:16384\n"
"ds_read_b64_tr_b16 a[18:19], v6 offset:17408\n"
"ds_read_b64_tr_b16 a[20:21], v7 offset:16384\n"
"ds_read_b64_tr_b16 a[22:23], v7 offset:17408\n"
"ds_read_b64_tr_b16 a[24:25], v8 offset:16384\n"
"ds_read_b64_tr_b16 a[26:27], v8 offset:17408\n"
"ds_read_b64_tr_b16 a[28:29], v9 offset:16384\n"
"ds_read_b64_tr_b16 a[30:31], v9 offset:17408\n"
"s_waitcnt lgkmcnt(15)\n"
"v_mfma_f32_16x16x32_bf16 v[16:19], a[0:3], a[32:35], 0\n"
"ds_read_b64_tr_b16 a[64:65], v6 offset:8192\n"
"s_add_u32 s89, s89, 1\n"
"v_mfma_f32_16x16x32_bf16 v[48:51], a[4:7], a[32:35], 0\n"
"s_add_u32 m0, s78, 98304\n"
"s_add_u32 s99, s74, 458752\n"
"v_mfma_f32_16x16x32_bf16 v[52:55], a[4:7], a[36:39], 0\n"
"ds_read_b64_tr_b16 a[66:67], v6 offset:9216\n"
"buffer_load_dwordx4 v3, s[44:47], s99 offen lds\n"
"v_mfma_f32_16x16x32_bf16 v[20:23], a[0:3], a[36:39], 0\n"
"s_waitcnt lgkmcnt(12)\n"
"s_cmp_eq_u32 s89, 3\n"
"s_cselect_b32 s87, 0xffff0000, s96\n"
"s_cselect_b32 s89, 0, s89\n"
"v_mfma_f32_16x16x32_bf16 v[24:27], a[0:3], a[40:43], 0\n"
"ds_read_b128 a[96:99], v5 offset:0\n"
"s_add_u32 m0, s78, 114688\n"
"s_add_u32 s99, s74, 458752\n"
"v_mfma_f32_16x16x32_bf16 v[56:59], a[4:7], a[40:43], 0\n"
"buffer_load_dwordx4 v3, s[48:51], s99 offen lds\n"
"v_mfma_f32_16x16x32_bf16 v[60:63], a[4:7], a[44:47], 0\n"
"ds_read_b64_tr_b16 a[68:69], v7 offset:8192\n"
"v_mfma_f32_16x16x32_bf16 v[28:31], a[0:3], a[44:47], 0\n"
"s_add_u32 m0, s68, 32768\n"
"s_add_u32 s99, s60, 128\n"
"v_mfma_f32_16x16x32_bf16 v[32:35], a[0:3], a[48:51], 0\n"
"ds_read_b64_tr_b16 a[70:71], v7 offset:9216\n"
"buffer_load_dwordx4 v2, s[40:43], s99 offen lds\n"
"v_mfma_f32_16x16x32_bf16 v[64:67], a[4:7], a[48:51], 0\n"
"v_min_u32 v14, 288, v10\n"
"v_mfma_f32_16x16x32_bf16 v[68:71], a[4:7], a[52:55], 0\n"
"ds_read_b128 a[100:103], v5 offset:2048\n"
"s_add_u32 m0, s69, 32768\n"
"s_add_u32 s99, s61, 128\n"
"v_mfma_f32_16x16x32_bf16 v[36:39], a[0:3], a[52:55], 0\n"
"buffer_load_dwordx4 v2, s[40:43], s99 offen lds\n"
"v_mfma_f32_16x16x32_bf16 v[40:43], a[0:3], a[56:59], 0\n"
"ds_read_b64_tr_b16 a[72:73], v8 offset:8192\n"
"v_mfma_f32_16x16x32_bf16 v[72:75], a[4:7], a[56:59], 0\n"
"s_add_u32 m0, s79, 98304\n"
"s_add_u32 s99, s75, 458752\n"
"v_mfma_f32_16x16x32_bf16 v[76:79], a[4:7], a[60:63], 0\n"
"ds_read_b64_tr_b16 a[74:75], v8 offset:9216\n"
"buffer_load_dwordx4 v3, s[44:47], s99 offen lds\n"
"v_mfma_f32_16x16x32_bf16 v[44:47], a[0:3], a[60:63], 0\n"
"s_waitcnt lgkmcnt(6)\n"
"v_lshlrev_b32 v14, 3, v14\n"
"v_mfma_f32_16x16x32_bf16 v[108:111], a[8:11], a[60:63], 0\n"
"ds_read_b128 a[104:107], v5 offset:4096\n"
"s_add_u32 m0, s79, 114688\n"
"s_add_u32 s99, s75, 458752\n"
"v_mfma_f32_16x16x32_bf16 v[140:143], a[12:15], a[60:63], 0\n"
"buffer_load_dwordx4 v3, s[48:51], s99 offen lds\n"
"v_mfma_f32_16x16x32_bf16 v[136:139], a[12:15], a[56:59], 0\n"
"ds_read_b64_tr_b16 a[76:77], v9 offset:8192\n"
"v_mfma_f32_16x16x32_bf16 v[104:107], a[8:11], a[56:59], 0\n"
"s_add_u32 m0, s70, 32768\n"
"s_add_u32 s99, s62, 128\n"
"v_mfma_f32_16x16x32_bf16 v[100:103], a[8:11], a[52:55], 0\n"
"ds_read_b64_tr_b16 a[78:79], v9 offset:9216\n"
"buffer_load_dwordx4 v2, s[40:43], s99 offen lds\n"
"v_mfma_f32_16x16x32_bf16 v[132:135], a[12:15], a[52:55], 0\n"
"v_mfma_f32_16x16x32_bf16 v[128:131], a[12:15], a[48:51], 0\n"
"ds_read_b128 a[108:111], v5 offset:6144\n"
"s_add_u32 m0, s71, 32768\n"
"s_add_u32 s99, s63, 128\n"
"v_mfma_f32_16x16x32_bf16 v[96:99], a[8:11], a[48:51], 0\n"
"buffer_load_dwordx4 v2, s[40:43], s99 offen lds\n"
"v_mfma_f32_16x16x32_bf16 v[92:95], a[8:11], a[44:47], 0\n"
"ds_read_b64_tr_b16 a[80:81], v6 offset:24576\n"
"v_mfma_f32_16x16x32_bf16 v[124:127], a[12:15], a[44:47], 0\n"
"s_add_u32 m0, s76, 131072\n"
"s_add_u32 s99, s72, 917504\n"
"v_mfma_f32_16x16x32_bf16 v[120:123], a[12:15], a[40:43], 0\n"
"ds_read_b64_tr_b16 a[82:83], v6 offset:25600\n"
"buffer_load_dwordx4 v3, s[44:47], s99 offen lds\n"
"v_mfma_f32_16x16x32_bf16 v[88:91], a[8:11], a[40:43], 0\n"
"v_mfma_f32_16x16x32_bf16 v[84:87], a[8:11], a[36:39], 0\n"
"ds_read_b128 a[112:115], v5 offset:8192\n"
"v_mfma_f32_16x16x32_bf16 v[116:119], a[12:15], a[36:39], 0\n"
"s_add_u32 m0, s76, 147456\n"
"s_add_u32 s99, s72, 917504\n"
"v_mfma_f32_16x16x32_bf16 v[112:115], a[12:15], a[32:35], 0\n"
"ds_read_b64_tr_b16 a[84:85], v7 offset:24576\n"
"buffer_load_dwordx4 v3, s[48:51], s99 offen lds\n"
"v_mfma_f32_16x16x32_bf16 v[80:83], a[8:11], a[32:35], 0\n"
"v_mfma_f32_16x16x32_bf16 v[144:147], a[16:19], a[32:35], 0\n"
"ds_read_b64_tr_b16 a[86:87], v7 offset:25600\n"
"v_mfma_f32_16x16x32_bf16 v[176:179], a[20:23], a[32:35], 0\n"
"s_add_u32 m0, s77, 131072\n"
"s_add_u32 s99, s73, 917504\n"
"v_mfma_f32_16x16x32_bf16 v[180:183], a[20:23], a[36:39], 0\n"
"ds_read_b128 a[116:119], v5 offset:10240\n"
"buffer_load_dwordx4 v3, s[44:47], s99 offen lds\n"
"v_mfma_f32_16x16x32_bf16 v[148:151], a[16:19], a[36:39], 0\n"
"v_mfma_f32_16x16x32_bf16 v[152:155], a[16:19], a[40:43], 0\n"
"ds_read_b64_tr_b16 a[88:89], v8 offset:24576\n"
"v_mfma_f32_16x16x32_bf16 v[184:187], a[20:23], a[40:43], 0\n"
"s_add_u32 m0, s77, 147456\n"
"s_add_u32 s99, s73, 917504\n"
"v_mfma_f32_16x16x32_bf16 v[188:191], a[20:23], a[44:47], 0\n"
"ds_read_b64_tr_b16 a[90:91], v8 offset:25600\n"
"buffer_load_dwordx4 v3, s[48:51], s99 offen lds\n"
"v_mfma_f32_16x16x32_bf16 v[156:159], a[16:19], a[44:47], 0\n"
"v_mfma_f32_16x16x32_bf16 v[160:163], a[16:19], a[48:51], 0\n"
"ds_read_b128 a[120:123], v5 offset:12288\n"
"v_mfma_f32_16x16x32_bf16 v[192:195], a[20:23], a[48:51], 0\n"
"s_add_u32 m0, s78, 131072\n"
"s_add_u32 s99, s74, 917504\n"
"v_mfma_f32_16x16x32_bf16 v[196:199], a[20:23], a[52:55], 0\n"
"ds_read_b64_tr_b16 a[92:93], v9 offset:24576\n"
"buffer_load_dwordx4 v3, s[44:47], s99 offen lds\n"
"v_mfma_f32_16x16x32_bf16 v[164:167], a[16:19], a[52:55], 0\n"
"v_mfma_f32_16x16x32_bf16 v[168:171], a[16:19], a[56:59], 0\n"
"ds_read_b64_tr_b16 a[94:95], v9 offset:25600\n"
"s_mul_i32 s38, s83, 2432\n"
"v_mfma_f32_16x16x32_bf16 v[200:203], a[20:23], a[56:59], 0\n"
"s_add_u32 m0, s78, 147456\n"
"s_add_u32 s99, s74, 917504\n"
"v_mfma_f32_16x16x32_bf16 v[204:207], a[20:23], a[60:63], 0\n"
"ds_read_b128 a[124:127], v5 offset:14336\n"
"buffer_load_dwordx4 v3, s[48:51], s99 offen lds\n"
"v_mfma_f32_16x16x32_bf16 v[172:175], a[16:19], a[60:63], 0\n"
"v_mfma_f32_16x16x32_bf16 v[236:239], a[24:27], a[60:63], 0\n"
"s_mov_b32 s54, 2101248\n"
"v_mfma_f32_16x16x32_bf16 a[140:143], a[28:31], a[60:63], 0\n"
"s_add_u32 m0, s79, 131072\n"
"s_add_u32 s99, s75, 917504\n"
"v_xor_b32 v4, 0x8000, v4\n"
"v_mfma_f32_16x16x32_bf16 a[136:139], a[28:31], a[56:59], 0\n"
"buffer_load_dwordx4 v3, s[44:47], s99 offen lds\n"
"v_mfma_f32_16x16x32_bf16 v[232:235], a[24:27], a[56:59], 0\n"
"v_xor_b32 v5, 0x8000, v5\n"
"v_mfma_f32_16x16x32_bf16 v[228:231], a[24:27], a[52:55], 0\n"
"s_mov_b32 s55, 0x20000\n"
"v_mfma_f32_16x16x32_bf16 a[132:135], a[28:31], a[52:55], 0\n"
"s_add_u32 m0, s79, 147456\n"
"s_add_u32 s99, s75, 917504\n"
"v_add_u32 v6, s87, v6\n"
"v_mfma_f32_16x16x32_bf16 a[128:131], a[28:31], a[48:51], 0\n"
"buffer_load_dwordx4 v3, s[48:51], s99 offen lds\n"
"v_mfma_f32_16x16x32_bf16 v[224:227], a[24:27], a[48:51], 0\n"
"v_add_u32 v7, s87, v7\n"
"v_mfma_f32_16x16x32_bf16 v[220:223], a[24:27], a[44:47], 0\n"
"v_mfma_f32_16x16x32_bf16 v[252:255], a[28:31], a[44:47], 0\n"
"v_add_u32 v8, s87, v8\n"
"v_mfma_f32_16x16x32_bf16 v[248:251], a[28:31], a[40:43], 0\n"
"s_load_dwordx4 s[24:27], s[16:17], 0x8\n"
"s_load_dwordx2 s[32:33], s[16:17], 0x28\n"
"v_mfma_f32_16x16x32_bf16 v[216:219], a[24:27], a[40:43], 0\n"
"v_add_u32 v9, s87, v9\n"
"v_mfma_f32_16x16x32_bf16 v[212:215], a[24:27], a[36:39], 0\n"
"s_add_u32 s40, s40, 256\n"
"s_addc_u32 s41, s41, 0\n"
"v_mfma_f32_16x16x32_bf16 v[244:247], a[28:31], a[36:39], 0\n"
"s_add_u32 s44, s44, 917504\n"
"s_addc_u32 s45, s45, 0\n"
"v_mfma_f32_16x16x32_bf16 v[240:243], a[28:31], a[32:35], 0\n"
"s_add_u32 s48, s48, 917504\n"
"s_addc_u32 s49, s49, 0\n"
"v_mfma_f32_16x16x32_bf16 v[208:211], a[24:27], a[32:35], 0\n"
"s_mov_b32 s85, 0x20000\n"
"s_mov_b32 s86, 0x24000\n"
"s_waitcnt vmcnt(8) lgkmcnt(0)\n"
"s_barrier\n"
"s_mov_b64 s[52:53], s[24:25]\n"
"s_cmp_eq_u32 s98, 0\n"
"s_cbranch_scc0 .Lg1arr\n"
"v_lshrrev_b32 v15, 1, v10\n"
"s_mov_b64 exec, 0xffff\n"
"buffer_store_dwordx2 v[12:13], v15, s[28:31], s81 offen sc1\n"
"s_mov_b64 exec, -1\n"
".Lg1arr:\n"
"s_mov_b32 s88, 13\n"
"s_mov_b32 s39, 0\n"
"s_branch .Lg1loop\n"
".Lg1tch:\n"
"s_cmp_eq_u32 s88, 13\n"
"s_cbranch_scc0 .Lg1tch2\n"
"s_mov_b64 s[52:53], s[26:27]\n"
"s_branch .Lg1tchd\n"
".Lg1tch2:\n"
"s_mov_b64 s[52:53], s[32:33]\n"
"s_mov_b32 s54, 7340032\n"
"s_mul_i32 s38, s83, 8448\n"
"s_mov_b32 s39, 768\n"
"v_min_u32 v14, 640, v14\n"
"s_branch .Lg1tchd\n"
".p2align 7\n"
"s_nop 0\n"
"s_nop 0\n"
"s_nop 0\n"
"s_nop 0\n"
"s_nop 0\n"
"s_nop 0\n"
"s_nop 0\n"
"s_nop 0\n"
"s_nop 0\n"
"s_nop 0\n"
"s_nop 0\n"
"s_nop 0\n"
"s_nop 0\n"
"s_nop 0\n"
"s_nop 0\n"
"s_nop 0\n"
".Lg1loop:\n"
"v_mfma_f32_16x16x32_bf16 v[16:19], a[64:67], a[96:99], v[16:19]\n"
"ds_read_b64_tr_b16 a[0:1], v6 offset:0\n"
"v_mfma_f32_16x16x32_bf16 v[48:51], a[68:71], a[96:99], v[48:51]\n"
"v_mfma_f32_16x16x32_bf16 v[52:55], a[68:71], a[100:103], v[52:55]\n"
"ds_read_b64_tr_b16 a[2:3], v6 offset:1024\n"
"s_add_u32 m0, s84, s64\n"
"v_mfma_f32_16x16x32_bf16 v[20:23], a[64:67], a[100:103], v[20:23]\n"
"buffer_load_dwordx4 v2, s[40:43], s56 offen lds\n"
"v_mfma_f32_16x16x32_bf16 v[24:27], a[64:67], a[104:107], v[24:27]\n"
"ds_read_b128 a[32:35], v4 offset:0\n"
"v_mfma_f32_16x16x32_bf16 v[56:59], a[68:71], a[104:107], v[56:59]\n"
"s_add_u32 s44, s44, 458752\n"
"s_addc_u32 s45, s45, 0\n"
"v_mfma_f32_16x16x32_bf16 v[60:63], a[68:71], a[108:111], v[60:63]\n"
"ds_read_b64_tr_b16 a[4:5], v7 offset:0\n"
"v_mfma_f32_16x16x32_bf16 v[28:31], a[64:67], a[108:111], v[28:31]\n"
"v_mfma_f32_16x16x32_bf16 v[32:35], a[64:67], a[112:115], v[32:35]\n"
"ds_read_b64_tr_b16 a[6:7], v7 offset:1024\n"
"v_mfma_f32_16x16x32_bf16 v[64:67], a[68:71], a[112:115], v[64:67]\n"
"v_mfma_f32_16x16x32_bf16 v[68:71], a[68:71], a[116:119], v[68:71]\n"
"ds_read_b128 a[36:39], v4 offset:2048\n"
"s_add_u32 m0, s84, s65\n"
"v_mfma_f32_16x16x32_bf16 v[36:39], a[64:67], a[116:119], v[36:39]\n"
"buffer_load_dwordx4 v2, s[40:43], s57 offen lds\n"
"v_mfma_f32_16x16x32_bf16 v[40:43], a[64:67], a[120:123], v[40:43]\n"
"ds_read_b64_tr_b16 a[8:9], v8 offset:0\n"
"v_mfma_f32_16x16x32_bf16 v[72:75], a[68:71], a[120:123], v[72:75]\n"
"s_add_u32 s48, s48, 458752\n"
"s_addc_u32 s49, s49, 0\n"
"v_mfma_f32_16x16x32_bf16 v[76:79], a[68:71], a[124:127], v[76:79]\n"
"ds_read_b64_tr_b16 a[10:11], v8 offset:1024\n"
"v_mfma_f32_16x16x32_bf16 v[44:47], a[64:67], a[124:127], v[44:47]\n"
"v_mfma_f32_16x16x32_bf16 v[108:111], a[72:75], a[124:127], v[108:111]\n"
"ds_read_b128 a[40:43], v4 offset:4096\n"
"v_mfma_f32_16x16x32_bf16 v[140:143], a[76:79], a[124:127], v[140:143]\n"
"v_mfma_f32_16x16x32_bf16 v[136:139], a[76:79], a[120:123], v[136:139]\n"
"ds_read_b64_tr_b16 a[12:13], v9 offset:0\n"
"s_add_u32 m0, s84, s66\n"
"v_mfma_f32_16x16x32_bf16 v[104:107], a[72:75], a[120:123], v[104:107]\n"
"buffer_load_dwordx4 v2, s[40:43], s58 offen lds\n"
"v_mfma_f32_16x16x32_bf16 v[100:103], a[72:75], a[116:119], v[100:103]\n"
"ds_read_b64_tr_b16 a[14:15], v9 offset:1024\n"
"v_mfma_f32_16x16x32_bf16 v[132:135], a[76:79], a[116:119], v[132:135]\n"
"s_add_u32 s85, s85, 0x8000\n"
"v_mfma_f32_16x16x32_bf16 v[128:131], a[76:79], a[112:115], v[128:131]\n"
"ds_read_b128 a[44:47], v4 offset:6144\n"
"v_mfma_f32_16x16x32_bf16 v[96:99], a[72:75], a[112:115], v[96:99]\n"
"v_mfma_f32_16x16x32_bf16 v[92:95], a[72:75], a[108:111], v[92:95]\n"
"ds_read_b64_tr_b16 a[16:17], v6 offset:16384\n"
"v_mfma_f32_16x16x32_bf16 v[124:127], a[76:79], a[108:111], v[124:127]\n"
"v_mfma_f32_16x16x32_bf16 v[120:123], a[76:79], a[104:107], v[120:123]\n"
"ds_read_b64_tr_b16 a[18:19], v6 offset:17408\n"
"s_add_u32 m0, s84, s67\n"
"v_mfma_f32_16x16x32_bf16 v[88:91], a[72:75], a[104:107], v[88:91]\n"
"buffer_load_dwordx4 v2, s[40:43], s59 offen lds\n"
"v_mfma_f32_16x16x32_bf16 v[84:87], a[72:75], a[100:103], v[84:87]\n"
"ds_read_b128 a[48:51], v4 offset:8192\n"
"v_mfma_f32_16x16x32_bf16 v[116:119], a[76:79], a[100:103], v[116:119]\n"
"s_cmp_eq_u32 s85, 0x28000\n"
"s_cselect_b32 s85, 0x10000, s85\n"
"v_mfma_f32_16x16x32_bf16 v[112:115], a[76:79], a[96:99], v[112:115]\n"
"ds_read_b64_tr_b16 a[20:21], v7 offset:16384\n"
"v_mfma_f32_16x16x32_bf16 v[80:83], a[72:75], a[96:99], v[80:83]\n"
"v_mfma_f32_16x16x32_bf16 v[144:147], a[80:83], a[96:99], v[144:147]\n"
"ds_read_b64_tr_b16 a[22:23], v7 offset:17408\n"
"v_mfma_f32_16x16x32_bf16 v[176:179], a[84:87], a[96:99], v[176:179]\n"
"v_mfma_f32_16x16x32_bf16 v[180:183], a[84:87], a[100:103], v[180:183]\n"
"ds_read_b128 a[52:55], v4 offset:10240\n"
"s_add_u32 m0, s84, s68\n"
"v_mfma_f32_16x16x32_bf16 v[148:151], a[80:83], a[100:103], v[148:151]\n"
"buffer_load_dwordx4 v2, s[40:43], s60 offen lds\n"
"v_mfma_f32_16x16x32_bf16 v[152:155], a[80:83], a[104:107], v[152:155]\n"
"ds_read_b64_tr_b16 a[24:25], v8 offset:16384\n"
"v_mfma_f32_16x16x32_bf16 v[184:187], a[84:87], a[104:107], v[184:187]\n"
"s_add_u32 s86, s85, 0x4000\n"
"v_mfma_f32_16x16x32_bf16 v[188:191], a[84:87], a[108:111], v[188:191]\n"
"ds_read_b64_tr_b16 a[26:27], v8 offset:17408\n"
"v_mfma_f32_16x16x32_bf16 v[156:159], a[80:83], a[108:111], v[156:159]\n"
"v_mfma_f32_16x16x32_bf16 v[160:163], a[80:83], a[112:115], v[160:163]\n"
"ds_read_b128 a[56:59], v4 offset:12288\n"
"v_mfma_f32_16x16x32_bf16 v[192:195], a[84:87], a[112:115], v[192:195]\n"
"v_mfma_f32_16x16x32_bf16 v[196:199], a[84:87], a[116:119], v[196:199]\n"
"ds_read_b64_tr_b16 a[28:29], v9 offset:16384\n"
"s_add_u32 m0, s84, s69\n"
"v_mfma_f32_16x16x32_bf16 v[164:167], a[80:83], a[116:119], v[164:167]\n"
"buffer_load_dwordx4 v2, s[40:43], s61 offen lds\n"
"v_mfma_f32_16x16x32_bf16 v[168:171], a[80:83], a[120:123], v[168:171]\n"
"ds_read_b64_tr_b16 a[30:31], v9 offset:17408\n"
"v_mfma_f32_16x16x32_bf16 v[200:203], a[84:87], a[120:123], v[200:203]\n"
"v_mfma_f32_16x16x32_bf16 v[204:207], a[84:87], a[124:127], v[204:207]\n"
"ds_read_b128 a[60:63], v4 offset:14336\n"
"v_mfma_f32_16x16x32_bf16 v[172:175], a[80:83], a[124:127], v[172:175]\n"
"v_mfma_f32_16x16x32_bf16 v[236:239], a[88:91], a[124:127], v[236:239]\n"
"v_mfma_f32_16x16x32_bf16 a[140:143], a[92:95], a[124:127], a[140:143]\n"
"v_mfma_f32_16x16x32_bf16 a[136:139], a[92:95], a[120:123], a[136:139]\n"
"s_add_u32 m0, s84, s70\n"
"v_mfma_f32_16x16x32_bf16 v[232:235], a[88:91], a[120:123], v[232:235]\n"
"buffer_load_dwordx4 v2, s[40:43], s62 offen lds\n"
"v_mfma_f32_16x16x32_bf16 v[228:231], a[88:91], a[116:119], v[228:231]\n"
"v_mfma_f32_16x16x32_bf16 a[132:135], a[92:95], a[116:119], a[132:135]\n"
"v_mfma_f32_16x16x32_bf16 a[128:131], a[92:95], a[112:115], a[128:131]\n"
"v_mfma_f32_16x16x32_bf16 v[224:227], a[88:91], a[112:115], v[224:227]\n"
"v_mfma_f32_16x16x32_bf16 v[220:223], a[88:91], a[108:111], v[220:223]\n"
"v_mfma_f32_16x16x32_bf16 v[252:255], a[92:95], a[108:111], v[252:255]\n"
"v_mfma_f32_16x16x32_bf16 v[248:251], a[92:95], a[104:107], v[248:251]\n"
"s_add_u32 m0, s84, s71\n"
"v_mfma_f32_16x16x32_bf16 v[216:219], a[88:91], a[104:107], v[216:219]\n"
"buffer_load_dwordx4 v2, s[40:43], s63 offen lds\n"
"v_mfma_f32_16x16x32_bf16 v[212:215], a[88:91], a[100:103], v[212:215]\n"
"v_mfma_f32_16x16x32_bf16 v[244:247], a[92:95], a[100:103], v[244:247]\n"
"v_mfma_f32_16x16x32_bf16 v[240:243], a[92:95], a[96:99], v[240:243]\n"
"v_mfma_f32_16x16x32_bf16 v[208:211], a[88:91], a[96:99], v[208:211]\n"
"s_waitcnt lgkmcnt(0)\n"
"v_mfma_f32_16x16x32_bf16 v[16:19], a[0:3], a[32:35], v[16:19]\n"
"ds_read_b64_tr_b16 a[64:65], v6 offset:8192\n"
"v_mfma_f32_16x16x32_bf16 v[48:51], a[4:7], a[32:35], v[48:51]\n"
"v_mfma_f32_16x16x32_bf16 v[52:55], a[4:7], a[36:39], v[52:55]\n"
"ds_read_b64_tr_b16 a[66:67], v6 offset:9216\n"
"s_add_u32 m0, s85, s76\n"
"v_mfma_f32_16x16x32_bf16 v[20:23], a[0:3], a[36:39], v[20:23]\n"
"buffer_load_dwordx4 v3, s[44:47], s72 offen lds\n"
"v_mfma_f32_16x16x32_bf16 v[24:27], a[0:3], a[40:43], v[24:27]\n"
"ds_read_b128 a[96:99], v5 offset:0\n"
"v_mfma_f32_16x16x32_bf16 v[56:59], a[4:7], a[40:43], v[56:59]\n"
"v_mfma_f32_16x16x32_bf16 v[60:63], a[4:7], a[44:47], v[60:63]\n"
"ds_read_b64_tr_b16 a[68:69], v7 offset:8192\n"
"v_mfma_f32_16x16x32_bf16 v[28:31], a[0:3], a[44:47], v[28:31]\n"
"v_mfma_f32_16x16x32_bf16 v[32:35], a[0:3], a[48:51], v[32:35]\n"
"ds_read_b64_tr_b16 a[70:71], v7 offset:9216\n"
"v_mfma_f32_16x16x32_bf16 v[64:67], a[4:7], a[48:51], v[64:67]\n"
"v_mfma_f32_16x16x32_bf16 v[68:71], a[4:7], a[52:55], v[68:71]\n"
"ds_read_b128 a[100:103], v5 offset:2048\n"
"s_add_u32 m0, s86, s76\n"
"v_mfma_f32_16x16x32_bf16 v[36:39], a[0:3], a[52:55], v[36:39]\n"
"buffer_load_dwordx4 v3, s[48:51], s72 offen lds\n"
"v_mfma_f32_16x16x32_bf16 v[40:43], a[0:3], a[56:59], v[40:43]\n"
"ds_read_b64_tr_b16 a[72:73], v8 offset:8192\n"
"v_mfma_f32_16x16x32_bf16 v[72:75], a[4:7], a[56:59], v[72:75]\n"
"v_mfma_f32_16x16x32_bf16 v[76:79], a[4:7], a[60:63], v[76:79]\n"
"ds_read_b64_tr_b16 a[74:75], v8 offset:9216\n"
"v_mfma_f32_16x16x32_bf16 v[44:47], a[0:3], a[60:63], v[44:47]\n"
"buffer_load_ubyte a146, v14, s[52:55], s38 offen\n"
"v_mfma_f32_16x16x32_bf16 v[108:111], a[8:11], a[60:63], v[108:111]\n"
"ds_read_b128 a[104:107], v5 offset:4096\n"
"v_mfma_f32_16x16x32_bf16 v[140:143], a[12:15], a[60:63], v[140:143]\n"
"s_add_u32 s38, s38, s39\n"
"v_mfma_f32_16x16x32_bf16 v[136:139], a[12:15], a[56:59], v[136:139]\n"
"ds_read_b64_tr_b16 a[76:77], v9 offset:8192\n"
"s_add_u32 m0, s85, s77\n"
"v_mfma_f32_16x16x32_bf16 v[104:107], a[8:11], a[56:59], v[104:107]\n"
"buffer_load_dwordx4 v3, s[44:47], s73 offen lds\n"
"s_bitcmp1_b32 0x3000, s88\n"
"s_cbranch_scc1 .Lg1tch\n"
".Lg1tchd:\n"
"v_mfma_f32_16x16x32_bf16 v[100:103], a[8:11], a[52:55], v[100:103]\n"
"ds_read_b64_tr_b16 a[78:79], v9 offset:9216\n"
"v_mfma_f32_16x16x32_bf16 v[132:135], a[12:15], a[52:55], v[132:135]\n"
"s_add_u32 s40, s40, 128\n"
"s_addc_u32 s41, s41, 0\n"
"v_mfma_f32_16x16x32_bf16 v[128:131], a[12:15], a[48:51], v[128:131]\n"
"ds_read_b128 a[108:111], v5 offset:6144\n"
"v_mfma_f32_16x16x32_bf16 v[96:99], a[8:11], a[48:51], v[96:99]\n"
"s_xor_b32 s84, s84, 0x8000\n"
"v_mfma_f32_16x16x32_bf16 v[92:95], a[8:11], a[44:47], v[92:95]\n"
"ds_read_b64_tr_b16 a[80:81], v6 offset:24576\n"
"v_mfma_f32_16x16x32_bf16 v[124:127], a[12:15], a[44:47], v[124:127]\n"
"v_mfma_f32_16x16x32_bf16 v[120:123], a[12:15], a[40:43], v[120:123]\n"
"ds_read_b64_tr_b16 a[82:83], v6 offset:25600\n"
"s_add_u32 m0, s86, s77\n"
"v_mfma_f32_16x16x32_bf16 v[88:91], a[8:11], a[40:43], v[88:91]\n"
"buffer_load_dwordx4 v3, s[48:51], s73 offen lds\n"
"v_mfma_f32_16x16x32_bf16 v[84:87], a[8:11], a[36:39], v[84:87]\n"
"ds_read_b128 a[112:115], v5 offset:8192\n"
"v_mfma_f32_16x16x32_bf16 v[116:119], a[12:15], a[36:39], v[116:119]\n"
"v_mfma_f32_16x16x32_bf16 v[112:115], a[12:15], a[32:35], v[112:115]\n"
"ds_read_b64_tr_b16 a[84:85], v7 offset:24576\n"
"v_mfma_f32_16x16x32_bf16 v[80:83], a[8:11], a[32:35], v[80:83]\n"
"v_mfma_f32_16x16x32_bf16 v[144:147], a[16:19], a[32:35], v[144:147]\n"
"ds_read_b64_tr_b16 a[86:87], v7 offset:25600\n"
"v_mfma_f32_16x16x32_bf16 v[176:179], a[20:23], a[32:35], v[176:179]\n"
"v_mfma_f32_16x16x32_bf16 v[180:183], a[20:23], a[36:39], v[180:183]\n"
"ds_read_b128 a[116:119], v5 offset:10240\n"
"s_add_u32 m0, s85, s78\n"
"v_mfma_f32_16x16x32_bf16 v[148:151], a[16:19], a[36:39], v[148:151]\n"
"buffer_load_dwordx4 v3, s[44:47], s74 offen lds\n"
"v_mfma_f32_16x16x32_bf16 v[152:155], a[16:19], a[40:43], v[152:155]\n"
"ds_read_b64_tr_b16 a[88:89], v8 offset:24576\n"
"v_mfma_f32_16x16x32_bf16 v[184:187], a[20:23], a[40:43], v[184:187]\n"
"s_add_u32 s89, s89, 1\n"
"v_mfma_f32_16x16x32_bf16 v[188:191], a[20:23], a[44:47], v[188:191]\n"
"ds_read_b64_tr_b16 a[90:91], v8 offset:25600\n"
"v_mfma_f32_16x16x32_bf16 v[156:159], a[16:19], a[44:47], v[156:159]\n"
"s_cmp_eq_u32 s89, 3\n"
"s_cselect_b32 s87, 0xffff0000, s96\n"
"s_cselect_b32 s89, 0, s89\n"
"v_mfma_f32_16x16x32_bf16 v[160:163], a[16:19], a[48:51], v[160:163]\n"
"ds_read_b128 a[120:123], v5 offset:12288\n"
"v_mfma_f32_16x16x32_bf16 v[192:195], a[20:23], a[48:51], v[192:195]\n"
"v_mfma_f32_16x16x32_bf16 v[196:199], a[20:23], a[52:55], v[196:199]\n"
"ds_read_b64_tr_b16 a[92:93], v9 offset:24576\n"
"s_add_u32 m0, s86, s78\n"
"v_mfma_f32_16x16x32_bf16 v[164:167], a[16:19], a[52:55], v[164:167]\n"
"buffer_load_dwordx4 v3, s[48:51], s74 offen lds\n"
"v_mfma_f32_16x16x32_bf16 v[168:171], a[16:19], a[56:59], v[168:171]\n"
"ds_read_b64_tr_b16 a[94:95], v9 offset:25600\n"
"v_mfma_f32_16x16x32_bf16 v[200:203], a[20:23], a[56:59], v[200:203]\n"
"v_mfma_f32_16x16x32_bf16 v[204:207], a[20:23], a[60:63], v[204:207]\n"
"ds_read_b128 a[124:127], v5 offset:14336\n"
"v_mfma_f32_16x16x32_bf16 v[172:175], a[16:19], a[60:63], v[172:175]\n"
"v_mfma_f32_16x16x32_bf16 v[236:239], a[24:27], a[60:63], v[236:239]\n"
"v_mfma_f32_16x16x32_bf16 a[140:143], a[28:31], a[60:63], a[140:143]\n"
"v_mfma_f32_16x16x32_bf16 a[136:139], a[28:31], a[56:59], a[136:139]\n"
"s_add_u32 m0, s85, s79\n"
"v_mfma_f32_16x16x32_bf16 v[232:235], a[24:27], a[56:59], v[232:235]\n"
"buffer_load_dwordx4 v3, s[44:47], s75 offen lds\n"
"v_mfma_f32_16x16x32_bf16 v[228:231], a[24:27], a[52:55], v[228:231]\n"
"v_xor_b32 v4, 0x8000, v4\n"
"v_mfma_f32_16x16x32_bf16 a[132:135], a[28:31], a[52:55], a[132:135]\n"
"v_mfma_f32_16x16x32_bf16 a[128:131], a[28:31], a[48:51], a[128:131]\n"
"v_xor_b32 v5, 0x8000, v5\n"
"v_mfma_f32_16x16x32_bf16 v[224:227], a[24:27], a[48:51], v[224:227]\n"
"v_mfma_f32_16x16x32_bf16 v[220:223], a[24:27], a[44:47], v[220:223]\n"
"v_add_u32 v6, s87, v6\n"
"v_mfma_f32_16x16x32_bf16 v[252:255], a[28:31], a[44:47], v[252:255]\n"
"v_mfma_f32_16x16x32_bf16 v[248:251], a[28:31], a[40:43], v[248:251]\n"
"s_add_u32 m0, s86, s79\n"
"v_add_u32 v7, s87, v7\n"
"v_mfma_f32_16x16x32_bf16 v[216:219], a[24:27], a[40:43], v[216:219]\n"
"buffer_load_dwordx4 v3, s[48:51], s75 offen lds\n"
"v_mfma_f32_16x16x32_bf16 v[212:215], a[24:27], a[36:39], v[212:215]\n"
"v_add_u32 v8, s87, v8\n"
"v_mfma_f32_16x16x32_bf16 v[244:247], a[28:31], a[36:39], v[244:247]\n"
"s_sub_u32 s88, s88, 1\n"
"s_cmp_lg_u32 s88, 0\n"
"v_mfma_f32_16x16x32_bf16 v[240:243], a[28:31], a[32:35], v[240:243]\n"
"v_add_u32 v9, s87, v9\n"
"v_mfma_f32_16x16x32_bf16 v[208:211], a[24:27], a[32:35], v[208:211]\n"
"s_waitcnt vmcnt(9) lgkmcnt(0)\n"
"s_barrier\n"
"s_cbranch_scc1 .Lg1loop\n"
"v_mfma_f32_16x16x32_bf16 v[16:19], a[64:67], a[96:99], v[16:19]\n"
"ds_read_b64_tr_b16 a[0:1], v6 offset:0\n"
"v_mfma_f32_16x16x32_bf16 v[48:51], a[68:71], a[96:99], v[48:51]\n"
"v_mfma_f32_16x16x32_bf16 v[52:55], a[68:71], a[100:103], v[52:55]\n"
"ds_read_b64_tr_b16 a[2:3], v6 offset:1024\n"
"s_add_u32 m0, s84, s64\n"
"v_mfma_f32_16x16x32_bf16 v[20:23], a[64:67], a[100:103], v[20:23]\n"
"buffer_load_dwordx4 v2, s[40:43], s56 offen lds\n"
"v_mfma_f32_16x16x32_bf16 v[24:27], a[64:67], a[104:107], v[24:27]\n"
"ds_read_b128 a[32:35], v4 offset:0\n"
"v_mfma_f32_16x16x32_bf16 v[56:59], a[68:71], a[104:107], v[56:59]\n"
"v_mfma_f32_16x16x32_bf16 v[60:63], a[68:71], a[108:111], v[60:63]\n"
"ds_read_b64_tr_b16 a[4:5], v7 offset:0\n"
"v_mfma_f32_16x16x32_bf16 v[28:31], a[64:67], a[108:111], v[28:31]\n"
"v_mfma_f32_16x16x32_bf16 v[32:35], a[64:67], a[112:115], v[32:35]\n"
"ds_read_b64_tr_b16 a[6:7], v7 offset:1024\n"
"v_mfma_f32_16x16x32_bf16 v[64:67], a[68:71], a[112:115], v[64:67]\n"
"v_mfma_f32_16x16x32_bf16 v[68:71], a[68:71], a[116:119], v[68:71]\n"
"ds_read_b128 a[36:39], v4 offset:2048\n"
"s_add_u32 m0, s84, s65\n"
"v_mfma_f32_16x16x32_bf16 v[36:39], a[64:67], a[116:119], v[36:39]\n"
"buffer_load_dwordx4 v2, s[40:43], s57 offen lds\n"
"v_mfma_f32_16x16x32_bf16 v[40:43], a[64:67], a[120:123], v[40:43]\n"
"ds_read_b64_tr_b16 a[8:9], v8 offset:0\n"
"v_mfma_f32_16x16x32_bf16 v[72:75], a[68:71], a[120:123], v[72:75]\n"
"v_mfma_f32_16x16x32_bf16 v[76:79], a[68:71], a[124:127], v[76:79]\n"
"ds_read_b64_tr_b16 a[10:11], v8 offset:1024\n"
"v_mfma_f32_16x16x32_bf16 v[44:47], a[64:67], a[124:127], v[44:47]\n"
"v_mfma_f32_16x16x32_bf16 v[108:111], a[72:75], a[124:127], v[108:111]\n"
"ds_read_b128 a[40:43], v4 offset:4096\n"
"v_mfma_f32_16x16x32_bf16 v[140:143], a[76:79], a[124:127], v[140:143]\n"
"v_mfma_f32_16x16x32_bf16 v[136:139], a[76:79], a[120:123], v[136:139]\n"
"ds_read_b64_tr_b16 a[12:13], v9 offset:0\n"
"s_add_u32 m0, s84, s66\n"
"v_mfma_f32_16x16x32_bf16 v[104:107], a[72:75], a[120:123], v[104:107]\n"
"buffer_load_dwordx4 v2, s[40:43], s58 offen lds\n"
"v_mfma_f32_16x16x32_bf16 v[100:103], a[72:75], a[116:119], v[100:103]\n"
"ds_read_b64_tr_b16 a[14:15], v9 offset:1024\n"
"v_mfma_f32_16x16x32_bf16 v[132:135], a[76:79], a[116:119], v[132:135]\n"
"v_mfma_f32_16x16x32_bf16 v[128:131], a[76:79], a[112:115], v[128:131]\n"
"ds_read_b128 a[44:47], v4 offset:6144\n"
"v_mfma_f32_16x16x32_bf16 v[96:99], a[72:75], a[112:115], v[96:99]\n"
"v_mfma_f32_16x16x32_bf16 v[92:95], a[72:75], a[108:111], v[92:95]\n"
"ds_read_b64_tr_b16 a[16:17], v6 offset:16384\n"
"v_mfma_f32_16x16x32_bf16 v[124:127], a[76:79], a[108:111], v[124:127]\n"
"v_mfma_f32_16x16x32_bf16 v[120:123], a[76:79], a[104:107], v[120:123]\n"
"ds_read_b64_tr_b16 a[18:19], v6 offset:17408\n"
"s_add_u32 m0, s84, s67\n"
"v_mfma_f32_16x16x32_bf16 v[88:91], a[72:75], a[104:107], v[88:91]\n"
"buffer_load_dwordx4 v2, s[40:43], s59 offen lds\n"
"v_mfma_f32_16x16x32_bf16 v[84:87], a[72:75], a[100:103], v[84:87]\n"
"ds_read_b128 a[48:51], v4 offset:8192\n"
"v_mfma_f32_16x16x32_bf16 v[116:119], a[76:79], a[100:103], v[116:119]\n"
"v_mfma_f32_16x16x32_bf16 v[112:115], a[76:79], a[96:99], v[112:115]\n"
"ds_read_b64_tr_b16 a[20:21], v7 offset:16384\n"
"v_mfma_f32_16x16x32_bf16 v[80:83], a[72:75], a[96:99], v[80:83]\n"
"v_mfma_f32_16x16x32_bf16 v[144:147], a[80:83], a[96:99], v[144:147]\n"
"ds_read_b64_tr_b16 a[22:23], v7 offset:17408\n"
"v_mfma_f32_16x16x32_bf16 v[176:179], a[84:87], a[96:99], v[176:179]\n"
"v_mfma_f32_16x16x32_bf16 v[180:183], a[84:87], a[100:103], v[180:183]\n"
"ds_read_b128 a[52:55], v4 offset:10240\n"
"s_add_u32 m0, s84, s68\n"
"v_mfma_f32_16x16x32_bf16 v[148:151], a[80:83], a[100:103], v[148:151]\n"
"buffer_load_dwordx4 v2, s[40:43], s60 offen lds\n"
"v_mfma_f32_16x16x32_bf16 v[152:155], a[80:83], a[104:107], v[152:155]\n"
"ds_read_b64_tr_b16 a[24:25], v8 offset:16384\n"
"v_mfma_f32_16x16x32_bf16 v[184:187], a[84:87], a[104:107], v[184:187]\n"
"v_mfma_f32_16x16x32_bf16 v[188:191], a[84:87], a[108:111], v[188:191]\n"
"ds_read_b64_tr_b16 a[26:27], v8 offset:17408\n"
"v_mfma_f32_16x16x32_bf16 v[156:159], a[80:83], a[108:111], v[156:159]\n"
"v_mfma_f32_16x16x32_bf16 v[160:163], a[80:83], a[112:115], v[160:163]\n"
"ds_read_b128 a[56:59], v4 offset:12288\n"
"v_mfma_f32_16x16x32_bf16 v[192:195], a[84:87], a[112:115], v[192:195]\n"
"v_mfma_f32_16x16x32_bf16 v[196:199], a[84:87], a[116:119], v[196:199]\n"
"ds_read_b64_tr_b16 a[28:29], v9 offset:16384\n"
"s_add_u32 m0, s84, s69\n"
"v_mfma_f32_16x16x32_bf16 v[164:167], a[80:83], a[116:119], v[164:167]\n"
"buffer_load_dwordx4 v2, s[40:43], s61 offen lds\n"
"v_mfma_f32_16x16x32_bf16 v[168:171], a[80:83], a[120:123], v[168:171]\n"
"ds_read_b64_tr_b16 a[30:31], v9 offset:17408\n"
"v_mfma_f32_16x16x32_bf16 v[200:203], a[84:87], a[120:123], v[200:203]\n"
"v_mfma_f32_16x16x32_bf16 v[204:207], a[84:87], a[124:127], v[204:207]\n"
"ds_read_b128 a[60:63], v4 offset:14336\n"
"v_mfma_f32_16x16x32_bf16 v[172:175], a[80:83], a[124:127], v[172:175]\n"
"v_mfma_f32_16x16x32_bf16 v[236:239], a[88:91], a[124:127], v[236:239]\n"
"v_mfma_f32_16x16x32_bf16 a[140:143], a[92:95], a[124:127], a[140:143]\n"
"v_mfma_f32_16x16x32_bf16 a[136:139], a[92:95], a[120:123], a[136:139]\n"
"s_add_u32 m0, s84, s70\n"
"v_mfma_f32_16x16x32_bf16 v[232:235], a[88:91], a[120:123], v[232:235]\n"
"buffer_load_dwordx4 v2, s[40:43], s62 offen lds\n"
"v_mfma_f32_16x16x32_bf16 v[228:231], a[88:91], a[116:119], v[228:231]\n"
"v_mfma_f32_16x16x32_bf16 a[132:135], a[92:95], a[116:119], a[132:135]\n"
"v_mfma_f32_16x16x32_bf16 a[128:131], a[92:95], a[112:115], a[128:131]\n"
"v_mfma_f32_16x16x32_bf16 v[224:227], a[88:91], a[112:115], v[224:227]\n"
"v_mfma_f32_16x16x32_bf16 v[220:223], a[88:91], a[108:111], v[220:223]\n"
"v_mfma_f32_16x16x32_bf16 v[252:255], a[92:95], a[108:111], v[252:255]\n"
"v_mfma_f32_16x16x32_bf16 v[248:251], a[92:95], a[104:107], v[248:251]\n"
"s_add_u32 m0, s84, s71\n"
"v_mfma_f32_16x16x32_bf16 v[216:219], a[88:91], a[104:107], v[216:219]\n"
"buffer_load_dwordx4 v2, s[40:43], s63 offen lds\n"
"v_mfma_f32_16x16x32_bf16 v[212:215], a[88:91], a[100:103], v[212:215]\n"
"v_mfma_f32_16x16x32_bf16 v[244:247], a[92:95], a[100:103], v[244:247]\n"
"v_mfma_f32_16x16x32_bf16 v[240:243], a[92:95], a[96:99], v[240:243]\n"
"v_mfma_f32_16x16x32_bf16 v[208:211], a[88:91], a[96:99], v[208:211]\n"
"s_waitcnt lgkmcnt(0)\n"
"v_mfma_f32_16x16x32_bf16 v[16:19], a[0:3], a[32:35], v[16:19]\n"
"ds_read_b64_tr_b16 a[64:65], v6 offset:8192\n"
"s_mul_i32 s99, s98, 6\n"
"s_mov_b64 s[40:41], s[32:33]\n"
"s_load_dwordx2 s[32:33], s[16:17], 0x40\n"
"v_mfma_f32_16x16x32_bf16 v[48:51], a[4:7], a[32:35], v[48:51]\n"
"s_add_u32 s99, s99, 32768\n"
"s_mov_b32 s42, 7340032\n"
"v_mfma_f32_16x16x32_bf16 v[52:55], a[4:7], a[36:39], v[52:55]\n"
"ds_read_b64_tr_b16 a[66:67], v6 offset:9216\n"
"s_add_u32 s95, s99, 4096\n"
"s_bfe_u32 s73, s19, 0x10001\n"
"v_mfma_f32_16x16x32_bf16 v[20:23], a[0:3], a[36:39], v[20:23]\n"
"buffer_load_dwordx4 a[196:199], v10, s[20:23], s99 offen offset:0\n"
"s_lshl_b32 s64, s73, 7\n"
"v_mfma_f32_16x16x32_bf16 v[24:27], a[0:3], a[40:43], v[24:27]\n"
"ds_read_b128 a[96:99], v5 offset:0\n"
"s_bfe_u32 s74, s19, 0x10002\n"
"s_andn2_b32 s75, 1, s19\n"
"v_mfma_f32_16x16x32_bf16 v[56:59], a[4:7], a[40:43], v[56:59]\n"
"buffer_load_dwordx4 a[200:203], v10, s[20:23], s99 offen offset:1024\n"
"s_lshl_b32 s75, s75, 1\n"
"v_mfma_f32_16x16x32_bf16 v[60:63], a[4:7], a[44:47], v[60:63]\n"
"ds_read_b64_tr_b16 a[68:69], v7 offset:8192\n"
"s_add_u32 s74, s74, s75\n"
"v_mfma_f32_16x16x32_bf16 v[28:31], a[0:3], a[44:47], v[28:31]\n"
"buffer_load_dwordx4 a[204:207], v10, s[20:23], s99 offen offset:2048\n"
"s_lshl_b32 s74, s74, 2\n"
"v_mfma_f32_16x16x32_bf16 v[32:35], a[0:3], a[48:51], v[32:35]\n"
"ds_read_b64_tr_b16 a[70:71], v7 offset:9216\n"
"s_bfe_u32 s75, s19, 0x20003\n"
"v_mfma_f32_16x16x32_bf16 v[64:67], a[4:7], a[48:51], v[64:67]\n"
"buffer_load_dwordx4 a[208:211], v10, s[20:23], s99 offen offset:3072\n"
"s_add_u32 s74, s74, s75\n"
"s_lshr_b32 s75, s19, 5\n"
"v_mfma_f32_16x16x32_bf16 v[68:71], a[4:7], a[52:55], v[68:71]\n"
"ds_read_b128 a[100:103], v5 offset:2048\n"
"s_lshl_b32 s72, s74, 9\n"
"v_mfma_f32_16x16x32_bf16 v[36:39], a[0:3], a[52:55], v[36:39]\n"
"buffer_load_dwordx4 a[212:215], v10, s[20:23], s95 offen offset:0\n"
"s_lshr_b32 s78, s98, 3\n"
"v_mfma_f32_16x16x32_bf16 v[40:43], a[0:3], a[56:59], v[40:43]\n"
"ds_read_b64_tr_b16 a[72:73], v8 offset:8192\n"
"s_add_u32 s72, s72, s78\n"
"v_mfma_f32_16x16x32_bf16 v[72:75], a[4:7], a[56:59], v[72:75]\n"
"buffer_load_dwordx4 a[216:219], v10, s[20:23], s95 offen offset:1024\n"
"s_add_u32 s72, s72, 65536\n"
"v_mfma_f32_16x16x32_bf16 v[76:79], a[4:7], a[60:63], v[76:79]\n"
"ds_read_b64_tr_b16 a[74:75], v8 offset:9216\n"
"s_lshl_b32 s71, s74, 11\n"
"s_lshl_b32 s78, s98, 10\n"
"v_mfma_f32_16x16x32_bf16 v[44:47], a[0:3], a[60:63], v[44:47]\n"
"s_add_u32 s71, s71, s78\n"
"v_mfma_f32_16x16x32_bf16 v[108:111], a[8:11], a[60:63], v[108:111]\n"
"ds_read_b128 a[104:107], v5 offset:4096\n"
"s_lshl_b32 s97, s75, 8\n"
"v_mfma_f32_16x16x32_bf16 v[140:143], a[12:15], a[60:63], v[140:143]\n"
"s_add_u32 s97, s97, s64\n"
"v_mfma_f32_16x16x32_bf16 v[136:139], a[12:15], a[56:59], v[136:139]\n"
"ds_read_b64_tr_b16 a[76:77], v9 offset:8192\n"
"s_add_u32 s71, s71, s97\n"
"v_mfma_f32_16x16x32_bf16 v[104:107], a[8:11], a[56:59], v[104:107]\n"
"s_lshl_b32 s78, s74, 18\n"
"s_add_u32 s78, s78, s97\n"
"v_mfma_f32_16x16x32_bf16 v[100:103], a[8:11], a[52:55], v[100:103]\n"
"ds_read_b64_tr_b16 a[78:79], v9 offset:9216\n"
"s_lshl_b32 s97, s98, 4\n"
"v_mfma_f32_16x16x32_bf16 v[132:135], a[12:15], a[52:55], v[132:135]\n"
"s_add_u32 s78, s78, s97\n"
"v_mfma_f32_16x16x32_bf16 v[128:131], a[12:15], a[48:51], v[128:131]\n"
"ds_read_b128 a[108:111], v5 offset:6144\n"
"s_add_u32 s78, s78, 8388608\n"
"v_mfma_f32_16x16x32_bf16 v[96:99], a[8:11], a[48:51], v[96:99]\n"
"s_add_u32 s52, s28, s78\n"
"s_addc_u32 s53, s29, 0\n"
"s_mov_b32 s54, 4194304\n"
"v_mfma_f32_16x16x32_bf16 v[92:95], a[8:11], a[44:47], v[92:95]\n"
"ds_read_b64_tr_b16 a[80:81], v6 offset:24576\n"
"s_mov_b32 s55, 0x20000\n"
"v_mfma_f32_16x16x32_bf16 v[124:127], a[12:15], a[44:47], v[124:127]\n"
"s_lshl_b32 s78, s74, 1\n"
"v_mfma_f32_16x16x32_bf16 v[120:123], a[12:15], a[40:43], v[120:123]\n"
"ds_read_b64_tr_b16 a[82:83], v6 offset:25600\n"
"s_add_u32 s78, s78, s73\n"
"v_mfma_f32_16x16x32_bf16 v[88:91], a[8:11], a[40:43], v[88:91]\n"
"s_lshl_b32 s77, s78, 12\n"
"v_mfma_f32_16x16x32_bf16 v[84:87], a[8:11], a[36:39], v[84:87]\n"
"ds_read_b128 a[112:115], v5 offset:8192\n"
"s_add_u32 s77, s77, 327680\n"
"s_mul_i32 s78, s78, 56\n"
"v_mfma_f32_16x16x32_bf16 v[116:119], a[12:15], a[36:39], v[116:119]\n"
"s_lshl_b32 s78, s78, 13\n"
"v_mfma_f32_16x16x32_bf16 v[112:115], a[12:15], a[32:35], v[112:115]\n"
"ds_read_b64_tr_b16 a[84:85], v7 offset:24576\n"
"s_lshl_b32 s97, s98, 1\n"
"v_mfma_f32_16x16x32_bf16 v[80:83], a[8:11], a[32:35], v[80:83]\n"
"s_add_u32 s78, s78, s97\n"
"v_mfma_f32_16x16x32_bf16 v[144:147], a[16:19], a[32:35], v[144:147]\n"
"ds_read_b64_tr_b16 a[86:87], v7 offset:25600\n"
"s_add_u32 s78, s78, 33554432\n"
"v_mfma_f32_16x16x32_bf16 v[176:179], a[20:23], a[32:35], v[176:179]\n"
"s_add_u32 s48, s28, s78\n"
"s_addc_u32 s49, s29, 0\n"
"s_mov_b32 s50, 14680064\n"
"v_mfma_f32_16x16x32_bf16 v[180:183], a[20:23], a[36:39], v[180:183]\n"
"ds_read_b128 a[116:119], v5 offset:10240\n"
"s_add_u32 s60, s28, 16777216\n"
"s_addc_u32 s61, s29, 0\n"
"v_mfma_f32_16x16x32_bf16 v[148:151], a[16:19], a[36:39], v[148:151]\n"
"s_mov_b32 s62, 8388608\n"
"v_mfma_f32_16x16x32_bf16 v[152:155], a[16:19], a[40:43], v[152:155]\n"
"ds_read_b64_tr_b16 a[88:89], v8 offset:24576\n"
"s_mov_b32 s63, 0x20000\n"
"v_mfma_f32_16x16x32_bf16 v[184:187], a[20:23], a[40:43], v[184:187]\n"
"s_add_u32 s89, s89, 1\n"
"s_lshl_b32 s97, s98, 3\n"
"v_mfma_f32_16x16x32_bf16 v[188:191], a[20:23], a[44:47], v[188:191]\n"
"ds_read_b64_tr_b16 a[90:91], v8 offset:25600\n"
"s_lshl_b32 s66, s98, 1\n"
"s_add_u32 s66, s66, 0\n"
"v_mfma_f32_16x16x32_bf16 v[156:159], a[16:19], a[44:47], v[156:159]\n"
"s_cmp_eq_u32 s89, 3\n"
"s_cselect_b32 s87, 0xffff0000, s96\n"
"s_cselect_b32 s89, 0, s89\n"
"s_lshl_b32 s67, s19, 14\n"
"v_mfma_f32_16x16x32_bf16 v[160:163], a[16:19], a[48:51], v[160:163]\n"
"ds_read_b128 a[120:123], v5 offset:12288\n"
"s_lshl_b32 s68, s98, 2\n"
"v_mfma_f32_16x16x32_bf16 v[192:195], a[20:23], a[48:51], v[192:195]\n"
"s_add_u32 s67, s67, s68\n"
"v_mfma_f32_16x16x32_bf16 v[196:199], a[20:23], a[52:55], v[196:199]\n"
"ds_read_b64_tr_b16 a[92:93], v9 offset:24576\n"
"s_xor_b32 s68, s67, 32768\n"
"s_lshl_b32 s69, s19, 9\n"
"v_mfma_f32_16x16x32_bf16 v[164:167], a[16:19], a[52:55], v[164:167]\n"
"s_lshr_b32 s78, s98, 3\n"
"v_mfma_f32_16x16x32_bf16 v[168:171], a[16:19], a[56:59], v[168:171]\n"
"ds_read_b64_tr_b16 a[94:95], v9 offset:25600\n"
"s_add_u32 s69, s69, s78\n"
"v_mfma_f32_16x16x32_bf16 v[200:203], a[20:23], a[56:59], v[200:203]\n"
"s_xor_b32 s70, s69, 1024\n"
"v_mfma_f32_16x16x32_bf16 v[204:207], a[20:23], a[60:63], v[204:207]\n"
"ds_read_b128 a[124:127], v5 offset:14336\n"
"s_add_u32 s69, s69, 196608\n"
"v_mfma_f32_16x16x32_bf16 v[172:175], a[16:19], a[60:63], v[172:175]\n"
"s_add_u32 s70, s70, 196608\n"
"s_lshr_b32 s79, s75, 1\n"
"v_mfma_f32_16x16x32_bf16 v[236:239], a[24:27], a[60:63], v[236:239]\n"
"s_lshl_b32 s79, s79, 12\n"
"v_mfma_f32_16x16x32_bf16 a[140:143], a[28:31], a[60:63], a[140:143]\n"
"s_add_u32 s79, s79, 36864\n"
"v_mfma_f32_16x16x32_bf16 a[136:139], a[28:31], a[56:59], a[136:139]\n"
"s_mov_b32 s34, 4194304\n"
"v_mfma_f32_16x16x32_bf16 v[232:235], a[24:27], a[56:59], v[232:235]\n"
"s_mul_i32 s78, s73, 3670016\n"
"v_mfma_f32_16x16x32_bf16 v[228:231], a[24:27], a[52:55], v[228:231]\n"
"v_xor_b32 v4, 0x8000, v4\n"
"s_lshl_b32 s88, s98, 2\n"
"s_lshl_b32 s97, s75, 8\n"
"v_mfma_f32_16x16x32_bf16 a[132:135], a[28:31], a[52:55], a[132:135]\n"
"s_add_u32 s78, s78, s97\n"
"v_mfma_f32_16x16x32_bf16 a[128:131], a[28:31], a[48:51], a[128:131]\n"
"v_xor_b32 v5, 0x8000, v5\n"
"s_add_u32 s40, s40, s78\n"
"s_addc_u32 s41, s41, 0\n"
"v_mfma_f32_16x16x32_bf16 v[224:227], a[24:27], a[48:51], v[224:227]\n"
"s_lshr_b32 s78, s19, 3\n"
"v_mfma_f32_16x16x32_bf16 v[220:223], a[24:27], a[44:47], v[220:223]\n"
"v_add_u32 v6, s87, v6\n"
"s_add_u32 s78, s78, s75\n"
"v_mfma_f32_16x16x32_bf16 v[252:255], a[28:31], a[44:47], v[252:255]\n"
"s_and_b32 s76, s78, 1\n"
"s_lshl_b32 s38, s76, 15\n"
"v_mfma_f32_16x16x32_bf16 v[248:251], a[28:31], a[40:43], v[248:251]\n"
"v_add_u32 v7, s87, v7\n"
"s_lshl_b32 s39, s76, 18\n"
"v_mfma_f32_16x16x32_bf16 v[216:219], a[24:27], a[40:43], v[216:219]\n"
"s_mul_i32 s99, s98, 6\n"
"v_mfma_f32_16x16x32_bf16 v[212:215], a[24:27], a[36:39], v[212:215]\n"
"v_add_u32 v8, s87, v8\n"
"s_add_u32 s99, s99, 32768\n"
"v_mfma_f32_16x16x32_bf16 v[244:247], a[28:31], a[36:39], v[244:247]\n"
"s_add_u32 s65, s82, 131072\n"
"v_mfma_f32_16x16x32_bf16 v[240:243], a[28:31], a[32:35], v[240:243]\n"
"v_add_u32 v9, s87, v9\n"
"v_mfma_f32_16x16x32_bf16 v[208:211], a[24:27], a[32:35], v[208:211]\n"
"s_waitcnt vmcnt(0) lgkmcnt(0)\n"
"s_barrier\n"
"s_lshr_b32 s99, s83, 5\n"
"s_lshl_b32 s99, s99, 2\n"
"s_and_b32 s97, s83, 3\n"
"s_add_u32 s99, s99, s97\n"
"s_getpc_b64 s[94:95]\n"
".Lpfg2:\n"
"s_lshl_b32 s99, s99, 8\n"
"s_add_u32 s99, s99, .Lg1mid-.Lpfg2\n"
"s_min_u32 s99, s99, .Lg2end-.Lpfg2-132\n"
"v_min_u32 v14, 0x80, v14\n"
"v_add_u32 v14, s99, v14\n"
"global_load_dword a145, v14, s[94:95]\n"
"v_mfma_f32_16x16x32_bf16 v[16:19], a[64:67], a[96:99], v[16:19]\n"
"ds_read_b64_tr_b16 a[0:1], v6 offset:0\n"
"v_accvgpr_read_b32 v2, a196\n"
"v_mfma_f32_16x16x32_bf16 v[48:51], a[68:71], a[96:99], v[48:51]\n"
"v_accvgpr_read_b32 v3, a197\n"
"v_mfma_f32_16x16x32_bf16 v[52:55], a[68:71], a[100:103], v[52:55]\n"
"ds_read_b64_tr_b16 a[2:3], v6 offset:1024\n"
"v_accvgpr_read_b32 v11, a198\n"
"v_mfma_f32_16x16x32_bf16 v[20:23], a[64:67], a[100:103], v[20:23]\n"
"v_accvgpr_read_b32 v15, a199\n"
"v_mfma_f32_16x16x32_bf16 v[24:27], a[64:67], a[104:107], v[24:27]\n"
"ds_read_b128 a[32:35], v4 offset:0\n"
"v_mfma_f32_16x16x32_bf16 v[56:59], a[68:71], a[104:107], v[56:59]\n"
"v_mfma_f32_16x16x32_bf16 v[60:63], a[68:71], a[108:111], v[60:63]\n"
"ds_read_b64_tr_b16 a[4:5], v7 offset:0\n"
"v_mfma_f32_16x16x32_bf16 v[28:31], a[64:67], a[108:111], v[28:31]\n"
"v_mfma_f32_16x16x32_bf16 v[32:35], a[64:67], a[112:115], v[32:35]\n"
"ds_read_b64_tr_b16 a[6:7], v7 offset:1024\n"
"s_xor_b32 s74, s39, 262144\n"
"v_mfma_f32_16x16x32_bf16 v[64:67], a[68:71], a[112:115], v[64:67]\n"
"s_add_u32 m0, s88, 98304\n"
"v_mfma_f32_16x16x32_bf16 v[68:71], a[68:71], a[116:119], v[68:71]\n"
"ds_read_b128 a[36:39], v4 offset:2048\n"
"buffer_load_dwordx4 v2, s[40:43], s74 offen offset:0 sc1 lds\n"
"v_mfma_f32_16x16x32_bf16 v[36:39], a[64:67], a[116:119], v[36:39]\n"
"v_mfma_f32_16x16x32_bf16 v[40:43], a[64:67], a[120:123], v[40:43]\n"
"ds_read_b64_tr_b16 a[8:9], v8 offset:0\n"
"v_mfma_f32_16x16x32_bf16 v[72:75], a[68:71], a[120:123], v[72:75]\n"
"v_mfma_f32_16x16x32_bf16 v[76:79], a[68:71], a[124:127], v[76:79]\n"
"ds_read_b64_tr_b16 a[10:11], v8 offset:1024\n"
"v_mfma_f32_16x16x32_bf16 v[44:47], a[64:67], a[124:127], v[44:47]\n"
"v_mfma_f32_16x16x32_bf16 v[108:111], a[72:75], a[124:127], v[108:111]\n"
"ds_read_b128 a[40:43], v4 offset:4096\n"
"v_mfma_f32_16x16x32_bf16 v[140:143], a[76:79], a[124:127], v[140:143]\n"
"buffer_load_dwordx4 v3, s[40:43], s74 offen offset:1024 sc1 lds\n"
"v_mfma_f32_16x16x32_bf16 v[136:139], a[76:79], a[120:123], v[136:139]\n"
"ds_read_b64_tr_b16 a[12:13], v9 offset:0\n"
"v_mfma_f32_16x16x32_bf16 v[104:107], a[72:75], a[120:123], v[104:107]\n"
"v_mfma_f32_16x16x32_bf16 v[100:103], a[72:75], a[116:119], v[100:103]\n"
"ds_read_b64_tr_b16 a[14:15], v9 offset:1024\n"
"v_mfma_f32_16x16x32_bf16 v[132:135], a[76:79], a[116:119], v[132:135]\n"
"v_mfma_f32_16x16x32_bf16 v[128:131], a[76:79], a[112:115], v[128:131]\n"
"ds_read_b128 a[44:47], v4 offset:6144\n"
"v_mfma_f32_16x16x32_bf16 v[96:99], a[72:75], a[112:115], v[96:99]\n"
"v_mfma_f32_16x16x32_bf16 v[92:95], a[72:75], a[108:111], v[92:95]\n"
"ds_read_b64_tr_b16 a[16:17], v6 offset:16384\n"
"buffer_load_dwordx4 v11, s[40:43], s74 offen offset:2048 sc1 lds\n"
"v_mfma_f32_16x16x32_bf16 v[124:127], a[76:79], a[108:111], v[124:127]\n"
"v_mfma_f32_16x16x32_bf16 v[120:123], a[76:79], a[104:107], v[120:123]\n"
"ds_read_b64_tr_b16 a[18:19], v6 offset:17408\n"
"v_mfma_f32_16x16x32_bf16 v[88:91], a[72:75], a[104:107], v[88:91]\n"
"v_mfma_f32_16x16x32_bf16 v[84:87], a[72:75], a[100:103], v[84:87]\n"
"ds_read_b128 a[48:51], v4 offset:8192\n"
"v_mfma_f32_16x16x32_bf16 v[116:119], a[76:79], a[100:103], v[116:119]\n"
"v_mfma_f32_16x16x32_bf16 v[112:115], a[76:79], a[96:99], v[112:115]\n"
"ds_read_b64_tr_b16 a[20:21], v7 offset:16384\n"
"v_mfma_f32_16x16x32_bf16 v[80:83], a[72:75], a[96:99], v[80:83]\n"
"buffer_load_dwordx4 v15, s[40:43], s74 offen offset:3072 sc1 lds\n"
"v_mfma_f32_16x16x32_bf16 v[144:147], a[80:83], a[96:99], v[144:147]\n"
"ds_read_b64_tr_b16 a[22:23], v7 offset:17408\n"
"v_mfma_f32_16x16x32_bf16 v[176:179], a[84:87], a[96:99], v[176:179]\n"
"v_mfma_f32_16x16x32_bf16 v[180:183], a[84:87], a[100:103], v[180:183]\n"
"ds_read_b128 a[52:55], v4 offset:10240\n"
"v_mfma_f32_16x16x32_bf16 v[148:151], a[80:83], a[100:103], v[148:151]\n"
"v_mfma_f32_16x16x32_bf16 v[152:155], a[80:83], a[104:107], v[152:155]\n"
"ds_read_b64_tr_b16 a[24:25], v8 offset:16384\n"
"s_xor_b32 s74, s39, 524288\n"
"v_mfma_f32_16x16x32_bf16 v[184:187], a[84:87], a[104:107], v[184:187]\n"
"s_add_u32 m0, s88, 114688\n"
"v_mfma_f32_16x16x32_bf16 v[188:191], a[84:87], a[108:111], v[188:191]\n"
"ds_read_b64_tr_b16 a[26:27], v8 offset:17408\n"
"buffer_load_dwordx4 v2, s[40:43], s74 offen offset:0 sc1 lds\n"
"v_mfma_f32_16x16x32_bf16 v[156:159], a[80:83], a[108:111], v[156:159]\n"
"v_mfma_f32_16x16x32_bf16 v[160:163], a[80:83], a[112:115], v[160:163]\n"
"ds_read_b128 a[56:59], v4 offset:12288\n"
"v_mfma_f32_16x16x32_bf16 v[192:195], a[84:87], a[112:115], v[192:195]\n"
"v_mfma_f32_16x16x32_bf16 v[196:199], a[84:87], a[116:119], v[196:199]\n"
"ds_read_b64_tr_b16 a[28:29], v9 offset:16384\n"
"v_mfma_f32_16x16x32_bf16 v[164:167], a[80:83], a[116:119], v[164:167]\n"
"v_mfma_f32_16x16x32_bf16 v[168:171], a[80:83], a[120:123], v[168:171]\n"
"ds_read_b64_tr_b16 a[30:31], v9 offset:17408\n"
"v_mfma_f32_16x16x32_bf16 v[200:203], a[84:87], a[120:123], v[200:203]\n"
"buffer_load_dwordx4 v3, s[40:43], s74 offen offset:1024 sc1 lds\n"
"v_mfma_f32_16x16x32_bf16 v[204:207], a[84:87], a[124:127], v[204:207]\n"
"ds_read_b128 a[60:63], v4 offset:14336\n"
"v_mfma_f32_16x16x32_bf16 v[172:175], a[80:83], a[124:127], v[172:175]\n"
"v_mfma_f32_16x16x32_bf16 v[236:239], a[88:91], a[124:127], v[236:239]\n"
"v_mfma_f32_16x16x32_bf16 a[140:143], a[92:95], a[124:127], a[140:143]\n"
"v_mfma_f32_16x16x32_bf16 a[136:139], a[92:95], a[120:123], a[136:139]\n"
"v_mfma_f32_16x16x32_bf16 v[232:235], a[88:91], a[120:123], v[232:235]\n"
"v_mfma_f32_16x16x32_bf16 v[228:231], a[88:91], a[116:119], v[228:231]\n"
"buffer_load_dwordx4 v11, s[40:43], s74 offen offset:2048 sc1 lds\n"
"v_mfma_f32_16x16x32_bf16 a[132:135], a[92:95], a[116:119], a[132:135]\n"
"v_mfma_f32_16x16x32_bf16 a[128:131], a[92:95], a[112:115], a[128:131]\n"
"v_mfma_f32_16x16x32_bf16 v[224:227], a[88:91], a[112:115], v[224:227]\n"
"v_mfma_f32_16x16x32_bf16 v[220:223], a[88:91], a[108:111], v[220:223]\n"
"v_mfma_f32_16x16x32_bf16 v[252:255], a[92:95], a[108:111], v[252:255]\n"
"v_mfma_f32_16x16x32_bf16 v[248:251], a[92:95], a[104:107], v[248:251]\n"
"v_mfma_f32_16x16x32_bf16 v[216:219], a[88:91], a[104:107], v[216:219]\n"
"buffer_load_dwordx4 v15, s[40:43], s74 offen offset:3072 sc1 lds\n"
"v_mfma_f32_16x16x32_bf16 v[212:215], a[88:91], a[100:103], v[212:215]\n"
"v_mfma_f32_16x16x32_bf16 v[244:247], a[92:95], a[100:103], v[244:247]\n"
"v_mfma_f32_16x16x32_bf16 v[240:243], a[92:95], a[96:99], v[240:243]\n"
"v_mfma_f32_16x16x32_bf16 v[208:211], a[88:91], a[96:99], v[208:211]\n"
"s_waitcnt lgkmcnt(0)\n"
"v_mfma_f32_16x16x32_bf16 v[16:19], a[0:3], a[32:35], v[16:19]\n"
"ds_read_b64_tr_b16 a[64:65], v6 offset:8192\n"
"v_accvgpr_write_b32 a192, -1\n"
"v_mfma_f32_16x16x32_bf16 v[48:51], a[4:7], a[32:35], v[48:51]\n"
"v_accvgpr_write_b32 a193, -1\n"
"v_mfma_f32_16x16x32_bf16 v[52:55], a[4:7], a[36:39], v[52:55]\n"
"ds_read_b64_tr_b16 a[66:67], v6 offset:9216\n"
"v_accvgpr_write_b32 a194, -1\n"
"v_mfma_f32_16x16x32_bf16 v[20:23], a[0:3], a[36:39], v[20:23]\n"
"v_accvgpr_write_b32 a195, -1\n"
"v_mfma_f32_16x16x32_bf16 v[24:27], a[0:3], a[40:43], v[24:27]\n"
"ds_read_b128 a[96:99], v5 offset:0\n"
"v_mfma_f32_16x16x32_bf16 v[56:59], a[4:7], a[40:43], v[56:59]\n"
"v_mfma_f32_16x16x32_bf16 v[60:63], a[4:7], a[44:47], v[60:63]\n"
"ds_read_b64_tr_b16 a[68:69], v7 offset:8192\n"
"s_xor_b32 s74, s39, 786432\n"
"v_mfma_f32_16x16x32_bf16 v[28:31], a[0:3], a[44:47], v[28:31]\n"
"s_add_u32 m0, s88, 0\n"
"v_mfma_f32_16x16x32_bf16 v[32:35], a[0:3], a[48:51], v[32:35]\n"
"ds_read_b64_tr_b16 a[70:71], v7 offset:9216\n"
"buffer_load_dwordx4 v2, s[40:43], s74 offen offset:0 sc1 lds\n"
"v_mfma_f32_16x16x32_bf16 v[64:67], a[4:7], a[48:51], v[64:67]\n"
"v_mfma_f32_16x16x32_bf16 v[68:71], a[4:7], a[52:55], v[68:71]\n"
"ds_read_b128 a[100:103], v5 offset:2048\n"
"v_mfma_f32_16x16x32_bf16 v[36:39], a[0:3], a[52:55], v[36:39]\n"
"v_mfma_f32_16x16x32_bf16 v[40:43], a[0:3], a[56:59], v[40:43]\n"
"ds_read_b64_tr_b16 a[72:73], v8 offset:8192\n"
"v_mfma_f32_16x16x32_bf16 v[72:75], a[4:7], a[56:59], v[72:75]\n"
"v_mfma_f32_16x16x32_bf16 v[76:79], a[4:7], a[60:63], v[76:79]\n"
"ds_read_b64_tr_b16 a[74:75], v8 offset:9216\n"
"v_mfma_f32_16x16x32_bf16 v[44:47], a[0:3], a[60:63], v[44:47]\n"
"buffer_load_dwordx4 v3, s[40:43], s74 offen offset:1024 sc1 lds\n"
"v_mfma_f32_16x16x32_bf16 v[108:111], a[8:11], a[60:63], v[108:111]\n"
"ds_read_b128 a[104:107], v5 offset:4096\n"
"v_mfma_f32_16x16x32_bf16 v[140:143], a[12:15], a[60:63], v[140:143]\n"
"v_mfma_f32_16x16x32_bf16 v[136:139], a[12:15], a[56:59], v[136:139]\n"
"ds_read_b64_tr_b16 a[76:77], v9 offset:8192\n"
"v_mfma_f32_16x16x32_bf16 v[104:107], a[8:11], a[56:59], v[104:107]\n"
"v_mfma_f32_16x16x32_bf16 v[100:103], a[8:11], a[52:55], v[100:103]\n"
"ds_read_b64_tr_b16 a[78:79], v9 offset:9216\n"
"v_mfma_f32_16x16x32_bf16 v[132:135], a[12:15], a[52:55], v[132:135]\n"
"v_mfma_f32_16x16x32_bf16 v[128:131], a[12:15], a[48:51], v[128:131]\n"
"ds_read_b128 a[108:111], v5 offset:6144\n"
"buffer_load_dwordx4 v11, s[40:43], s74 offen offset:2048 sc1 lds\n"
"v_mfma_f32_16x16x32_bf16 v[96:99], a[8:11], a[48:51], v[96:99]\n"
"v_mfma_f32_16x16x32_bf16 v[92:95], a[8:11], a[44:47], v[92:95]\n"
"ds_read_b64_tr_b16 a[80:81], v6 offset:24576\n"
"v_mfma_f32_16x16x32_bf16 v[124:127], a[12:15], a[44:47], v[124:127]\n"
"v_mfma_f32_16x16x32_bf16 v[120:123], a[12:15], a[40:43], v[120:123]\n"
"ds_read_b64_tr_b16 a[82:83], v6 offset:25600\n"
"v_mfma_f32_16x16x32_bf16 v[88:91], a[8:11], a[40:43], v[88:91]\n"
"v_mfma_f32_16x16x32_bf16 v[84:87], a[8:11], a[36:39], v[84:87]\n"
"ds_read_b128 a[112:115], v5 offset:8192\n"
"v_mfma_f32_16x16x32_bf16 v[116:119], a[12:15], a[36:39], v[116:119]\n"
"buffer_load_dwordx4 v15, s[40:43], s74 offen offset:3072 sc1 lds\n"
"v_mfma_f32_16x16x32_bf16 v[112:115], a[12:15], a[32:35], v[112:115]\n"
"ds_read_b64_tr_b16 a[84:85], v7 offset:24576\n"
"v_mfma_f32_16x16x32_bf16 v[80:83], a[8:11], a[32:35], v[80:83]\n"
"v_mfma_f32_16x16x32_bf16 v[144:147], a[16:19], a[32:35], v[144:147]\n"
"ds_read_b64_tr_b16 a[86:87], v7 offset:25600\n"
"v_mfma_f32_16x16x32_bf16 v[176:179], a[20:23], a[32:35], v[176:179]\n"
"v_mfma_f32_16x16x32_bf16 v[180:183], a[20:23], a[36:39], v[180:183]\n"
"ds_read_b128 a[116:119], v5 offset:10240\n"
"v_mfma_f32_16x16x32_bf16 v[148:151], a[16:19], a[36:39], v[148:151]\n"
"buffer_store_dwordx4 a[192:195], v10, s[60:63], s67 offen offset:0 sc1\n"
"v_mfma_f32_16x16x32_bf16 v[152:155], a[16:19], a[40:43], v[152:155]\n"
"ds_read_b64_tr_b16 a[88:89], v8 offset:24576\n"
"v_mfma_f32_16x16x32_bf16 v[184:187], a[20:23], a[40:43], v[184:187]\n"
"v_mfma_f32_16x16x32_bf16 v[188:191], a[20:23], a[44:47], v[188:191]\n"
"ds_read_b64_tr_b16 a[90:91], v8 offset:25600\n"
"v_mfma_f32_16x16x32_bf16 v[156:159], a[16:19], a[44:47], v[156:159]\n"
"v_mfma_f32_16x16x32_bf16 v[160:163], a[16:19], a[48:51], v[160:163]\n"
"ds_read_b128 a[120:123], v5 offset:12288\n"
"v_mfma_f32_16x16x32_bf16 v[192:195], a[20:23], a[48:51], v[192:195]\n"
"v_mfma_f32_16x16x32_bf16 v[196:199], a[20:23], a[52:55], v[196:199]\n"
"ds_read_b64_tr_b16 a[92:93], v9 offset:24576\n"
"buffer_store_dwordx4 a[192:195], v10, s[60:63], s67 offen offset:1024 sc1\n"
"v_mfma_f32_16x16x32_bf16 v[164:167], a[16:19], a[52:55], v[164:167]\n"
"v_mfma_f32_16x16x32_bf16 v[168:171], a[16:19], a[56:59], v[168:171]\n"
"ds_read_b64_tr_b16 a[94:95], v9 offset:25600\n"
"v_mfma_f32_16x16x32_bf16 v[200:203], a[20:23], a[56:59], v[200:203]\n"
"v_mfma_f32_16x16x32_bf16 v[204:207], a[20:23], a[60:63], v[204:207]\n"
"ds_read_b128 a[124:127], v5 offset:14336\n"
"v_mfma_f32_16x16x32_bf16 v[172:175], a[16:19], a[60:63], v[172:175]\n"
"v_mfma_f32_16x16x32_bf16 v[236:239], a[24:27], a[60:63], v[236:239]\n"
"v_mfma_f32_16x16x32_bf16 a[140:143], a[28:31], a[60:63], a[140:143]\n"
"buffer_store_dwordx4 a[192:195], v10, s[60:63], s67 offen offset:2048 sc1\n"
"v_mfma_f32_16x16x32_bf16 a[136:139], a[28:31], a[56:59], a[136:139]\n"
"v_mfma_f32_16x16x32_bf16 v[232:235], a[24:27], a[56:59], v[232:235]\n"
"v_mfma_f32_16x16x32_bf16 v[228:231], a[24:27], a[52:55], v[228:231]\n"
"v_mfma_f32_16x16x32_bf16 a[132:135], a[28:31], a[52:55], a[132:135]\n"
"v_mfma_f32_16x16x32_bf16 a[128:131], a[28:31], a[48:51], a[128:131]\n"
"v_mfma_f32_16x16x32_bf16 v[224:227], a[24:27], a[48:51], v[224:227]\n"
"v_mfma_f32_16x16x32_bf16 v[220:223], a[24:27], a[44:47], v[220:223]\n"
"buffer_store_dwordx4 a[192:195], v10, s[60:63], s67 offen offset:3072 sc1\n"
"v_mfma_f32_16x16x32_bf16 v[252:255], a[28:31], a[44:47], v[252:255]\n"
"v_mfma_f32_16x16x32_bf16 v[248:251], a[28:31], a[40:43], v[248:251]\n"
"v_mfma_f32_16x16x32_bf16 v[216:219], a[24:27], a[40:43], v[216:219]\n"
"v_mfma_f32_16x16x32_bf16 v[212:215], a[24:27], a[36:39], v[212:215]\n"
"v_mfma_f32_16x16x32_bf16 v[244:247], a[28:31], a[36:39], v[244:247]\n"
"v_mfma_f32_16x16x32_bf16 v[240:243], a[28:31], a[32:35], v[240:243]\n"
"v_mfma_f32_16x16x32_bf16 v[208:211], a[24:27], a[32:35], v[208:211]\n"
"s_waitcnt lgkmcnt(0)\n"
"s_barrier\n"
".Lg1mid:\n"
"v_mfma_f32_16x16x32_bf16 v[16:19], a[64:67], a[96:99], v[16:19]\n"
"s_add_u32 s44, s28, 33554432\n"
"s_addc_u32 s45, s29, 0\n"
"s_mov_b32 s46, 14680064\n"
"s_add_u32 s56, s80, 0\n"
"s_add_u32 s57, s80, 4096\n"
"s_add_u32 s58, s80, 8192\n"
"s_add_u32 s59, s80, 12288\n"
"v_mfma_f32_16x16x32_bf16 v[48:51], a[68:71], a[96:99], v[48:51]\n"
"v_mfma_f32_16x16x32_bf16 v[144:147], a[80:83], a[96:99], v[144:147]\n"
"s_xor_b32 s74, s39, 0\n"
"v_mfma_f32_16x16x32_bf16 v[176:179], a[84:87], a[96:99], v[176:179]\n"
"s_add_u32 m0, s88, 81920\n"
"v_mfma_f32_16x16x32_bf16 v[20:23], a[64:67], a[100:103], v[20:23]\n"
"buffer_load_dwordx4 v2, s[40:43], s74 offen offset:0 sc1 lds\n"
"v_mfma_f32_16x16x32_bf16 v[52:55], a[68:71], a[100:103], v[52:55]\n"
"buffer_load_dwordx4 v3, s[40:43], s74 offen offset:1024 sc1 lds\n"
"v_mfma_f32_16x16x32_bf16 v[148:151], a[80:83], a[100:103], v[148:151]\n"
"buffer_load_dwordx4 v11, s[40:43], s74 offen offset:2048 sc1 lds\n"
"v_mfma_f32_16x16x32_bf16 v[180:183], a[84:87], a[100:103], v[180:183]\n"
"buffer_load_dwordx4 v15, s[40:43], s74 offen offset:3072 sc1 lds\n"
"v_mul_f32 v144, v16, v144\n"
"v_mfma_f32_16x16x32_bf16 v[24:27], a[64:67], a[104:107], v[24:27]\n"
"v_mul_f32 v145, v17, v145\n"
"v_mul_f32 v146, v18, v146\n"
"v_mfma_f32_16x16x32_bf16 v[56:59], a[68:71], a[104:107], v[56:59]\n"
"v_mul_f32 v147, v19, v147\n"
"v_mul_f32 v176, v48, v176\n"
"v_mfma_f32_16x16x32_bf16 v[152:155], a[80:83], a[104:107], v[152:155]\n"
"v_mul_f32 v177, v49, v177\n"
"v_mul_f32 v178, v50, v178\n"
"v_mfma_f32_16x16x32_bf16 v[184:187], a[84:87], a[104:107], v[184:187]\n"
"v_mul_f32 v179, v51, v179\n"
"v_mul_f32 v16, s90, v16\n"
"v_mfma_f32_16x16x32_bf16 v[28:31], a[64:67], a[108:111], v[28:31]\n"
"v_mul_f32 v17, s90, v17\n"
"v_mul_f32 v18, s90, v18\n"
"v_mfma_f32_16x16x32_bf16 v[60:63], a[68:71], a[108:111], v[60:63]\n"
"v_mul_f32 v19, s90, v19\n"
"v_mul_f32 v48, s90, v48\n"
"v_mfma_f32_16x16x32_bf16 v[156:159], a[80:83], a[108:111], v[156:159]\n"
"v_mul_f32 v49, s90, v49\n"
"v_mul_f32 v50, s90, v50\n"
"v_mfma_f32_16x16x32_bf16 v[188:191], a[84:87], a[108:111], v[188:191]\n"
"v_mul_f32 v51, s90, v51\n"
"v_mfma_f32_16x16x32_bf16 v[32:35], a[64:67], a[112:115], v[32:35]\n"
"v_exp_f32 v16, v16\n"
"v_mfma_f32_16x16x32_bf16 v[64:67], a[68:71], a[112:115], v[64:67]\n"
"v_exp_f32 v17, v17\n"
"v_mfma_f32_16x16x32_bf16 v[160:163], a[80:83], a[112:115], v[160:163]\n"
"v_exp_f32 v18, v18\n"
"v_mfma_f32_16x16x32_bf16 v[192:195], a[84:87], a[112:115], v[192:195]\n"
"v_exp_f32 v19, v19\n"
"v_mfma_f32_16x16x32_bf16 v[36:39], a[64:67], a[116:119], v[36:39]\n"
"v_exp_f32 v48, v48\n"
"v_mfma_f32_16x16x32_bf16 v[68:71], a[68:71], a[116:119], v[68:71]\n"
"v_exp_f32 v49, v49\n"
"v_mfma_f32_16x16x32_bf16 v[164:167], a[80:83], a[116:119], v[164:167]\n"
"v_exp_f32 v50, v50\n"
"v_mfma_f32_16x16x32_bf16 v[196:199], a[84:87], a[116:119], v[196:199]\n"
"v_exp_f32 v51, v51\n"
"v_mfma_f32_16x16x32_bf16 v[40:43], a[64:67], a[120:123], v[40:43]\n"
"v_add_f32 v16, 1.0, v16\n"
"v_add_f32 v17, 1.0, v17\n"
"v_mfma_f32_16x16x32_bf16 v[72:75], a[68:71], a[120:123], v[72:75]\n"
"v_add_f32 v18, 1.0, v18\n"
"v_add_f32 v19, 1.0, v19\n"
"v_mfma_f32_16x16x32_bf16 v[168:171], a[80:83], a[120:123], v[168:171]\n"
"v_add_f32 v48, 1.0, v48\n"
"v_add_f32 v49, 1.0, v49\n"
"v_mfma_f32_16x16x32_bf16 v[200:203], a[84:87], a[120:123], v[200:203]\n"
"v_add_f32 v50, 1.0, v50\n"
"v_add_f32 v51, 1.0, v51\n"
"v_mfma_f32_16x16x32_bf16 v[44:47], a[64:67], a[124:127], v[44:47]\n"
"v_rcp_f32 v16, v16\n"
"v_mfma_f32_16x16x32_bf16 v[76:79], a[68:71], a[124:127], v[76:79]\n"
"v_rcp_f32 v17, v17\n"
"v_mfma_f32_16x16x32_bf16 v[172:175], a[80:83], a[124:127], v[172:175]\n"
"v_rcp_f32 v18, v18\n"
"v_mfma_f32_16x16x32_bf16 v[204:207], a[84:87], a[124:127], v[204:207]\n"
"v_rcp_f32 v19, v19\n"
"v_mfma_f32_16x16x32_bf16 v[80:83], a[72:75], a[96:99], v[80:83]\n"
"v_rcp_f32 v48, v48\n"
"v_mfma_f32_16x16x32_bf16 v[112:115], a[76:79], a[96:99], v[112:115]\n"
"v_rcp_f32 v49, v49\n"
"v_mfma_f32_16x16x32_bf16 v[208:211], a[88:91], a[96:99], v[208:211]\n"
"v_rcp_f32 v50, v50\n"
"v_mfma_f32_16x16x32_bf16 v[240:243], a[92:95], a[96:99], v[240:243]\n"
"v_rcp_f32 v51, v51\n"
"v_mfma_f32_16x16x32_bf16 v[84:87], a[72:75], a[100:103], v[84:87]\n"
"v_mul_f32 v144, v16, v144\n"
"v_mul_f32 v145, v17, v145\n"
"v_mfma_f32_16x16x32_bf16 v[116:119], a[76:79], a[100:103], v[116:119]\n"
"v_mul_f32 v146, v18, v146\n"
"v_mul_f32 v147, v19, v147\n"
"v_mfma_f32_16x16x32_bf16 v[212:215], a[88:91], a[100:103], v[212:215]\n"
"v_mul_f32 v176, v48, v176\n"
"v_mul_f32 v177, v49, v177\n"
"v_mfma_f32_16x16x32_bf16 v[244:247], a[92:95], a[100:103], v[244:247]\n"
"v_mul_f32 v178, v50, v178\n"
"v_mul_f32 v179, v51, v179\n"
"v_mfma_f32_16x16x32_bf16 v[88:91], a[72:75], a[104:107], v[88:91]\n"
"v_cvt_pk_bf16_f32 v16, v144, v145\n"
"v_cvt_pk_bf16_f32 v17, v146, v147\n"
"v_mfma_f32_16x16x32_bf16 v[120:123], a[76:79], a[104:107], v[120:123]\n"
"v_cvt_pk_bf16_f32 v18, v176, v177\n"
"v_cvt_pk_bf16_f32 v19, v178, v179\n"
"v_mfma_f32_16x16x32_bf16 v[216:219], a[88:91], a[104:107], v[216:219]\n"
"buffer_store_dwordx4 v[16:19], v10, s[44:47], s56 offen offset:0 sc1\n"
"v_mul_f32 v148, v20, v148\n"
"v_mfma_f32_16x16x32_bf16 v[248:251], a[92:95], a[104:107], v[248:251]\n"
"v_mul_f32 v149, v21, v149\n"
"v_mul_f32 v150, v22, v150\n"
"v_mfma_f32_16x16x32_bf16 v[92:95], a[72:75], a[108:111], v[92:95]\n"
"v_mul_f32 v151, v23, v151\n"
"v_mul_f32 v180, v52, v180\n"
"v_mfma_f32_16x16x32_bf16 v[124:127], a[76:79], a[108:111], v[124:127]\n"
"v_mul_f32 v181, v53, v181\n"
"v_mul_f32 v182, v54, v182\n"
"v_mfma_f32_16x16x32_bf16 v[220:223], a[88:91], a[108:111], v[220:223]\n"
"v_mul_f32 v183, v55, v183\n"
"v_mul_f32 v20, s90, v20\n"
"v_mfma_f32_16x16x32_bf16 v[252:255], a[92:95], a[108:111], v[252:255]\n"
"v_mul_f32 v21, s90, v21\n"
"v_mul_f32 v22, s90, v22\n"
"v_mfma_f32_16x16x32_bf16 v[96:99], a[72:75], a[112:115], v[96:99]\n"
"v_mul_f32 v23, s90, v23\n"
"v_mul_f32 v52, s90, v52\n"
"v_mfma_f32_16x16x32_bf16 v[128:131], a[76:79], a[112:115], v[128:131]\n"
"v_mul_f32 v53, s90, v53\n"
"v_mul_f32 v54, s90, v54\n"
"v_mfma_f32_16x16x32_bf16 v[224:227], a[88:91], a[112:115], v[224:227]\n"
"v_mul_f32 v55, s90, v55\n"
"v_mfma_f32_16x16x32_bf16 a[128:131], a[92:95], a[112:115], a[128:131]\n"
"v_exp_f32 v20, v20\n"
"v_mfma_f32_16x16x32_bf16 v[100:103], a[72:75], a[116:119], v[100:103]\n"
"v_exp_f32 v21, v21\n"
"v_mfma_f32_16x16x32_bf16 v[132:135], a[76:79], a[116:119], v[132:135]\n"
"v_exp_f32 v22, v22\n"
"v_mfma_f32_16x16x32_bf16 v[228:231], a[88:91], a[116:119], v[228:231]\n"
"v_exp_f32 v23, v23\n"
"v_mfma_f32_16x16x32_bf16 a[132:135], a[92:95], a[116:119], a[132:135]\n"
"v_exp_f32 v52, v52\n"
"v_mfma_f32_16x16x32_bf16 v[104:107], a[72:75], a[120:123], v[104:107]\n"
"v_exp_f32 v53, v53\n"
"v_mfma_f32_16x16x32_bf16 v[136:139], a[76:79], a[120:123], v[136:139]\n"
"v_exp_f32 v54, v54\n"
"v_mfma_f32_16x16x32_bf16 v[232:235], a[88:91], a[120:123], v[232:235]\n"
"v_exp_f32 v55, v55\n"
"v_mfma_f32_16x16x32_bf16 a[136:139], a[92:95], a[120:123], a[136:139]\n"
"v_add_f32 v20, 1.0, v20\n"
"v_add_f32 v21, 1.0, v21\n"
"v_mfma_f32_16x16x32_bf16 v[108:111], a[72:75], a[124:127], v[108:111]\n"
"v_add_f32 v22, 1.0, v22\n"
"v_add_f32 v23, 1.0, v23\n"
"v_mfma_f32_16x16x32_bf16 v[140:143], a[76:79], a[124:127], v[140:143]\n"
"v_add_f32 v52, 1.0, v52\n"
"v_add_f32 v53, 1.0, v53\n"
"v_mfma_f32_16x16x32_bf16 v[236:239], a[88:91], a[124:127], v[236:239]\n"
"v_add_f32 v54, 1.0, v54\n"
"v_add_f32 v55, 1.0, v55\n"
"v_mfma_f32_16x16x32_bf16 a[140:143], a[92:95], a[124:127], a[140:143]\n"
"v_rcp_f32 v20, v20\n"
"s_nop 7\n"
"v_rcp_f32 v21, v21\n"
"v_rcp_f32 v22, v22\n"
"v_rcp_f32 v23, v23\n"
"v_rcp_f32 v52, v52\n"
"v_rcp_f32 v53, v53\n"
"v_rcp_f32 v54, v54\n"
"v_rcp_f32 v55, v55\n"
"v_mul_f32 v148, v20, v148\n"
"v_mul_f32 v149, v21, v149\n"
"v_mul_f32 v150, v22, v150\n"
"v_mul_f32 v151, v23, v151\n"
"v_mul_f32 v180, v52, v180\n"
"v_mul_f32 v181, v53, v181\n"
"v_mul_f32 v182, v54, v182\n"
"v_mul_f32 v183, v55, v183\n"
"v_cvt_pk_bf16_f32 v20, v148, v149\n"
"v_cvt_pk_bf16_f32 v21, v150, v151\n"
"v_cvt_pk_bf16_f32 v22, v180, v181\n"
"v_cvt_pk_bf16_f32 v23, v182, v183\n"
"buffer_store_dwordx4 v[20:23], v10, s[44:47], s56 offen offset:1024 sc1\n"
"v_pk_mul_f32 v[152:153], v[24:25], v[152:153]\n"
"v_pk_mul_f32 v[154:155], v[26:27], v[154:155]\n"
"v_pk_mul_f32 v[184:185], v[56:57], v[184:185]\n"
"v_pk_mul_f32 v[186:187], v[58:59], v[186:187]\n"
"v_pk_mul_f32 v[24:25], v[24:25], s[90:91]\n"
"v_pk_mul_f32 v[26:27], v[26:27], s[90:91]\n"
"v_pk_mul_f32 v[56:57], v[56:57], s[90:91]\n"
"v_pk_mul_f32 v[58:59], v[58:59], s[90:91]\n"
"v_exp_f32 v24, v24\n"
"v_exp_f32 v25, v25\n"
"v_exp_f32 v26, v26\n"
"v_exp_f32 v27, v27\n"
"v_exp_f32 v56, v56\n"
"v_exp_f32 v57, v57\n"
"v_exp_f32 v58, v58\n"
"v_exp_f32 v59, v59\n"
"v_pk_add_f32 v[24:25], v[24:25], s[92:93]\n"
"v_pk_add_f32 v[26:27], v[26:27], s[92:93]\n"
"v_pk_add_f32 v[56:57], v[56:57], s[92:93]\n"
"v_pk_add_f32 v[58:59], v[58:59], s[92:93]\n"
"v_rcp_f32 v24, v24\n"
"v_rcp_f32 v25, v25\n"
"v_rcp_f32 v26, v26\n"
"v_rcp_f32 v27, v27\n"
"v_rcp_f32 v56, v56\n"
"v_rcp_f32 v57, v57\n"
"v_rcp_f32 v58, v58\n"
"v_rcp_f32 v59, v59\n"
"v_pk_mul_f32 v[152:153], v[24:25], v[152:153]\n"
"v_pk_mul_f32 v[154:155], v[26:27], v[154:155]\n"
"v_pk_mul_f32 v[184:185], v[56:57], v[184:185]\n"
"v_pk_mul_f32 v[186:187], v[58:59], v[186:187]\n"
"v_cvt_pk_bf16_f32 v24, v152, v153\n"
"v_cvt_pk_bf16_f32 v25, v154, v155\n"
"v_cvt_pk_bf16_f32 v26, v184, v185\n"
"v_cvt_pk_bf16_f32 v27, v186, v187\n"
"buffer_store_dwordx4 v[24:27], v10, s[44:47], s56 offen offset:2048 sc1\n"
"v_pk_mul_f32 v[156:157], v[28:29], v[156:157]\n"
"v_pk_mul_f32 v[158:159], v[30:31], v[158:159]\n"
"v_pk_mul_f32 v[188:189], v[60:61], v[188:189]\n"
"v_pk_mul_f32 v[190:191], v[62:63], v[190:191]\n"
"v_pk_mul_f32 v[28:29], v[28:29], s[90:91]\n"
"v_pk_mul_f32 v[30:31], v[30:31], s[90:91]\n"
"v_pk_mul_f32 v[60:61], v[60:61], s[90:91]\n"
"v_pk_mul_f32 v[62:63], v[62:63], s[90:91]\n"
"v_exp_f32 v28, v28\n"
"v_exp_f32 v29, v29\n"
"v_exp_f32 v30, v30\n"
"v_exp_f32 v31, v31\n"
"v_exp_f32 v60, v60\n"
"v_exp_f32 v61, v61\n"
"v_exp_f32 v62, v62\n"
"v_exp_f32 v63, v63\n"
"v_pk_add_f32 v[28:29], v[28:29], s[92:93]\n"
"v_pk_add_f32 v[30:31], v[30:31], s[92:93]\n"
"v_pk_add_f32 v[60:61], v[60:61], s[92:93]\n"
"v_pk_add_f32 v[62:63], v[62:63], s[92:93]\n"
"v_rcp_f32 v28, v28\n"
"v_rcp_f32 v29, v29\n"
"v_rcp_f32 v30, v30\n"
"v_rcp_f32 v31, v31\n"
"v_rcp_f32 v60, v60\n"
"v_rcp_f32 v61, v61\n"
"v_rcp_f32 v62, v62\n"
"v_rcp_f32 v63, v63\n"
"v_pk_mul_f32 v[156:157], v[28:29], v[156:157]\n"
"v_pk_mul_f32 v[158:159], v[30:31], v[158:159]\n"
"v_pk_mul_f32 v[188:189], v[60:61], v[188:189]\n"
"v_pk_mul_f32 v[190:191], v[62:63], v[190:191]\n"
"v_cvt_pk_bf16_f32 v28, v156, v157\n"
"v_cvt_pk_bf16_f32 v29, v158, v159\n"
"v_cvt_pk_bf16_f32 v30, v188, v189\n"
"v_cvt_pk_bf16_f32 v31, v190, v191\n"
"buffer_store_dwordx4 v[28:31], v10, s[44:47], s56 offen offset:3072 sc1\n"
"v_pk_mul_f32 v[160:161], v[32:33], v[160:161]\n"
"v_pk_mul_f32 v[162:163], v[34:35], v[162:163]\n"
"v_pk_mul_f32 v[192:193], v[64:65], v[192:193]\n"
"v_pk_mul_f32 v[194:195], v[66:67], v[194:195]\n"
"v_pk_mul_f32 v[32:33], v[32:33], s[90:91]\n"
"v_pk_mul_f32 v[34:35], v[34:35], s[90:91]\n"
"v_pk_mul_f32 v[64:65], v[64:65], s[90:91]\n"
"v_pk_mul_f32 v[66:67], v[66:67], s[90:91]\n"
"v_exp_f32 v32, v32\n"
"v_exp_f32 v33, v33\n"
"v_exp_f32 v34, v34\n"
"v_exp_f32 v35, v35\n"
"v_exp_f32 v64, v64\n"
"v_exp_f32 v65, v65\n"
"v_exp_f32 v66, v66\n"
"v_exp_f32 v67, v67\n"
"v_pk_add_f32 v[32:33], v[32:33], s[92:93]\n"
"v_pk_add_f32 v[34:35], v[34:35], s[92:93]\n"
"v_pk_add_f32 v[64:65], v[64:65], s[92:93]\n"
"v_pk_add_f32 v[66:67], v[66:67], s[92:93]\n"
"v_rcp_f32 v32, v32\n"
"v_rcp_f32 v33, v33\n"
"v_rcp_f32 v34, v34\n"
"v_rcp_f32 v35, v35\n"
"v_rcp_f32 v64, v64\n"
"v_rcp_f32 v65, v65\n"
"v_rcp_f32 v66, v66\n"
"v_rcp_f32 v67, v67\n"
"v_pk_mul_f32 v[160:161], v[32:33], v[160:161]\n"
"v_pk_mul_f32 v[162:163], v[34:35], v[162:163]\n"
"v_pk_mul_f32 v[192:193], v[64:65], v[192:193]\n"
"v_pk_mul_f32 v[194:195], v[66:67], v[194:195]\n"
"v_cvt_pk_bf16_f32 v32, v160, v161\n"
"v_cvt_pk_bf16_f32 v33, v162, v163\n"
"v_cvt_pk_bf16_f32 v34, v192, v193\n"
"v_cvt_pk_bf16_f32 v35, v194, v195\n"
"buffer_store_dwordx4 v[32:35], v10, s[44:47], s57 offen offset:0 sc1\n"
"v_pk_mul_f32 v[164:165], v[36:37], v[164:165]\n"
"v_pk_mul_f32 v[166:167], v[38:39], v[166:167]\n"
"v_pk_mul_f32 v[196:197], v[68:69], v[196:197]\n"
"v_pk_mul_f32 v[198:199], v[70:71], v[198:199]\n"
"v_pk_mul_f32 v[36:37], v[36:37], s[90:91]\n"
"v_pk_mul_f32 v[38:39], v[38:39], s[90:91]\n"
"v_pk_mul_f32 v[68:69], v[68:69], s[90:91]\n"
"v_pk_mul_f32 v[70:71], v[70:71], s[90:91]\n"
"v_exp_f32 v36, v36\n"
"v_exp_f32 v37, v37\n"
"v_exp_f32 v38, v38\n"
"v_exp_f32 v39, v39\n"
"v_exp_f32 v68, v68\n"
"v_exp_f32 v69, v69\n"
"v_exp_f32 v70, v70\n"
"v_exp_f32 v71, v71\n"
"v_pk_add_f32 v[36:37], v[36:37], s[92:93]\n"
"v_pk_add_f32 v[38:39], v[38:39], s[92:93]\n"
"v_pk_add_f32 v[68:69], v[68:69], s[92:93]\n"
"v_pk_add_f32 v[70:71], v[70:71], s[92:93]\n"
"v_rcp_f32 v36, v36\n"
"v_rcp_f32 v37, v37\n"
"v_rcp_f32 v38, v38\n"
"v_rcp_f32 v39, v39\n"
"v_rcp_f32 v68, v68\n"
"v_rcp_f32 v69, v69\n"
"v_rcp_f32 v70, v70\n"
"v_rcp_f32 v71, v71\n"
"v_pk_mul_f32 v[164:165], v[36:37], v[164:165]\n"
"v_pk_mul_f32 v[166:167], v[38:39], v[166:167]\n"
"v_pk_mul_f32 v[196:197], v[68:69], v[196:197]\n"
"v_pk_mul_f32 v[198:199], v[70:71], v[198:199]\n"
"v_cvt_pk_bf16_f32 v36, v164, v165\n"
"v_cvt_pk_bf16_f32 v37, v166, v167\n"
"v_cvt_pk_bf16_f32 v38, v196, v197\n"
"v_cvt_pk_bf16_f32 v39, v198, v199\n"
"buffer_store_dwordx4 v[36:39], v10, s[44:47], s57 offen offset:1024 sc1\n"
"v_pk_mul_f32 v[168:169], v[40:41], v[168:169]\n"
"v_pk_mul_f32 v[170:171], v[42:43], v[170:171]\n"
"v_pk_mul_f32 v[200:201], v[72:73], v[200:201]\n"
"v_pk_mul_f32 v[202:203], v[74:75], v[202:203]\n"
"v_pk_mul_f32 v[40:41], v[40:41], s[90:91]\n"
"v_pk_mul_f32 v[42:43], v[42:43], s[90:91]\n"
"v_pk_mul_f32 v[72:73], v[72:73], s[90:91]\n"
"v_pk_mul_f32 v[74:75], v[74:75], s[90:91]\n"
"v_exp_f32 v40, v40\n"
"v_exp_f32 v41, v41\n"
"v_exp_f32 v42, v42\n"
"v_exp_f32 v43, v43\n"
"v_exp_f32 v72, v72\n"
"v_exp_f32 v73, v73\n"
"v_exp_f32 v74, v74\n"
"v_exp_f32 v75, v75\n"
"v_pk_add_f32 v[40:41], v[40:41], s[92:93]\n"
"v_pk_add_f32 v[42:43], v[42:43], s[92:93]\n"
"v_pk_add_f32 v[72:73], v[72:73], s[92:93]\n"
"v_pk_add_f32 v[74:75], v[74:75], s[92:93]\n"
"v_rcp_f32 v40, v40\n"
"v_rcp_f32 v41, v41\n"
"v_rcp_f32 v42, v42\n"
"v_rcp_f32 v43, v43\n"
"v_rcp_f32 v72, v72\n"
"v_rcp_f32 v73, v73\n"
"v_rcp_f32 v74, v74\n"
"v_rcp_f32 v75, v75\n"
"v_pk_mul_f32 v[168:169], v[40:41], v[168:169]\n"
"v_pk_mul_f32 v[170:171], v[42:43], v[170:171]\n"
"v_pk_mul_f32 v[200:201], v[72:73], v[200:201]\n"
"v_pk_mul_f32 v[202:203], v[74:75], v[202:203]\n"
"v_cvt_pk_bf16_f32 v40, v168, v169\n"
"v_cvt_pk_bf16_f32 v41, v170, v171\n"
"v_cvt_pk_bf16_f32 v42, v200, v201\n"
"v_cvt_pk_bf16_f32 v43, v202, v203\n"
"buffer_store_dwordx4 v[40:43], v10, s[44:47], s57 offen offset:2048 sc1\n"
"v_pk_mul_f32 v[172:173], v[44:45], v[172:173]\n"
"v_pk_mul_f32 v[174:175], v[46:47], v[174:175]\n"
"v_pk_mul_f32 v[204:205], v[76:77], v[204:205]\n"
"v_pk_mul_f32 v[206:207], v[78:79], v[206:207]\n"
"v_pk_mul_f32 v[44:45], v[44:45], s[90:91]\n"
"v_pk_mul_f32 v[46:47], v[46:47], s[90:91]\n"
"v_pk_mul_f32 v[76:77], v[76:77], s[90:91]\n"
"v_pk_mul_f32 v[78:79], v[78:79], s[90:91]\n"
"v_exp_f32 v44, v44\n"
"v_exp_f32 v45, v45\n"
"v_exp_f32 v46, v46\n"
"v_exp_f32 v47, v47\n"
"v_exp_f32 v76, v76\n"
"v_exp_f32 v77, v77\n"
"v_exp_f32 v78, v78\n"
"v_exp_f32 v79, v79\n"
"v_pk_add_f32 v[44:45], v[44:45], s[92:93]\n"
"v_pk_add_f32 v[46:47], v[46:47], s[92:93]\n"
"v_pk_add_f32 v[76:77], v[76:77], s[92:93]\n"
"v_pk_add_f32 v[78:79], v[78:79], s[92:93]\n"
"v_rcp_f32 v44, v44\n"
"v_rcp_f32 v45, v45\n"
"v_rcp_f32 v46, v46\n"
"v_rcp_f32 v47, v47\n"
"v_rcp_f32 v76, v76\n"
"v_rcp_f32 v77, v77\n"
"v_rcp_f32 v78, v78\n"
"v_rcp_f32 v79, v79\n"
"v_pk_mul_f32 v[172:173], v[44:45], v[172:173]\n"
"v_pk_mul_f32 v[174:175], v[46:47], v[174:175]\n"
"v_pk_mul_f32 v[204:205], v[76:77], v[204:205]\n"
"v_pk_mul_f32 v[206:207], v[78:79], v[206:207]\n"
"v_cvt_pk_bf16_f32 v44, v172, v173\n"
"v_cvt_pk_bf16_f32 v45, v174, v175\n"
"v_cvt_pk_bf16_f32 v46, v204, v205\n"
"v_cvt_pk_bf16_f32 v47, v206, v207\n"
"buffer_store_dwordx4 v[44:47], v10, s[44:47], s57 offen offset:3072 sc1\n"
"v_pk_mul_f32 v[208:209], v[80:81], v[208:209]\n"
"v_pk_mul_f32 v[210:211], v[82:83], v[210:211]\n"
"v_pk_mul_f32 v[240:241], v[112:113], v[240:241]\n"
"v_pk_mul_f32 v[242:243], v[114:115], v[242:243]\n"
"v_pk_mul_f32 v[80:81], v[80:81], s[90:91]\n"
"v_pk_mul_f32 v[82:83], v[82:83], s[90:91]\n"
"v_pk_mul_f32 v[112:113], v[112:113], s[90:91]\n"
"v_pk_mul_f32 v[114:115], v[114:115], s[90:91]\n"
"v_exp_f32 v80, v80\n"
"v_exp_f32 v81, v81\n"
"v_exp_f32 v82, v82\n"
"v_exp_f32 v83, v83\n"
"v_exp_f32 v112, v112\n"
"v_exp_f32 v113, v113\n"
"v_exp_f32 v114, v114\n"
"v_exp_f32 v115, v115\n"
"v_pk_add_f32 v[80:81], v[80:81], s[92:93]\n"
"v_pk_add_f32 v[82:83], v[82:83], s[92:93]\n"
"v_pk_add_f32 v[112:113], v[112:113], s[92:93]\n"
"v_pk_add_f32 v[114:115], v[114:115], s[92:93]\n"
"v_rcp_f32 v80, v80\n"
"v_rcp_f32 v81, v81\n"
"v_rcp_f32 v82, v82\n"
"v_rcp_f32 v83, v83\n"
"v_rcp_f32 v112, v112\n"
"v_rcp_f32 v113, v113\n"
"v_rcp_f32 v114, v114\n"
"v_rcp_f32 v115, v115\n"
"v_pk_mul_f32 v[208:209], v[80:81], v[208:209]\n"
"v_pk_mul_f32 v[210:211], v[82:83], v[210:211]\n"
"v_pk_mul_f32 v[240:241], v[112:113], v[240:241]\n"
"v_pk_mul_f32 v[242:243], v[114:115], v[242:243]\n"
"v_cvt_pk_bf16_f32 v80, v208, v209\n"
"v_cvt_pk_bf16_f32 v81, v210, v211\n"
"v_cvt_pk_bf16_f32 v82, v240, v241\n"
"v_cvt_pk_bf16_f32 v83, v242, v243\n"
"buffer_store_dwordx4 v[80:83], v10, s[44:47], s58 offen offset:0 sc1\n"
"v_accvgpr_read_b32 v32, a196\n"
"v_accvgpr_read_b32 v33, a197\n"
"v_accvgpr_read_b32 v34, a198\n"
"v_accvgpr_read_b32 v35, a199\n"
"v_accvgpr_read_b32 v36, a200\n"
"v_accvgpr_read_b32 v37, a201\n"
"v_accvgpr_read_b32 v38, a202\n"
"v_accvgpr_read_b32 v39, a203\n"
"v_accvgpr_read_b32 v40, a204\n"
"v_accvgpr_read_b32 v41, a205\n"
"v_accvgpr_read_b32 v42, a206\n"
"v_accvgpr_read_b32 v43, a207\n"
"v_accvgpr_read_b32 v44, a208\n"
"v_accvgpr_read_b32 v45, a209\n"
"v_accvgpr_read_b32 v46, a210\n"
"v_accvgpr_read_b32 v47, a211\n"
"v_accvgpr_read_b32 v48, a212\n"
"v_accvgpr_read_b32 v49, a213\n"
"v_accvgpr_read_b32 v50, a214\n"
"v_accvgpr_read_b32 v51, a215\n"
"v_accvgpr_read_b32 v52, a216\n"
"v_accvgpr_read_b32 v53, a217\n"
"v_accvgpr_read_b32 v54, a218\n"
"v_accvgpr_read_b32 v55, a219\n"
"v_xor_b32 v40, s64, v40\n"
"v_xor_b32 v41, s64, v41\n"
"v_xor_b32 v42, s64, v42\n"
"v_xor_b32 v43, s64, v43\n"
"v_xor_b32 v44, s64, v44\n"
"v_xor_b32 v45, s64, v45\n"
"v_xor_b32 v46, s64, v46\n"
"v_xor_b32 v47, s64, v47\n"
"v_add_u32 v56, s79, v38\n"
"v_writelane_b32 v56, s70, 32\n"
"v_pk_mul_f32 v[212:213], v[84:85], v[212:213]\n"
"v_pk_mul_f32 v[214:215], v[86:87], v[214:215]\n"
"v_pk_mul_f32 v[244:245], v[116:117], v[244:245]\n"
"v_pk_mul_f32 v[246:247], v[118:119], v[246:247]\n"
"v_pk_mul_f32 v[84:85], v[84:85], s[90:91]\n"
"v_pk_mul_f32 v[86:87], v[86:87], s[90:91]\n"
"v_pk_mul_f32 v[116:117], v[116:117], s[90:91]\n"
"v_pk_mul_f32 v[118:119], v[118:119], s[90:91]\n"
"v_exp_f32 v84, v84\n"
"v_exp_f32 v85, v85\n"
"v_exp_f32 v86, v86\n"
"v_exp_f32 v87, v87\n"
"v_exp_f32 v116, v116\n"
"v_exp_f32 v117, v117\n"
"v_exp_f32 v118, v118\n"
"v_exp_f32 v119, v119\n"
"v_pk_add_f32 v[84:85], v[84:85], s[92:93]\n"
"v_pk_add_f32 v[86:87], v[86:87], s[92:93]\n"
"v_pk_add_f32 v[116:117], v[116:117], s[92:93]\n"
"v_pk_add_f32 v[118:119], v[118:119], s[92:93]\n"
"v_rcp_f32 v84, v84\n"
"v_rcp_f32 v85, v85\n"
"v_rcp_f32 v86, v86\n"
"v_rcp_f32 v87, v87\n"
"v_rcp_f32 v116, v116\n"
"v_rcp_f32 v117, v117\n"
"v_rcp_f32 v118, v118\n"
"v_rcp_f32 v119, v119\n"
"v_pk_mul_f32 v[212:213], v[84:85], v[212:213]\n"
"v_pk_mul_f32 v[214:215], v[86:87], v[214:215]\n"
"v_pk_mul_f32 v[244:245], v[116:117], v[244:245]\n"
"v_pk_mul_f32 v[246:247], v[118:119], v[246:247]\n"
"v_cvt_pk_bf16_f32 v84, v212, v213\n"
"v_cvt_pk_bf16_f32 v85, v214, v215\n"
"v_cvt_pk_bf16_f32 v86, v244, v245\n"
"v_cvt_pk_bf16_f32 v87, v246, v247\n"
"buffer_store_dwordx4 v[84:87], v10, s[44:47], s58 offen offset:1024 sc1\n"
"v_pk_mul_f32 v[216:217], v[88:89], v[216:217]\n"
"v_pk_mul_f32 v[218:219], v[90:91], v[218:219]\n"
"v_pk_mul_f32 v[248:249], v[120:121], v[248:249]\n"
"v_pk_mul_f32 v[250:251], v[122:123], v[250:251]\n"
"v_pk_mul_f32 v[88:89], v[88:89], s[90:91]\n"
"v_pk_mul_f32 v[90:91], v[90:91], s[90:91]\n"
"v_pk_mul_f32 v[120:121], v[120:121], s[90:91]\n"
"v_pk_mul_f32 v[122:123], v[122:123], s[90:91]\n"
"v_exp_f32 v88, v88\n"
"v_exp_f32 v89, v89\n"
"v_exp_f32 v90, v90\n"
"v_exp_f32 v91, v91\n"
"v_exp_f32 v120, v120\n"
"v_exp_f32 v121, v121\n"
"v_exp_f32 v122, v122\n"
"v_exp_f32 v123, v123\n"
"v_pk_add_f32 v[88:89], v[88:89], s[92:93]\n"
"v_pk_add_f32 v[90:91], v[90:91], s[92:93]\n"
"v_pk_add_f32 v[120:121], v[120:121], s[92:93]\n"
"v_pk_add_f32 v[122:123], v[122:123], s[92:93]\n"
"v_rcp_f32 v88, v88\n"
"v_rcp_f32 v89, v89\n"
"v_rcp_f32 v90, v90\n"
"v_rcp_f32 v91, v91\n"
"v_rcp_f32 v120, v120\n"
"v_rcp_f32 v121, v121\n"
"v_rcp_f32 v122, v122\n"
"v_rcp_f32 v123, v123\n"
"v_pk_mul_f32 v[216:217], v[88:89], v[216:217]\n"
"v_pk_mul_f32 v[218:219], v[90:91], v[218:219]\n"
"v_pk_mul_f32 v[248:249], v[120:121], v[248:249]\n"
"v_pk_mul_f32 v[250:251], v[122:123], v[250:251]\n"
"v_cvt_pk_bf16_f32 v88, v216, v217\n"
"v_cvt_pk_bf16_f32 v89, v218, v219\n"
"v_cvt_pk_bf16_f32 v90, v248, v249\n"
"v_cvt_pk_bf16_f32 v91, v250, v251\n"
"buffer_store_dwordx4 v[88:91], v10, s[44:47], s58 offen offset:2048 sc1\n"
"s_waitcnt vmcnt(3)\n"
"v_lshrrev_b32 v11, 1, v10\n"
"s_mov_b64 exec, 0xffff\n"
"buffer_store_dwordx2 v[12:13], v11, s[28:31], s82 offen sc1\n"
"s_mov_b64 exec, -1\n"
"s_mov_b64 exec, 0xffff\n"
"buffer_store_dwordx2 v[12:13], v11, s[28:31], s69 offen sc1\n"
"s_mov_b64 exec, -1\n"
"v_pk_mul_f32 v[220:221], v[92:93], v[220:221]\n"
"v_pk_mul_f32 v[222:223], v[94:95], v[222:223]\n"
"v_pk_mul_f32 v[252:253], v[124:125], v[252:253]\n"
"v_pk_mul_f32 v[254:255], v[126:127], v[254:255]\n"
"v_pk_mul_f32 v[92:93], v[92:93], s[90:91]\n"
"v_pk_mul_f32 v[94:95], v[94:95], s[90:91]\n"
"v_pk_mul_f32 v[124:125], v[124:125], s[90:91]\n"
"v_pk_mul_f32 v[126:127], v[126:127], s[90:91]\n"
"v_exp_f32 v92, v92\n"
"v_exp_f32 v93, v93\n"
"v_exp_f32 v94, v94\n"
"v_exp_f32 v95, v95\n"
"v_exp_f32 v124, v124\n"
"v_exp_f32 v125, v125\n"
"v_exp_f32 v126, v126\n"
"v_exp_f32 v127, v127\n"
"v_pk_add_f32 v[92:93], v[92:93], s[92:93]\n"
"v_pk_add_f32 v[94:95], v[94:95], s[92:93]\n"
"v_pk_add_f32 v[124:125], v[124:125], s[92:93]\n"
"v_pk_add_f32 v[126:127], v[126:127], s[92:93]\n"
"v_rcp_f32 v92, v92\n"
"v_rcp_f32 v93, v93\n"
"v_rcp_f32 v94, v94\n"
"v_rcp_f32 v95, v95\n"
"v_rcp_f32 v124, v124\n"
"v_rcp_f32 v125, v125\n"
"v_rcp_f32 v126, v126\n"
"v_rcp_f32 v127, v127\n"
"v_pk_mul_f32 v[220:221], v[92:93], v[220:221]\n"
"v_pk_mul_f32 v[222:223], v[94:95], v[222:223]\n"
"v_pk_mul_f32 v[252:253], v[124:125], v[252:253]\n"
"v_pk_mul_f32 v[254:255], v[126:127], v[254:255]\n"
"v_cvt_pk_bf16_f32 v92, v220, v221\n"
"v_cvt_pk_bf16_f32 v93, v222, v223\n"
"v_cvt_pk_bf16_f32 v94, v252, v253\n"
"v_cvt_pk_bf16_f32 v95, v254, v255\n"
"buffer_store_dwordx4 v[92:95], v10, s[44:47], s58 offen offset:3072 sc1\n"
"v_accvgpr_read_b32 v16, a128\n"
"v_accvgpr_read_b32 v17, a129\n"
"v_accvgpr_read_b32 v18, a130\n"
"v_accvgpr_read_b32 v19, a131\n"
"v_pk_mul_f32 v[224:225], v[96:97], v[224:225]\n"
"v_pk_mul_f32 v[226:227], v[98:99], v[226:227]\n"
"v_pk_mul_f32 v[16:17], v[128:129], v[16:17]\n"
"v_pk_mul_f32 v[18:19], v[130:131], v[18:19]\n"
"v_pk_mul_f32 v[96:97], v[96:97], s[90:91]\n"
"v_pk_mul_f32 v[98:99], v[98:99], s[90:91]\n"
"v_pk_mul_f32 v[128:129], v[128:129], s[90:91]\n"
"v_pk_mul_f32 v[130:131], v[130:131], s[90:91]\n"
"v_exp_f32 v96, v96\n"
"v_exp_f32 v97, v97\n"
"v_exp_f32 v98, v98\n"
"v_exp_f32 v99, v99\n"
"v_exp_f32 v128, v128\n"
"v_exp_f32 v129, v129\n"
"v_exp_f32 v130, v130\n"
"v_exp_f32 v131, v131\n"
"v_pk_add_f32 v[96:97], v[96:97], s[92:93]\n"
"v_pk_add_f32 v[98:99], v[98:99], s[92:93]\n"
"v_pk_add_f32 v[128:129], v[128:129], s[92:93]\n"
"v_pk_add_f32 v[130:131], v[130:131], s[92:93]\n"
"v_rcp_f32 v96, v96\n"
"v_rcp_f32 v97, v97\n"
"v_rcp_f32 v98, v98\n"
"v_rcp_f32 v99, v99\n"
"v_rcp_f32 v128, v128\n"
"v_rcp_f32 v129, v129\n"
"v_rcp_f32 v130, v130\n"
"v_rcp_f32 v131, v131\n"
"v_pk_mul_f32 v[224:225], v[96:97], v[224:225]\n"
"v_pk_mul_f32 v[226:227], v[98:99], v[226:227]\n"
"v_pk_mul_f32 v[16:17], v[128:129], v[16:17]\n"
"v_pk_mul_f32 v[18:19], v[130:131], v[18:19]\n"
"v_cvt_pk_bf16_f32 v224, v224, v225\n"
"v_cvt_pk_bf16_f32 v225, v226, v227\n"
"v_cvt_pk_bf16_f32 v226, v16, v17\n"
"v_cvt_pk_bf16_f32 v227, v18, v19\n"
"v_accvgpr_read_b32 v20, a132\n"
"v_accvgpr_read_b32 v21, a133\n"
"v_accvgpr_read_b32 v22, a134\n"
"v_accvgpr_read_b32 v23, a135\n"
"v_pk_mul_f32 v[228:229], v[100:101], v[228:229]\n"
"v_pk_mul_f32 v[230:231], v[102:103], v[230:231]\n"
"v_pk_mul_f32 v[20:21], v[132:133], v[20:21]\n"
"v_pk_mul_f32 v[22:23], v[134:135], v[22:23]\n"
"v_pk_mul_f32 v[100:101], v[100:101], s[90:91]\n"
"v_pk_mul_f32 v[102:103], v[102:103], s[90:91]\n"
"v_pk_mul_f32 v[132:133], v[132:133], s[90:91]\n"
"v_pk_mul_f32 v[134:135], v[134:135], s[90:91]\n"
"v_exp_f32 v100, v100\n"
"v_exp_f32 v101, v101\n"
"v_exp_f32 v102, v102\n"
"v_exp_f32 v103, v103\n"
"v_exp_f32 v132, v132\n"
"v_exp_f32 v133, v133\n"
"v_exp_f32 v134, v134\n"
"v_exp_f32 v135, v135\n"
"v_pk_add_f32 v[100:101], v[100:101], s[92:93]\n"
"v_pk_add_f32 v[102:103], v[102:103], s[92:93]\n"
"v_pk_add_f32 v[132:133], v[132:133], s[92:93]\n"
"v_pk_add_f32 v[134:135], v[134:135], s[92:93]\n"
"v_rcp_f32 v100, v100\n"
"v_rcp_f32 v101, v101\n"
"v_rcp_f32 v102, v102\n"
"v_rcp_f32 v103, v103\n"
"v_rcp_f32 v132, v132\n"
"v_rcp_f32 v133, v133\n"
"v_rcp_f32 v134, v134\n"
"v_rcp_f32 v135, v135\n"
"v_pk_mul_f32 v[228:229], v[100:101], v[228:229]\n"
"v_pk_mul_f32 v[230:231], v[102:103], v[230:231]\n"
"v_pk_mul_f32 v[20:21], v[132:133], v[20:21]\n"
"v_pk_mul_f32 v[22:23], v[134:135], v[22:23]\n"
"v_cvt_pk_bf16_f32 v228, v228, v229\n"
"v_cvt_pk_bf16_f32 v229, v230, v231\n"
"v_cvt_pk_bf16_f32 v230, v20, v21\n"
"v_cvt_pk_bf16_f32 v231, v22, v23\n"
"buffer_load_dwordx2 v[14:15], v38, s[28:31], s77 offen sc1\n"
"v_accvgpr_read_b32 v24, a136\n"
"v_accvgpr_read_b32 v25, a137\n"
"v_accvgpr_read_b32 v26, a138\n"
"v_accvgpr_read_b32 v27, a139\n"
"v_pk_mul_f32 v[232:233], v[104:105], v[232:233]\n"
"v_pk_mul_f32 v[234:235], v[106:107], v[234:235]\n"
"v_pk_mul_f32 v[24:25], v[136:137], v[24:25]\n"
"v_pk_mul_f32 v[26:27], v[138:139], v[26:27]\n"
"v_pk_mul_f32 v[104:105], v[104:105], s[90:91]\n"
"v_pk_mul_f32 v[106:107], v[106:107], s[90:91]\n"
"v_pk_mul_f32 v[136:137], v[136:137], s[90:91]\n"
"v_pk_mul_f32 v[138:139], v[138:139], s[90:91]\n"
"v_exp_f32 v104, v104\n"
"v_exp_f32 v105, v105\n"
"v_exp_f32 v106, v106\n"
"v_exp_f32 v107, v107\n"
"v_exp_f32 v136, v136\n"
"v_exp_f32 v137, v137\n"
"v_exp_f32 v138, v138\n"
"v_exp_f32 v139, v139\n"
"v_pk_add_f32 v[104:105], v[104:105], s[92:93]\n"
"v_pk_add_f32 v[106:107], v[106:107], s[92:93]\n"
"v_pk_add_f32 v[136:137], v[136:137], s[92:93]\n"
"v_pk_add_f32 v[138:139], v[138:139], s[92:93]\n"
"v_rcp_f32 v104, v104\n"
"v_rcp_f32 v105, v105\n"
"v_rcp_f32 v106, v106\n"
"v_rcp_f32 v107, v107\n"
"v_rcp_f32 v136, v136\n"
"v_rcp_f32 v137, v137\n"
"v_rcp_f32 v138, v138\n"
"v_rcp_f32 v139, v139\n"
"v_pk_mul_f32 v[232:233], v[104:105], v[232:233]\n"
"v_pk_mul_f32 v[234:235], v[106:107], v[234:235]\n"
"v_pk_mul_f32 v[24:25], v[136:137], v[24:25]\n"
"v_pk_mul_f32 v[26:27], v[138:139], v[26:27]\n"
"v_cvt_pk_bf16_f32 v232, v232, v233\n"
"v_cvt_pk_bf16_f32 v233, v234, v235\n"
"v_cvt_pk_bf16_f32 v234, v24, v25\n"
"v_cvt_pk_bf16_f32 v235, v26, v27\n"
"v_accvgpr_read_b32 v28, a140\n"
"v_accvgpr_read_b32 v29, a141\n"
"v_accvgpr_read_b32 v30, a142\n"
"v_accvgpr_read_b32 v31, a143\n"
"v_pk_mul_f32 v[236:237], v[108:109], v[236:237]\n"
"v_pk_mul_f32 v[238:239], v[110:111], v[238:239]\n"
"v_pk_mul_f32 v[28:29], v[140:141], v[28:29]\n"
"v_pk_mul_f32 v[30:31], v[142:143], v[30:31]\n"
"v_pk_mul_f32 v[108:109], v[108:109], s[90:91]\n"
"v_pk_mul_f32 v[110:111], v[110:111], s[90:91]\n"
"v_pk_mul_f32 v[140:141], v[140:141], s[90:91]\n"
"v_pk_mul_f32 v[142:143], v[142:143], s[90:91]\n"
"v_exp_f32 v108, v108\n"
"v_exp_f32 v109, v109\n"
"v_exp_f32 v110, v110\n"
"v_exp_f32 v111, v111\n"
"v_exp_f32 v140, v140\n"
"v_exp_f32 v141, v141\n"
"v_exp_f32 v142, v142\n"
"v_exp_f32 v143, v143\n"
"v_pk_add_f32 v[108:109], v[108:109], s[92:93]\n"
"v_pk_add_f32 v[110:111], v[110:111], s[92:93]\n"
"v_pk_add_f32 v[140:141], v[140:141], s[92:93]\n"
"v_pk_add_f32 v[142:143], v[142:143], s[92:93]\n"
"v_rcp_f32 v108, v108\n"
"v_rcp_f32 v109, v109\n"
"v_rcp_f32 v110, v110\n"
"v_rcp_f32 v111, v111\n"
"v_rcp_f32 v140, v140\n"
"v_rcp_f32 v141, v141\n"
"v_rcp_f32 v142, v142\n"
"v_rcp_f32 v143, v143\n"
"v_pk_mul_f32 v[236:237], v[108:109], v[236:237]\n"
"v_pk_mul_f32 v[238:239], v[110:111], v[238:239]\n"
"v_pk_mul_f32 v[28:29], v[140:141], v[28:29]\n"
"v_pk_mul_f32 v[30:31], v[142:143], v[30:31]\n"
"v_cvt_pk_bf16_f32 v236, v236, v237\n"
"v_cvt_pk_bf16_f32 v237, v238, v239\n"
"v_cvt_pk_bf16_f32 v238, v28, v29\n"
"v_cvt_pk_bf16_f32 v239, v30, v31\n"
"s_mov_b32 s75, s38\n"
"s_mov_b32 s84, 0x0fffffff\n"
"s_mov_b32 s85, 0\n"
"s_waitcnt vmcnt(0)\n"
"v_cmp_eq_u64 vcc, v[14:15], v[12:13]\n"
"s_nop 1\n"
"s_andn2_b64 s[86:87], s[84:85], vcc\n"
"s_cbranch_scc1 .Lg1slow\n"
".Lg1back:\n"
"s_xor_b32 s75, s38, 0\n"
"buffer_load_dwordx4 a[64:67], v52, s[48:51], s75 offen offset:0 sc1\n"
"buffer_load_dwordx4 a[68:71], v52, s[48:51], s75 offen offset:1024 sc1\n"
"buffer_load_dwordx4 a[72:75], v36, s[48:51], s75 offen offset:0 sc1\n"
"buffer_load_dwordx4 a[76:79], v36, s[48:51], s75 offen offset:1024 sc1\n"
"s_xor_b32 s75, s38, 32768\n"
"buffer_load_dwordx4 a[80:83], v52, s[48:51], s75 offen offset:0 sc1\n"
"buffer_load_dwordx4 a[84:87], v52, s[48:51], s75 offen offset:1024 sc1\n"
"buffer_load_dwordx4 a[88:91], v36, s[48:51], s75 offen offset:0 sc1\n"
"buffer_load_dwordx4 a[92:95], v36, s[48:51], s75 offen offset:1024 sc1\n"
"s_xor_b32 s75, s38, 65536\n"
"buffer_load_dwordx4 a[96:99], v52, s[48:51], s75 offen offset:0 sc1\n"
"buffer_load_dwordx4 a[100:103], v52, s[48:51], s75 offen offset:1024 sc1\n"
"buffer_load_dwordx4 a[104:107], v36, s[48:51], s75 offen offset:0 sc1\n"
"buffer_load_dwordx4 a[108:111], v36, s[48:51], s75 offen offset:1024 sc1\n"
".Lg2entry:\n"
"s_xor_b32 s75, s38, 98304\n"
"s_xor_b32 s74, s39, 1048576\n"
"s_mov_b32 s73, 98304\n"
"s_mov_b32 s83, 1048576\n"
"s_mov_b32 s78, s88\n"
"s_mov_b32 s76, 6\n"
"s_barrier\n"
"ds_read_b64_tr_b16 a[0:1], v40 offset:16384\n"
"ds_read_b64_tr_b16 a[2:3], v40 offset:20480\n"
"ds_read_b64_tr_b16 a[4:5], v41 offset:16384\n"
"ds_read_b64_tr_b16 a[6:7], v41 offset:20480\n"
"ds_read_b64_tr_b16 a[8:9], v42 offset:16384\n"
"ds_read_b64_tr_b16 a[10:11], v42 offset:20480\n"
"ds_read_b64_tr_b16 a[12:13], v43 offset:16384\n"
"ds_read_b64_tr_b16 a[14:15], v43 offset:20480\n"
"ds_read_b64_tr_b16 a[16:17], v44 offset:16384\n"
"ds_read_b64_tr_b16 a[18:19], v44 offset:20480\n"
"ds_read_b64_tr_b16 a[20:21], v45 offset:16384\n"
"ds_read_b64_tr_b16 a[22:23], v45 offset:20480\n"
"ds_read_b64_tr_b16 a[24:25], v46 offset:16384\n"
"ds_read_b64_tr_b16 a[26:27], v46 offset:20480\n"
"ds_read_b64_tr_b16 a[28:29], v47 offset:16384\n"
"ds_read_b64_tr_b16 a[30:31], v47 offset:20480\n"
"s_waitcnt vmcnt(8)\n"
"s_waitcnt lgkmcnt(8)\n"
"v_mfma_f32_16x16x32_bf16 v[64:67], a[0:3], a[64:67], 0\n"
"ds_read_b64_tr_b16 a[32:33], v40 offset:24576\n"
"v_mfma_f32_16x16x32_bf16 v[68:71], a[0:3], a[68:71], 0\n"
"ds_read_b64_tr_b16 a[34:35], v40 offset:28672\n"
"v_mfma_f32_16x16x32_bf16 v[76:79], a[4:7], a[68:71], 0\n"
"ds_read_b64_tr_b16 a[36:37], v41 offset:24576\n"
"v_mfma_f32_16x16x32_bf16 v[72:75], a[4:7], a[64:67], 0\n"
"ds_read_b64_tr_b16 a[38:39], v41 offset:28672\n"
"v_mfma_f32_16x16x32_bf16 v[80:83], a[8:11], a[64:67], 0\n"
"ds_read_b64_tr_b16 a[40:41], v42 offset:24576\n"
"v_mfma_f32_16x16x32_bf16 v[84:87], a[8:11], a[68:71], 0\n"
"ds_read_b64_tr_b16 a[42:43], v42 offset:28672\n"
"v_mfma_f32_16x16x32_bf16 v[92:95], a[12:15], a[68:71], 0\n"
"ds_read_b64_tr_b16 a[44:45], v43 offset:24576\n"
"v_mfma_f32_16x16x32_bf16 v[88:91], a[12:15], a[64:67], 0\n"
"ds_read_b64_tr_b16 a[46:47], v43 offset:28672\n"
"s_waitcnt lgkmcnt(8)\n"
"v_mfma_f32_16x16x32_bf16 v[96:99], a[16:19], a[64:67], 0\n"
"ds_read_b64_tr_b16 a[48:49], v44 offset:24576\n"
"v_mfma_f32_16x16x32_bf16 v[100:103], a[16:19], a[68:71], 0\n"
"ds_read_b64_tr_b16 a[50:51], v44 offset:28672\n"
"v_mfma_f32_16x16x32_bf16 v[108:111], a[20:23], a[68:71], 0\n"
"ds_read_b64_tr_b16 a[52:53], v45 offset:24576\n"
"v_mfma_f32_16x16x32_bf16 v[104:107], a[20:23], a[64:67], 0\n"
"ds_read_b64_tr_b16 a[54:55], v45 offset:28672\n"
"v_mfma_f32_16x16x32_bf16 v[112:115], a[24:27], a[64:67], 0\n"
"ds_read_b64_tr_b16 a[56:57], v46 offset:24576\n"
"v_mfma_f32_16x16x32_bf16 v[116:119], a[24:27], a[68:71], 0\n"
"ds_read_b64_tr_b16 a[58:59], v46 offset:28672\n"
"v_mfma_f32_16x16x32_bf16 v[124:127], a[28:31], a[68:71], 0\n"
"ds_read_b64_tr_b16 a[60:61], v47 offset:24576\n"
"v_mfma_f32_16x16x32_bf16 v[120:123], a[28:31], a[64:67], 0\n"
"ds_read_b64_tr_b16 a[62:63], v47 offset:28672\n"
".Lg2pub:\n"
"s_waitcnt vmcnt(4)\n"
".Lg2go:\n"
"s_waitcnt lgkmcnt(8)\n"
"v_mfma_f32_16x16x32_bf16 v[64:67], a[32:35], a[72:75], v[64:67]\n"
"ds_read_b64_tr_b16 a[0:1], v40 offset:32768\n"
"s_add_u32 m0, s78, 16384\n"
"v_mfma_f32_16x16x32_bf16 v[68:71], a[32:35], a[76:79], v[68:71]\n"
"ds_read_b64_tr_b16 a[2:3], v40 offset:36864\n"
"buffer_load_dwordx4 v32, s[40:43], s74 offen offset:0 sc1 lds\n"
"v_mfma_f32_16x16x32_bf16 v[76:79], a[36:39], a[76:79], v[76:79]\n"
"ds_read_b64_tr_b16 a[4:5], v41 offset:32768\n"
"s_add_u32 s83, s83, 0x40000\n"
"buffer_store_dwordx4 v[224:227], v10, s[44:47], s59 offen offset:0 sc1\n"
"v_mfma_f32_16x16x32_bf16 v[72:75], a[36:39], a[72:75], v[72:75]\n"
"ds_read_b64_tr_b16 a[6:7], v41 offset:36864\n"
"buffer_store_dwordx4 v[228:231], v10, s[44:47], s59 offen offset:1024 sc1\n"
"v_mfma_f32_16x16x32_bf16 v[80:83], a[40:43], a[72:75], v[80:83]\n"
"ds_read_b64_tr_b16 a[8:9], v42 offset:32768\n"
"v_mfma_f32_16x16x32_bf16 v[84:87], a[40:43], a[76:79], v[84:87]\n"
"ds_read_b64_tr_b16 a[10:11], v42 offset:36864\n"
"buffer_load_dwordx4 a[112:115], v52, s[48:51], s75 offen offset:0 sc1\n"
"v_mfma_f32_16x16x32_bf16 v[92:95], a[44:47], a[76:79], v[92:95]\n"
"ds_read_b64_tr_b16 a[12:13], v43 offset:32768\n"
"s_add_u32 s73, s73, 0x8000\n"
"buffer_store_dwordx4 v[232:235], v10, s[44:47], s59 offen offset:2048 sc1\n"
"v_mfma_f32_16x16x32_bf16 v[88:91], a[44:47], a[72:75], v[88:91]\n"
"ds_read_b64_tr_b16 a[14:15], v43 offset:36864\n"
"buffer_store_dwordx4 v[236:239], v10, s[44:47], s59 offen offset:3072 sc1\n"
"s_waitcnt lgkmcnt(8)\n"
"v_mfma_f32_16x16x32_bf16 v[96:99], a[48:51], a[72:75], v[96:99]\n"
"ds_read_b64_tr_b16 a[16:17], v44 offset:32768\n"
"v_mfma_f32_16x16x32_bf16 v[100:103], a[48:51], a[76:79], v[100:103]\n"
"ds_read_b64_tr_b16 a[18:19], v44 offset:36864\n"
"buffer_load_dwordx4 v33, s[40:43], s74 offen offset:1024 sc1 lds\n"
"v_mfma_f32_16x16x32_bf16 v[108:111], a[52:55], a[76:79], v[108:111]\n"
"ds_read_b64_tr_b16 a[20:21], v45 offset:32768\n"
"v_mfma_f32_16x16x32_bf16 v[104:107], a[52:55], a[72:75], v[104:107]\n"
"ds_read_b64_tr_b16 a[22:23], v45 offset:36864\n"
"v_mfma_f32_16x16x32_bf16 v[112:115], a[56:59], a[72:75], v[112:115]\n"
"ds_read_b64_tr_b16 a[24:25], v46 offset:32768\n"
"v_mfma_f32_16x16x32_bf16 v[116:119], a[56:59], a[76:79], v[116:119]\n"
"ds_read_b64_tr_b16 a[26:27], v46 offset:36864\n"
"buffer_load_dwordx4 a[116:119], v52, s[48:51], s75 offen offset:1024 sc1\n"
"v_mfma_f32_16x16x32_bf16 v[124:127], a[60:63], a[76:79], v[124:127]\n"
"ds_read_b64_tr_b16 a[28:29], v47 offset:32768\n"
"v_mfma_f32_16x16x32_bf16 v[120:123], a[60:63], a[72:75], v[120:123]\n"
"ds_read_b64_tr_b16 a[30:31], v47 offset:36864\n"
"s_waitcnt lgkmcnt(8)\n"
"v_mfma_f32_16x16x32_bf16 v[64:67], a[0:3], a[80:83], v[64:67]\n"
"ds_read_b64_tr_b16 a[32:33], v40 offset:40960\n"
"v_mfma_f32_16x16x32_bf16 v[68:71], a[0:3], a[84:87], v[68:71]\n"
"ds_read_b64_tr_b16 a[34:35], v40 offset:45056\n"
"buffer_load_dwordx4 v34, s[40:43], s74 offen offset:2048 sc1 lds\n"
"v_mfma_f32_16x16x32_bf16 v[76:79], a[4:7], a[84:87], v[76:79]\n"
"ds_read_b64_tr_b16 a[36:37], v41 offset:40960\n"
"v_mfma_f32_16x16x32_bf16 v[72:75], a[4:7], a[80:83], v[72:75]\n"
"ds_read_b64_tr_b16 a[38:39], v41 offset:45056\n"
"v_mfma_f32_16x16x32_bf16 v[80:83], a[8:11], a[80:83], v[80:83]\n"
"ds_read_b64_tr_b16 a[40:41], v42 offset:40960\n"
"v_mfma_f32_16x16x32_bf16 v[84:87], a[8:11], a[84:87], v[84:87]\n"
"ds_read_b64_tr_b16 a[42:43], v42 offset:45056\n"
"buffer_load_dwordx4 a[120:123], v36, s[48:51], s75 offen offset:0 sc1\n"
"v_mfma_f32_16x16x32_bf16 v[92:95], a[12:15], a[84:87], v[92:95]\n"
"ds_read_b64_tr_b16 a[44:45], v43 offset:40960\n"
"v_mfma_f32_16x16x32_bf16 v[88:91], a[12:15], a[80:83], v[88:91]\n"
"ds_read_b64_tr_b16 a[46:47], v43 offset:45056\n"
"s_waitcnt lgkmcnt(8)\n"
"v_mfma_f32_16x16x32_bf16 v[96:99], a[16:19], a[80:83], v[96:99]\n"
"ds_read_b64_tr_b16 a[48:49], v44 offset:40960\n"
"v_mfma_f32_16x16x32_bf16 v[100:103], a[16:19], a[84:87], v[100:103]\n"
"ds_read_b64_tr_b16 a[50:51], v44 offset:45056\n"
"buffer_load_dwordx4 v35, s[40:43], s74 offen offset:3072 sc1 lds\n"
"v_mfma_f32_16x16x32_bf16 v[108:111], a[20:23], a[84:87], v[108:111]\n"
"ds_read_b64_tr_b16 a[52:53], v45 offset:40960\n"
"s_xor_b32 s74, s83, s39\n"
"v_mfma_f32_16x16x32_bf16 v[104:107], a[20:23], a[80:83], v[104:107]\n"
"ds_read_b64_tr_b16 a[54:55], v45 offset:45056\n"
"v_mfma_f32_16x16x32_bf16 v[112:115], a[24:27], a[80:83], v[112:115]\n"
"ds_read_b64_tr_b16 a[56:57], v46 offset:40960\n"
"buffer_load_dwordx4 a[124:127], v36, s[48:51], s75 offen offset:1024 sc1\n"
"v_mfma_f32_16x16x32_bf16 v[116:119], a[24:27], a[84:87], v[116:119]\n"
"ds_read_b64_tr_b16 a[58:59], v46 offset:45056\n"
"s_xor_b32 s75, s73, s38\n"
"v_mfma_f32_16x16x32_bf16 v[124:127], a[28:31], a[84:87], v[124:127]\n"
"ds_read_b64_tr_b16 a[60:61], v47 offset:40960\n"
"v_mfma_f32_16x16x32_bf16 v[120:123], a[28:31], a[80:83], v[120:123]\n"
"ds_read_b64_tr_b16 a[62:63], v47 offset:45056\n"
"s_waitcnt vmcnt(12)\n"
"s_waitcnt lgkmcnt(8)\n"
"v_mfma_f32_16x16x32_bf16 v[64:67], a[32:35], a[88:91], v[64:67]\n"
"ds_read_b64_tr_b16 a[0:1], v40 offset:49152\n"
"s_add_u32 m0, s78, 32768\n"
"v_mfma_f32_16x16x32_bf16 v[68:71], a[32:35], a[92:95], v[68:71]\n"
"ds_read_b64_tr_b16 a[2:3], v40 offset:53248\n"
"buffer_load_dwordx4 v32, s[40:43], s74 offen offset:0 sc1 lds\n"
"v_mfma_f32_16x16x32_bf16 v[76:79], a[36:39], a[92:95], v[76:79]\n"
"ds_read_b64_tr_b16 a[4:5], v41 offset:49152\n"
"s_add_u32 s83, s83, 0x40000\n"
"v_mfma_f32_16x16x32_bf16 v[72:75], a[36:39], a[88:91], v[72:75]\n"
"ds_read_b64_tr_b16 a[6:7], v41 offset:53248\n"
"v_mfma_f32_16x16x32_bf16 v[80:83], a[40:43], a[88:91], v[80:83]\n"
"ds_read_b64_tr_b16 a[8:9], v42 offset:49152\n"
"s_cmp_eq_u32 s83, 3670016\n"
"s_cselect_b32 s83, 0x10000, s83\n"
"v_mfma_f32_16x16x32_bf16 v[84:87], a[40:43], a[92:95], v[84:87]\n"
"ds_read_b64_tr_b16 a[10:11], v42 offset:53248\n"
"buffer_load_dwordx4 a[64:67], v52, s[48:51], s75 offen offset:0 sc1\n"
"v_mfma_f32_16x16x32_bf16 v[92:95], a[44:47], a[92:95], v[92:95]\n"
"ds_read_b64_tr_b16 a[12:13], v43 offset:49152\n"
"s_add_u32 s73, s73, 0x8000\n"
"v_mfma_f32_16x16x32_bf16 v[88:91], a[44:47], a[88:91], v[88:91]\n"
"ds_read_b64_tr_b16 a[14:15], v43 offset:53248\n"
"s_waitcnt lgkmcnt(8)\n"
"v_mfma_f32_16x16x32_bf16 v[96:99], a[48:51], a[88:91], v[96:99]\n"
"ds_read_b64_tr_b16 a[16:17], v44 offset:49152\n"
"v_mfma_f32_16x16x32_bf16 v[100:103], a[48:51], a[92:95], v[100:103]\n"
"ds_read_b64_tr_b16 a[18:19], v44 offset:53248\n"
"buffer_load_dwordx4 v33, s[40:43], s74 offen offset:1024 sc1 lds\n"
"v_mfma_f32_16x16x32_bf16 v[108:111], a[52:55], a[92:95], v[108:111]\n"
"ds_read_b64_tr_b16 a[20:21], v45 offset:49152\n"
"v_mfma_f32_16x16x32_bf16 v[104:107], a[52:55], a[88:91], v[104:107]\n"
"ds_read_b64_tr_b16 a[22:23], v45 offset:53248\n"
"v_mfma_f32_16x16x32_bf16 v[112:115], a[56:59], a[88:91], v[112:115]\n"
"ds_read_b64_tr_b16 a[24:25], v46 offset:49152\n"
"v_mfma_f32_16x16x32_bf16 v[116:119], a[56:59], a[92:95], v[116:119]\n"
"ds_read_b64_tr_b16 a[26:27], v46 offset:53248\n"
"buffer_load_dwordx4 a[68:71], v52, s[48:51], s75 offen offset:1024 sc1\n"
"v_mfma_f32_16x16x32_bf16 v[124:127], a[60:63], a[92:95], v[124:127]\n"
"ds_read_b64_tr_b16 a[28:29], v47 offset:49152\n"
"v_mfma_f32_16x16x32_bf16 v[120:123], a[60:63], a[88:91], v[120:123]\n"
"ds_read_b64_tr_b16 a[30:31], v47 offset:53248\n"
"s_waitcnt lgkmcnt(8)\n"
"v_mfma_f32_16x16x32_bf16 v[64:67], a[0:3], a[96:99], v[64:67]\n"
"ds_read_b64_tr_b16 a[32:33], v40 offset:57344\n"
"v_mfma_f32_16x16x32_bf16 v[68:71], a[0:3], a[100:103], v[68:71]\n"
"ds_read_b64_tr_b16 a[34:35], v40 offset:61440\n"
"buffer_load_dwordx4 v34, s[40:43], s74 offen offset:2048 sc1 lds\n"
"v_mfma_f32_16x16x32_bf16 v[76:79], a[4:7], a[100:103], v[76:79]\n"
"ds_read_b64_tr_b16 a[36:37], v41 offset:57344\n"
"v_xor_b32 v40, 0x10000, v40\n"
"v_mfma_f32_16x16x32_bf16 v[72:75], a[4:7], a[96:99], v[72:75]\n"
"ds_read_b64_tr_b16 a[38:39], v41 offset:61440\n"
"v_mfma_f32_16x16x32_bf16 v[80:83], a[8:11], a[96:99], v[80:83]\n"
"ds_read_b64_tr_b16 a[40:41], v42 offset:57344\n"
"v_xor_b32 v41, 0x10000, v41\n"
"v_mfma_f32_16x16x32_bf16 v[84:87], a[8:11], a[100:103], v[84:87]\n"
"ds_read_b64_tr_b16 a[42:43], v42 offset:61440\n"
"buffer_load_dwordx4 a[72:75], v36, s[48:51], s75 offen offset:0 sc1\n"
"v_mfma_f32_16x16x32_bf16 v[92:95], a[12:15], a[100:103], v[92:95]\n"
"ds_read_b64_tr_b16 a[44:45], v43 offset:57344\n"
"v_xor_b32 v42, 0x10000, v42\n"
"v_mfma_f32_16x16x32_bf16 v[88:91], a[12:15], a[96:99], v[88:91]\n"
"ds_read_b64_tr_b16 a[46:47], v43 offset:61440\n"
"s_waitcnt lgkmcnt(8)\n"
"v_mfma_f32_16x16x32_bf16 v[96:99], a[16:19], a[96:99], v[96:99]\n"
"ds_read_b64_tr_b16 a[48:49], v44 offset:57344\n"
"v_xor_b32 v43, 0x10000, v43\n"
"v_mfma_f32_16x16x32_bf16 v[100:103], a[16:19], a[100:103], v[100:103]\n"
"ds_read_b64_tr_b16 a[50:51], v44 offset:61440\n"
"buffer_load_dwordx4 v35, s[40:43], s74 offen offset:3072 sc1 lds\n"
"v_mfma_f32_16x16x32_bf16 v[108:111], a[20:23], a[100:103], v[108:111]\n"
"ds_read_b64_tr_b16 a[52:53], v45 offset:57344\n"
"s_xor_b32 s74, s83, s39\n"
"v_xor_b32 v44, 0x10000, v44\n"
"v_mfma_f32_16x16x32_bf16 v[104:107], a[20:23], a[96:99], v[104:107]\n"
"ds_read_b64_tr_b16 a[54:55], v45 offset:61440\n"
"v_mfma_f32_16x16x32_bf16 v[112:115], a[24:27], a[96:99], v[112:115]\n"
"ds_read_b64_tr_b16 a[56:57], v46 offset:57344\n"
"buffer_load_dwordx4 a[76:79], v36, s[48:51], s75 offen offset:1024 sc1\n"
"v_xor_b32 v45, 0x10000, v45\n"
"v_mfma_f32_16x16x32_bf16 v[116:119], a[24:27], a[100:103], v[116:119]\n"
"ds_read_b64_tr_b16 a[58:59], v46 offset:61440\n"
"s_xor_b32 s75, s73, s38\n"
"v_mfma_f32_16x16x32_bf16 v[124:127], a[28:31], a[100:103], v[124:127]\n"
"ds_read_b64_tr_b16 a[60:61], v47 offset:57344\n"
"v_xor_b32 v46, 0x10000, v46\n"
"v_mfma_f32_16x16x32_bf16 v[120:123], a[28:31], a[96:99], v[120:123]\n"
"ds_read_b64_tr_b16 a[62:63], v47 offset:61440\n"
"v_xor_b32 v47, 0x10000, v47\n"
"s_waitcnt vmcnt(8)\n"
"s_barrier\n"
"s_waitcnt lgkmcnt(8)\n"
"v_mfma_f32_16x16x32_bf16 v[64:67], a[32:35], a[104:107], v[64:67]\n"
"ds_read_b64_tr_b16 a[0:1], v40 offset:0\n"
"s_add_u32 m0, s78, 49152\n"
"v_mfma_f32_16x16x32_bf16 v[68:71], a[32:35], a[108:111], v[68:71]\n"
"ds_read_b64_tr_b16 a[2:3], v40 offset:4096\n"
"buffer_load_dwordx4 v32, s[40:43], s74 offen offset:0 sc1 lds\n"
"v_mfma_f32_16x16x32_bf16 v[76:79], a[36:39], a[108:111], v[76:79]\n"
"ds_read_b64_tr_b16 a[4:5], v41 offset:0\n"
"s_add_u32 s83, s83, 0x40000\n"
"v_mfma_f32_16x16x32_bf16 v[72:75], a[36:39], a[104:107], v[72:75]\n"
"ds_read_b64_tr_b16 a[6:7], v41 offset:4096\n"
"v_mfma_f32_16x16x32_bf16 v[80:83], a[40:43], a[104:107], v[80:83]\n"
"ds_read_b64_tr_b16 a[8:9], v42 offset:0\n"
"v_mfma_f32_16x16x32_bf16 v[84:87], a[40:43], a[108:111], v[84:87]\n"
"ds_read_b64_tr_b16 a[10:11], v42 offset:4096\n"
"buffer_load_dwordx4 a[80:83], v52, s[48:51], s75 offen offset:0 sc1\n"
"v_mfma_f32_16x16x32_bf16 v[92:95], a[44:47], a[108:111], v[92:95]\n"
"ds_read_b64_tr_b16 a[12:13], v43 offset:0\n"
"s_add_u32 s73, s73, 0x8000\n"
"v_mfma_f32_16x16x32_bf16 v[88:91], a[44:47], a[104:107], v[88:91]\n"
"ds_read_b64_tr_b16 a[14:15], v43 offset:4096\n"
"s_waitcnt lgkmcnt(8)\n"
"v_mfma_f32_16x16x32_bf16 v[96:99], a[48:51], a[104:107], v[96:99]\n"
"ds_read_b64_tr_b16 a[16:17], v44 offset:0\n"
"v_mfma_f32_16x16x32_bf16 v[100:103], a[48:51], a[108:111], v[100:103]\n"
"ds_read_b64_tr_b16 a[18:19], v44 offset:4096\n"
"buffer_load_dwordx4 v33, s[40:43], s74 offen offset:1024 sc1 lds\n"
"v_mfma_f32_16x16x32_bf16 v[108:111], a[52:55], a[108:111], v[108:111]\n"
"ds_read_b64_tr_b16 a[20:21], v45 offset:0\n"
"s_cmp_eq_u32 s73, 458752\n"
"s_cselect_b32 s73, 0x2000, s73\n"
"v_mfma_f32_16x16x32_bf16 v[104:107], a[52:55], a[104:107], v[104:107]\n"
"ds_read_b64_tr_b16 a[22:23], v45 offset:4096\n"
"v_mfma_f32_16x16x32_bf16 v[112:115], a[56:59], a[104:107], v[112:115]\n"
"ds_read_b64_tr_b16 a[24:25], v46 offset:0\n"
"s_xor_b32 s78, s78, 0x10000\n"
"v_mfma_f32_16x16x32_bf16 v[116:119], a[56:59], a[108:111], v[116:119]\n"
"ds_read_b64_tr_b16 a[26:27], v46 offset:4096\n"
"buffer_load_dwordx4 a[84:87], v52, s[48:51], s75 offen offset:1024 sc1\n"
"v_mfma_f32_16x16x32_bf16 v[124:127], a[60:63], a[108:111], v[124:127]\n"
"ds_read_b64_tr_b16 a[28:29], v47 offset:0\n"
"v_mfma_f32_16x16x32_bf16 v[120:123], a[60:63], a[104:107], v[120:123]\n"
"ds_read_b64_tr_b16 a[30:31], v47 offset:4096\n"
"s_waitcnt lgkmcnt(8)\n"
"v_mfma_f32_16x16x32_bf16 v[64:67], a[0:3], a[112:115], v[64:67]\n"
"ds_read_b64_tr_b16 a[32:33], v40 offset:8192\n"
"v_mfma_f32_16x16x32_bf16 v[68:71], a[0:3], a[116:119], v[68:71]\n"
"ds_read_b64_tr_b16 a[34:35], v40 offset:12288\n"
"buffer_load_dwordx4 v34, s[40:43], s74 offen offset:2048 sc1 lds\n"
"v_mfma_f32_16x16x32_bf16 v[76:79], a[4:7], a[116:119], v[76:79]\n"
"ds_read_b64_tr_b16 a[36:37], v41 offset:8192\n"
"v_mfma_f32_16x16x32_bf16 v[72:75], a[4:7], a[112:115], v[72:75]\n"
"ds_read_b64_tr_b16 a[38:39], v41 offset:12288\n"
"v_mfma_f32_16x16x32_bf16 v[80:83], a[8:11], a[112:115], v[80:83]\n"
"ds_read_b64_tr_b16 a[40:41], v42 offset:8192\n"
"v_mfma_f32_16x16x32_bf16 v[84:87], a[8:11], a[116:119], v[84:87]\n"
"ds_read_b64_tr_b16 a[42:43], v42 offset:12288\n"
"buffer_load_dwordx4 a[88:91], v36, s[48:51], s75 offen offset:0 sc1\n"
"v_mfma_f32_16x16x32_bf16 v[92:95], a[12:15], a[116:119], v[92:95]\n"
"ds_read_b64_tr_b16 a[44:45], v43 offset:8192\n"
"v_mfma_f32_16x16x32_bf16 v[88:91], a[12:15], a[112:115], v[88:91]\n"
"ds_read_b64_tr_b16 a[46:47], v43 offset:12288\n"
"s_waitcnt lgkmcnt(8)\n"
"v_mfma_f32_16x16x32_bf16 v[96:99], a[16:19], a[112:115], v[96:99]\n"
"ds_read_b64_tr_b16 a[48:49], v44 offset:8192\n"
"v_mfma_f32_16x16x32_bf16 v[100:103], a[16:19], a[116:119], v[100:103]\n"
"ds_read_b64_tr_b16 a[50:51], v44 offset:12288\n"
"buffer_load_dwordx4 v35, s[40:43], s74 offen offset:3072 sc1 lds\n"
"v_mfma_f32_16x16x32_bf16 v[108:111], a[20:23], a[116:119], v[108:111]\n"
"ds_read_b64_tr_b16 a[52:53], v45 offset:8192\n"
"s_xor_b32 s74, s83, s39\n"
"v_mfma_f32_16x16x32_bf16 v[104:107], a[20:23], a[112:115], v[104:107]\n"
"ds_read_b64_tr_b16 a[54:55], v45 offset:12288\n"
"v_mfma_f32_16x16x32_bf16 v[112:115], a[24:27], a[112:115], v[112:115]\n"
"ds_read_b64_tr_b16 a[56:57], v46 offset:8192\n"
"buffer_load_dwordx4 a[92:95], v36, s[48:51], s75 offen offset:1024 sc1\n"
"v_mfma_f32_16x16x32_bf16 v[116:119], a[24:27], a[116:119], v[116:119]\n"
"ds_read_b64_tr_b16 a[58:59], v46 offset:12288\n"
"s_xor_b32 s75, s73, s38\n"
"v_mfma_f32_16x16x32_bf16 v[124:127], a[28:31], a[116:119], v[124:127]\n"
"ds_read_b64_tr_b16 a[60:61], v47 offset:8192\n"
"v_mfma_f32_16x16x32_bf16 v[120:123], a[28:31], a[112:115], v[120:123]\n"
"ds_read_b64_tr_b16 a[62:63], v47 offset:12288\n"
"s_waitcnt vmcnt(8)\n"
"s_waitcnt lgkmcnt(8)\n"
"v_mfma_f32_16x16x32_bf16 v[64:67], a[32:35], a[120:123], v[64:67]\n"
"ds_read_b64_tr_b16 a[0:1], v40 offset:16384\n"
"s_add_u32 m0, s78, 0\n"
"v_mfma_f32_16x16x32_bf16 v[68:71], a[32:35], a[124:127], v[68:71]\n"
"ds_read_b64_tr_b16 a[2:3], v40 offset:20480\n"
"buffer_load_dwordx4 v32, s[40:43], s74 offen offset:0 sc1 lds\n"
"v_mfma_f32_16x16x32_bf16 v[76:79], a[36:39], a[124:127], v[76:79]\n"
"ds_read_b64_tr_b16 a[4:5], v41 offset:16384\n"
"s_add_u32 s83, s83, 0x40000\n"
"v_mfma_f32_16x16x32_bf16 v[72:75], a[36:39], a[120:123], v[72:75]\n"
"ds_read_b64_tr_b16 a[6:7], v41 offset:20480\n"
"v_mfma_f32_16x16x32_bf16 v[80:83], a[40:43], a[120:123], v[80:83]\n"
"ds_read_b64_tr_b16 a[8:9], v42 offset:16384\n"
"v_mfma_f32_16x16x32_bf16 v[84:87], a[40:43], a[124:127], v[84:87]\n"
"ds_read_b64_tr_b16 a[10:11], v42 offset:20480\n"
"buffer_load_dwordx4 a[96:99], v52, s[48:51], s75 offen offset:0 sc1\n"
"v_mfma_f32_16x16x32_bf16 v[92:95], a[44:47], a[124:127], v[92:95]\n"
"ds_read_b64_tr_b16 a[12:13], v43 offset:16384\n"
"s_add_u32 s73, s73, 0x8000\n"
"v_mfma_f32_16x16x32_bf16 v[88:91], a[44:47], a[120:123], v[88:91]\n"
"ds_read_b64_tr_b16 a[14:15], v43 offset:20480\n"
"s_waitcnt lgkmcnt(8)\n"
"v_mfma_f32_16x16x32_bf16 v[96:99], a[48:51], a[120:123], v[96:99]\n"
"ds_read_b64_tr_b16 a[16:17], v44 offset:16384\n"
"v_mfma_f32_16x16x32_bf16 v[100:103], a[48:51], a[124:127], v[100:103]\n"
"ds_read_b64_tr_b16 a[18:19], v44 offset:20480\n"
"buffer_load_dwordx4 v33, s[40:43], s74 offen offset:1024 sc1 lds\n"
"v_mfma_f32_16x16x32_bf16 v[108:111], a[52:55], a[124:127], v[108:111]\n"
"ds_read_b64_tr_b16 a[20:21], v45 offset:16384\n"
"v_mfma_f32_16x16x32_bf16 v[104:107], a[52:55], a[120:123], v[104:107]\n"
"ds_read_b64_tr_b16 a[22:23], v45 offset:20480\n"
"v_mfma_f32_16x16x32_bf16 v[112:115], a[56:59], a[120:123], v[112:115]\n"
"ds_read_b64_tr_b16 a[24:25], v46 offset:16384\n"
"v_mfma_f32_16x16x32_bf16 v[116:119], a[56:59], a[124:127], v[116:119]\n"
"ds_read_b64_tr_b16 a[26:27], v46 offset:20480\n"
"buffer_load_dwordx4 a[100:103], v52, s[48:51], s75 offen offset:1024 sc1\n"
"v_mfma_f32_16x16x32_bf16 v[124:127], a[60:63], a[124:127], v[124:127]\n"
"ds_read_b64_tr_b16 a[28:29], v47 offset:16384\n"
"v_mfma_f32_16x16x32_bf16 v[120:123], a[60:63], a[120:123], v[120:123]\n"
"ds_read_b64_tr_b16 a[30:31], v47 offset:20480\n"
"s_waitcnt lgkmcnt(8)\n"
"v_mfma_f32_16x16x32_bf16 v[64:67], a[0:3], a[64:67], v[64:67]\n"
"ds_read_b64_tr_b16 a[32:33], v40 offset:24576\n"
"v_mfma_f32_16x16x32_bf16 v[68:71], a[0:3], a[68:71], v[68:71]\n"
"ds_read_b64_tr_b16 a[34:35], v40 offset:28672\n"
"buffer_load_dwordx4 v34, s[40:43], s74 offen offset:2048 sc1 lds\n"
"v_mfma_f32_16x16x32_bf16 v[76:79], a[4:7], a[68:71], v[76:79]\n"
"ds_read_b64_tr_b16 a[36:37], v41 offset:24576\n"
"v_mfma_f32_16x16x32_bf16 v[72:75], a[4:7], a[64:67], v[72:75]\n"
"ds_read_b64_tr_b16 a[38:39], v41 offset:28672\n"
"v_mfma_f32_16x16x32_bf16 v[80:83], a[8:11], a[64:67], v[80:83]\n"
"ds_read_b64_tr_b16 a[40:41], v42 offset:24576\n"
"v_mfma_f32_16x16x32_bf16 v[84:87], a[8:11], a[68:71], v[84:87]\n"
"ds_read_b64_tr_b16 a[42:43], v42 offset:28672\n"
"buffer_load_dwordx4 a[104:107], v36, s[48:51], s75 offen offset:0 sc1\n"
"v_mfma_f32_16x16x32_bf16 v[92:95], a[12:15], a[68:71], v[92:95]\n"
"ds_read_b64_tr_b16 a[44:45], v43 offset:24576\n"
"v_mfma_f32_16x16x32_bf16 v[88:91], a[12:15], a[64:67], v[88:91]\n"
"ds_read_b64_tr_b16 a[46:47], v43 offset:28672\n"
"s_waitcnt lgkmcnt(8)\n"
"v_mfma_f32_16x16x32_bf16 v[96:99], a[16:19], a[64:67], v[96:99]\n"
"ds_read_b64_tr_b16 a[48:49], v44 offset:24576\n"
"v_mfma_f32_16x16x32_bf16 v[100:103], a[16:19], a[68:71], v[100:103]\n"
"ds_read_b64_tr_b16 a[50:51], v44 offset:28672\n"
"buffer_load_dwordx4 v35, s[40:43], s74 offen offset:3072 sc1 lds\n"
"v_mfma_f32_16x16x32_bf16 v[108:111], a[20:23], a[68:71], v[108:111]\n"
"ds_read_b64_tr_b16 a[52:53], v45 offset:24576\n"
"s_xor_b32 s74, s83, s39\n"
"v_mfma_f32_16x16x32_bf16 v[104:107], a[20:23], a[64:67], v[104:107]\n"
"ds_read_b64_tr_b16 a[54:55], v45 offset:28672\n"
"v_mfma_f32_16x16x32_bf16 v[112:115], a[24:27], a[64:67], v[112:115]\n"
"ds_read_b64_tr_b16 a[56:57], v46 offset:24576\n"
"buffer_load_dwordx4 a[108:111], v36, s[48:51], s75 offen offset:1024 sc1\n"
"v_mfma_f32_16x16x32_bf16 v[116:119], a[24:27], a[68:71], v[116:119]\n"
"ds_read_b64_tr_b16 a[58:59], v46 offset:28672\n"
"s_xor_b32 s75, s73, s38\n"
"v_mfma_f32_16x16x32_bf16 v[124:127], a[28:31], a[68:71], v[124:127]\n"
"ds_read_b64_tr_b16 a[60:61], v47 offset:24576\n"
"v_mfma_f32_16x16x32_bf16 v[120:123], a[28:31], a[64:67], v[120:123]\n"
"ds_read_b64_tr_b16 a[62:63], v47 offset:28672\n"
"s_waitcnt vmcnt(8)\n"
"s_barrier\n"
"s_mov_b32 s76, 5\n"
"s_branch .Lg2loop\n"
".p2align 7\n"
"s_nop 0\n"
"s_nop 0\n"
"s_nop 0\n"
"s_nop 0\n"
"s_nop 0\n"
"s_nop 0\n"
"s_nop 0\n"
"s_nop 0\n"
"s_nop 0\n"
"s_nop 0\n"
"s_nop 0\n"
"s_nop 0\n"
"s_nop 0\n"
"s_nop 0\n"
"s_nop 0\n"
"s_nop 0\n"
".Lg2loop:\n"
"s_waitcnt lgkmcnt(8)\n"
"v_mfma_f32_16x16x32_bf16 v[64:67], a[32:35], a[72:75], v[64:67]\n"
"ds_read_b64_tr_b16 a[0:1], v40 offset:32768\n"
"s_add_u32 m0, s78, 16384\n"
"v_mfma_f32_16x16x32_bf16 v[68:71], a[32:35], a[76:79], v[68:71]\n"
"ds_read_b64_tr_b16 a[2:3], v40 offset:36864\n"
"buffer_load_dwordx4 v32, s[40:43], s74 offen offset:0 sc1 lds\n"
"v_mfma_f32_16x16x32_bf16 v[76:79], a[36:39], a[76:79], v[76:79]\n"
"ds_read_b64_tr_b16 a[4:5], v41 offset:32768\n"
"s_add_u32 s83, s83, 0x40000\n"
"v_mfma_f32_16x16x32_bf16 v[72:75], a[36:39], a[72:75], v[72:75]\n"
"ds_read_b64_tr_b16 a[6:7], v41 offset:36864\n"
"v_mfma_f32_16x16x32_bf16 v[80:83], a[40:43], a[72:75], v[80:83]\n"
"ds_read_b64_tr_b16 a[8:9], v42 offset:32768\n"
"v_mfma_f32_16x16x32_bf16 v[84:87], a[40:43], a[76:79], v[84:87]\n"
"ds_read_b64_tr_b16 a[10:11], v42 offset:36864\n"
"buffer_load_dwordx4 a[112:115], v52, s[48:51], s75 offen offset:0 sc1\n"
"v_mfma_f32_16x16x32_bf16 v[92:95], a[44:47], a[76:79], v[92:95]\n"
"ds_read_b64_tr_b16 a[12:13], v43 offset:32768\n"
"s_add_u32 s73, s73, 0x8000\n"
"v_mfma_f32_16x16x32_bf16 v[88:91], a[44:47], a[72:75], v[88:91]\n"
"ds_read_b64_tr_b16 a[14:15], v43 offset:36864\n"
"s_waitcnt lgkmcnt(8)\n"
"v_mfma_f32_16x16x32_bf16 v[96:99], a[48:51], a[72:75], v[96:99]\n"
"ds_read_b64_tr_b16 a[16:17], v44 offset:32768\n"
"v_mfma_f32_16x16x32_bf16 v[100:103], a[48:51], a[76:79], v[100:103]\n"
"ds_read_b64_tr_b16 a[18:19], v44 offset:36864\n"
"buffer_load_dwordx4 v33, s[40:43], s74 offen offset:1024 sc1 lds\n"
"v_mfma_f32_16x16x32_bf16 v[108:111], a[52:55], a[76:79], v[108:111]\n"
"ds_read_b64_tr_b16 a[20:21], v45 offset:32768\n"
"s_mov_b64 s[84:85], 0\n"
"v_mfma_f32_16x16x32_bf16 v[104:107], a[52:55], a[72:75], v[104:107]\n"
"ds_read_b64_tr_b16 a[22:23], v45 offset:36864\n"
"v_mfma_f32_16x16x32_bf16 v[112:115], a[56:59], a[72:75], v[112:115]\n"
"ds_read_b64_tr_b16 a[24:25], v46 offset:32768\n"
"v_mfma_f32_16x16x32_bf16 v[116:119], a[56:59], a[76:79], v[116:119]\n"
"ds_read_b64_tr_b16 a[26:27], v46 offset:36864\n"
"buffer_load_dwordx4 a[116:119], v52, s[48:51], s75 offen offset:1024 sc1\n"
"v_mfma_f32_16x16x32_bf16 v[124:127], a[60:63], a[76:79], v[124:127]\n"
"ds_read_b64_tr_b16 a[28:29], v47 offset:32768\n"
"v_mfma_f32_16x16x32_bf16 v[120:123], a[60:63], a[72:75], v[120:123]\n"
"ds_read_b64_tr_b16 a[30:31], v47 offset:36864\n"
"s_waitcnt lgkmcnt(8)\n"
"v_mfma_f32_16x16x32_bf16 v[64:67], a[0:3], a[80:83], v[64:67]\n"
"ds_read_b64_tr_b16 a[32:33], v40 offset:40960\n"
"v_mfma_f32_16x16x32_bf16 v[68:71], a[0:3], a[84:87], v[68:71]\n"
"ds_read_b64_tr_b16 a[34:35], v40 offset:45056\n"
"buffer_load_dwordx4 v34, s[40:43], s74 offen offset:2048 sc1 lds\n"
"v_mfma_f32_16x16x32_bf16 v[76:79], a[4:7], a[84:87], v[76:79]\n"
"ds_read_b64_tr_b16 a[36:37], v41 offset:40960\n"
"v_mfma_f32_16x16x32_bf16 v[72:75], a[4:7], a[80:83], v[72:75]\n"
"ds_read_b64_tr_b16 a[38:39], v41 offset:45056\n"
"v_mfma_f32_16x16x32_bf16 v[80:83], a[8:11], a[80:83], v[80:83]\n"
"ds_read_b64_tr_b16 a[40:41], v42 offset:40960\n"
"v_mfma_f32_16x16x32_bf16 v[84:87], a[8:11], a[84:87], v[84:87]\n"
"ds_read_b64_tr_b16 a[42:43], v42 offset:45056\n"
"buffer_load_dwordx4 a[120:123], v36, s[48:51], s75 offen offset:0 sc1\n"
"v_mfma_f32_16x16x32_bf16 v[92:95], a[12:15], a[84:87], v[92:95]\n"
"ds_read_b64_tr_b16 a[44:45], v43 offset:40960\n"
"v_mfma_f32_16x16x32_bf16 v[88:91], a[12:15], a[80:83], v[88:91]\n"
"ds_read_b64_tr_b16 a[46:47], v43 offset:45056\n"
"s_waitcnt lgkmcnt(8)\n"
"v_mfma_f32_16x16x32_bf16 v[96:99], a[16:19], a[80:83], v[96:99]\n"
"ds_read_b64_tr_b16 a[48:49], v44 offset:40960\n"
"v_mfma_f32_16x16x32_bf16 v[100:103], a[16:19], a[84:87], v[100:103]\n"
"ds_read_b64_tr_b16 a[50:51], v44 offset:45056\n"
"buffer_load_dwordx4 v35, s[40:43], s74 offen offset:3072 sc1 lds\n"
"v_mfma_f32_16x16x32_bf16 v[108:111], a[20:23], a[84:87], v[108:111]\n"
"ds_read_b64_tr_b16 a[52:53], v45 offset:40960\n"
"s_xor_b32 s74, s83, s39\n"
"v_mfma_f32_16x16x32_bf16 v[104:107], a[20:23], a[80:83], v[104:107]\n"
"ds_read_b64_tr_b16 a[54:55], v45 offset:45056\n"
"v_mfma_f32_16x16x32_bf16 v[112:115], a[24:27], a[80:83], v[112:115]\n"
"ds_read_b64_tr_b16 a[56:57], v46 offset:40960\n"
"buffer_load_dwordx4 a[124:127], v36, s[48:51], s75 offen offset:1024 sc1\n"
"v_mfma_f32_16x16x32_bf16 v[116:119], a[24:27], a[84:87], v[116:119]\n"
"ds_read_b64_tr_b16 a[58:59], v46 offset:45056\n"
"s_xor_b32 s75, s73, s38\n"
"v_mfma_f32_16x16x32_bf16 v[124:127], a[28:31], a[84:87], v[124:127]\n"
"ds_read_b64_tr_b16 a[60:61], v47 offset:40960\n"
"v_mfma_f32_16x16x32_bf16 v[120:123], a[28:31], a[80:83], v[120:123]\n"
"ds_read_b64_tr_b16 a[62:63], v47 offset:45056\n"
"s_waitcnt vmcnt(8)\n"
"s_waitcnt lgkmcnt(8)\n"
"v_mfma_f32_16x16x32_bf16 v[64:67], a[32:35], a[88:91], v[64:67]\n"
"ds_read_b64_tr_b16 a[0:1], v40 offset:49152\n"
"s_add_u32 m0, s78, 32768\n"
"v_mfma_f32_16x16x32_bf16 v[68:71], a[32:35], a[92:95], v[68:71]\n"
"ds_read_b64_tr_b16 a[2:3], v40 offset:53248\n"
"buffer_load_dwordx4 v32, s[40:43], s74 offen offset:0 sc1 lds\n"
"v_mfma_f32_16x16x32_bf16 v[76:79], a[36:39], a[92:95], v[76:79]\n"
"ds_read_b64_tr_b16 a[4:5], v41 offset:49152\n"
"s_add_u32 s83, s83, 0x40000\n"
"v_mfma_f32_16x16x32_bf16 v[72:75], a[36:39], a[88:91], v[72:75]\n"
"ds_read_b64_tr_b16 a[6:7], v41 offset:53248\n"
"v_mfma_f32_16x16x32_bf16 v[80:83], a[40:43], a[88:91], v[80:83]\n"
"ds_read_b64_tr_b16 a[8:9], v42 offset:49152\n"
"s_cmp_eq_u32 s83, 3670016\n"
"s_cselect_b32 s83, 0x10000, s83\n"
"v_mfma_f32_16x16x32_bf16 v[84:87], a[40:43], a[92:95], v[84:87]\n"
"ds_read_b64_tr_b16 a[10:11], v42 offset:53248\n"
"buffer_load_dwordx4 a[64:67], v52, s[48:51], s75 offen offset:0 sc1\n"
"v_mfma_f32_16x16x32_bf16 v[92:95], a[44:47], a[92:95], v[92:95]\n"
"ds_read_b64_tr_b16 a[12:13], v43 offset:49152\n"
"s_add_u32 s73, s73, 0x8000\n"
"v_mfma_f32_16x16x32_bf16 v[88:91], a[44:47], a[88:91], v[88:91]\n"
"ds_read_b64_tr_b16 a[14:15], v43 offset:53248\n"
"s_waitcnt lgkmcnt(8)\n"
"v_mfma_f32_16x16x32_bf16 v[96:99], a[48:51], a[88:91], v[96:99]\n"
"ds_read_b64_tr_b16 a[16:17], v44 offset:49152\n"
"v_mfma_f32_16x16x32_bf16 v[100:103], a[48:51], a[92:95], v[100:103]\n"
"ds_read_b64_tr_b16 a[18:19], v44 offset:53248\n"
"buffer_load_dwordx4 v33, s[40:43], s74 offen offset:1024 sc1 lds\n"
"v_mfma_f32_16x16x32_bf16 v[108:111], a[52:55], a[92:95], v[108:111]\n"
"ds_read_b64_tr_b16 a[20:21], v45 offset:49152\n"
"s_bitcmp1_b32 0x32, s76\n"
"s_cbranch_scc1 .Lg2fl\n"
".Lg2flb:\n"
"v_mfma_f32_16x16x32_bf16 v[104:107], a[52:55], a[88:91], v[104:107]\n"
"ds_read_b64_tr_b16 a[22:23], v45 offset:53248\n"
"v_mfma_f32_16x16x32_bf16 v[112:115], a[56:59], a[88:91], v[112:115]\n"
"ds_read_b64_tr_b16 a[24:25], v46 offset:49152\n"
"v_mfma_f32_16x16x32_bf16 v[116:119], a[56:59], a[92:95], v[116:119]\n"
"ds_read_b64_tr_b16 a[26:27], v46 offset:53248\n"
"buffer_load_dwordx4 a[68:71], v52, s[48:51], s75 offen offset:1024 sc1\n"
"v_mfma_f32_16x16x32_bf16 v[124:127], a[60:63], a[92:95], v[124:127]\n"
"ds_read_b64_tr_b16 a[28:29], v47 offset:49152\n"
"v_mfma_f32_16x16x32_bf16 v[120:123], a[60:63], a[88:91], v[120:123]\n"
"ds_read_b64_tr_b16 a[30:31], v47 offset:53248\n"
"s_waitcnt lgkmcnt(8)\n"
"v_mfma_f32_16x16x32_bf16 v[64:67], a[0:3], a[96:99], v[64:67]\n"
"ds_read_b64_tr_b16 a[32:33], v40 offset:57344\n"
"v_mfma_f32_16x16x32_bf16 v[68:71], a[0:3], a[100:103], v[68:71]\n"
"ds_read_b64_tr_b16 a[34:35], v40 offset:61440\n"
"buffer_load_dwordx4 v34, s[40:43], s74 offen offset:2048 sc1 lds\n"
"v_mfma_f32_16x16x32_bf16 v[76:79], a[4:7], a[100:103], v[76:79]\n"
"ds_read_b64_tr_b16 a[36:37], v41 offset:57344\n"
"v_xor_b32 v40, 0x10000, v40\n"
"v_mfma_f32_16x16x32_bf16 v[72:75], a[4:7], a[96:99], v[72:75]\n"
"ds_read_b64_tr_b16 a[38:39], v41 offset:61440\n"
"v_mfma_f32_16x16x32_bf16 v[80:83], a[8:11], a[96:99], v[80:83]\n"
"ds_read_b64_tr_b16 a[40:41], v42 offset:57344\n"
"v_xor_b32 v41, 0x10000, v41\n"
"v_mfma_f32_16x16x32_bf16 v[84:87], a[8:11], a[100:103], v[84:87]\n"
"ds_read_b64_tr_b16 a[42:43], v42 offset:61440\n"
"buffer_load_dwordx4 a[72:75], v36, s[48:51], s75 offen offset:0 sc1\n"
"v_mfma_f32_16x16x32_bf16 v[92:95], a[12:15], a[100:103], v[92:95]\n"
"ds_read_b64_tr_b16 a[44:45], v43 offset:57344\n"
"v_xor_b32 v42, 0x10000, v42\n"
"v_mfma_f32_16x16x32_bf16 v[88:91], a[12:15], a[96:99], v[88:91]\n"
"ds_read_b64_tr_b16 a[46:47], v43 offset:61440\n"
"s_waitcnt lgkmcnt(8)\n"
"v_mfma_f32_16x16x32_bf16 v[96:99], a[16:19], a[96:99], v[96:99]\n"
"ds_read_b64_tr_b16 a[48:49], v44 offset:57344\n"
"v_xor_b32 v43, 0x10000, v43\n"
"v_mfma_f32_16x16x32_bf16 v[100:103], a[16:19], a[100:103], v[100:103]\n"
"ds_read_b64_tr_b16 a[50:51], v44 offset:61440\n"
"buffer_load_dwordx4 v35, s[40:43], s74 offen offset:3072 sc1 lds\n"
"v_mfma_f32_16x16x32_bf16 v[108:111], a[20:23], a[100:103], v[108:111]\n"
"ds_read_b64_tr_b16 a[52:53], v45 offset:57344\n"
"s_xor_b32 s74, s83, s39\n"
"v_xor_b32 v44, 0x10000, v44\n"
"v_mfma_f32_16x16x32_bf16 v[104:107], a[20:23], a[96:99], v[104:107]\n"
"ds_read_b64_tr_b16 a[54:55], v45 offset:61440\n"
"v_mfma_f32_16x16x32_bf16 v[112:115], a[24:27], a[96:99], v[112:115]\n"
"ds_read_b64_tr_b16 a[56:57], v46 offset:57344\n"
"buffer_load_dwordx4 a[76:79], v36, s[48:51], s75 offen offset:1024 sc1\n"
"v_xor_b32 v45, 0x10000, v45\n"
"v_mfma_f32_16x16x32_bf16 v[116:119], a[24:27], a[100:103], v[116:119]\n"
"ds_read_b64_tr_b16 a[58:59], v46 offset:61440\n"
"s_xor_b32 s75, s73, s38\n"
"v_mfma_f32_16x16x32_bf16 v[124:127], a[28:31], a[100:103], v[124:127]\n"
"ds_read_b64_tr_b16 a[60:61], v47 offset:57344\n"
"v_xor_b32 v46, 0x10000, v46\n"
"v_mfma_f32_16x16x32_bf16 v[120:123], a[28:31], a[96:99], v[120:123]\n"
"ds_read_b64_tr_b16 a[62:63], v47 offset:61440\n"
"v_xor_b32 v47, 0x10000, v47\n"
"s_waitcnt vmcnt(8)\n"
"s_barrier\n"
"s_waitcnt lgkmcnt(8)\n"
"v_mfma_f32_16x16x32_bf16 v[64:67], a[32:35], a[104:107], v[64:67]\n"
"ds_read_b64_tr_b16 a[0:1], v40 offset:0\n"
"s_add_u32 m0, s78, 49152\n"
"v_mfma_f32_16x16x32_bf16 v[68:71], a[32:35], a[108:111], v[68:71]\n"
"ds_read_b64_tr_b16 a[2:3], v40 offset:4096\n"
"buffer_load_dwordx4 v32, s[40:43], s74 offen offset:0 sc1 lds\n"
"v_mfma_f32_16x16x32_bf16 v[76:79], a[36:39], a[108:111], v[76:79]\n"
"ds_read_b64_tr_b16 a[4:5], v41 offset:0\n"
"s_add_u32 s83, s83, 0x40000\n"
"v_mfma_f32_16x16x32_bf16 v[72:75], a[36:39], a[104:107], v[72:75]\n"
"ds_read_b64_tr_b16 a[6:7], v41 offset:4096\n"
"v_mfma_f32_16x16x32_bf16 v[80:83], a[40:43], a[104:107], v[80:83]\n"
"ds_read_b64_tr_b16 a[8:9], v42 offset:0\n"
"v_mfma_f32_16x16x32_bf16 v[84:87], a[40:43], a[108:111], v[84:87]\n"
"ds_read_b64_tr_b16 a[10:11], v42 offset:4096\n"
"buffer_load_dwordx4 a[80:83], v52, s[48:51], s75 offen offset:0 sc1\n"
"v_mfma_f32_16x16x32_bf16 v[92:95], a[44:47], a[108:111], v[92:95]\n"
"ds_read_b64_tr_b16 a[12:13], v43 offset:0\n"
"s_add_u32 s73, s73, 0x8000\n"
"v_mfma_f32_16x16x32_bf16 v[88:91], a[44:47], a[104:107], v[88:91]\n"
"ds_read_b64_tr_b16 a[14:15], v43 offset:4096\n"
"s_waitcnt lgkmcnt(8)\n"
"v_mfma_f32_16x16x32_bf16 v[96:99], a[48:51], a[104:107], v[96:99]\n"
"ds_read_b64_tr_b16 a[16:17], v44 offset:0\n"
"v_mfma_f32_16x16x32_bf16 v[100:103], a[48:51], a[108:111], v[100:103]\n"
"ds_read_b64_tr_b16 a[18:19], v44 offset:4096\n"
"buffer_load_dwordx4 v33, s[40:43], s74 offen offset:1024 sc1 lds\n"
"v_mfma_f32_16x16x32_bf16 v[108:111], a[52:55], a[108:111], v[108:111]\n"
"ds_read_b64_tr_b16 a[20:21], v45 offset:0\n"
"s_cmp_eq_u32 s73, 458752\n"
"s_cselect_b32 s73, 0x2000, s73\n"
"v_mfma_f32_16x16x32_bf16 v[104:107], a[52:55], a[104:107], v[104:107]\n"
"ds_read_b64_tr_b16 a[22:23], v45 offset:4096\n"
"v_mfma_f32_16x16x32_bf16 v[112:115], a[56:59], a[104:107], v[112:115]\n"
"ds_read_b64_tr_b16 a[24:25], v46 offset:0\n"
"s_xor_b32 s78, s78, 0x10000\n"
"v_mfma_f32_16x16x32_bf16 v[116:119], a[56:59], a[108:111], v[116:119]\n"
"ds_read_b64_tr_b16 a[26:27], v46 offset:4096\n"
"buffer_load_dwordx4 a[84:87], v52, s[48:51], s75 offen offset:1024 sc1\n"
"v_mfma_f32_16x16x32_bf16 v[124:127], a[60:63], a[108:111], v[124:127]\n"
"ds_read_b64_tr_b16 a[28:29], v47 offset:0\n"
"v_mfma_f32_16x16x32_bf16 v[120:123], a[60:63], a[104:107], v[120:123]\n"
"ds_read_b64_tr_b16 a[30:31], v47 offset:4096\n"
"s_waitcnt lgkmcnt(8)\n"
"v_mfma_f32_16x16x32_bf16 v[64:67], a[0:3], a[112:115], v[64:67]\n"
"ds_read_b64_tr_b16 a[32:33], v40 offset:8192\n"
"v_mfma_f32_16x16x32_bf16 v[68:71], a[0:3], a[116:119], v[68:71]\n"
"ds_read_b64_tr_b16 a[34:35], v40 offset:12288\n"
"buffer_load_dwordx4 v34, s[40:43], s74 offen offset:2048 sc1 lds\n"
"v_mfma_f32_16x16x32_bf16 v[76:79], a[4:7], a[116:119], v[76:79]\n"
"ds_read_b64_tr_b16 a[36:37], v41 offset:8192\n"
"v_mfma_f32_16x16x32_bf16 v[72:75], a[4:7], a[112:115], v[72:75]\n"
"ds_read_b64_tr_b16 a[38:39], v41 offset:12288\n"
"v_mfma_f32_16x16x32_bf16 v[80:83], a[8:11], a[112:115], v[80:83]\n"
"ds_read_b64_tr_b16 a[40:41], v42 offset:8192\n"
"v_mfma_f32_16x16x32_bf16 v[84:87], a[8:11], a[116:119], v[84:87]\n"
"ds_read_b64_tr_b16 a[42:43], v42 offset:12288\n"
"buffer_load_dwordx4 a[88:91], v36, s[48:51], s75 offen offset:0 sc1\n"
"v_mfma_f32_16x16x32_bf16 v[92:95], a[12:15], a[116:119], v[92:95]\n"
"ds_read_b64_tr_b16 a[44:45], v43 offset:8192\n"
"v_mfma_f32_16x16x32_bf16 v[88:91], a[12:15], a[112:115], v[88:91]\n"
"ds_read_b64_tr_b16 a[46:47], v43 offset:12288\n"
"s_waitcnt lgkmcnt(8)\n"
"v_mfma_f32_16x16x32_bf16 v[96:99], a[16:19], a[112:115], v[96:99]\n"
"ds_read_b64_tr_b16 a[48:49], v44 offset:8192\n"
"v_mfma_f32_16x16x32_bf16 v[100:103], a[16:19], a[116:119], v[100:103]\n"
"ds_read_b64_tr_b16 a[50:51], v44 offset:12288\n"
"buffer_load_dwordx4 v35, s[40:43], s74 offen offset:3072 sc1 lds\n"
"v_mfma_f32_16x16x32_bf16 v[108:111], a[20:23], a[116:119], v[108:111]\n"
"ds_read_b64_tr_b16 a[52:53], v45 offset:8192\n"
"s_xor_b32 s74, s83, s39\n"
"v_mfma_f32_16x16x32_bf16 v[104:107], a[20:23], a[112:115], v[104:107]\n"
"ds_read_b64_tr_b16 a[54:55], v45 offset:12288\n"
"v_mfma_f32_16x16x32_bf16 v[112:115], a[24:27], a[112:115], v[112:115]\n"
"ds_read_b64_tr_b16 a[56:57], v46 offset:8192\n"
"buffer_load_dwordx4 a[92:95], v36, s[48:51], s75 offen offset:1024 sc1\n"
"v_mfma_f32_16x16x32_bf16 v[116:119], a[24:27], a[116:119], v[116:119]\n"
"ds_read_b64_tr_b16 a[58:59], v46 offset:12288\n"
"s_xor_b32 s75, s73, s38\n"
"v_mfma_f32_16x16x32_bf16 v[124:127], a[28:31], a[116:119], v[124:127]\n"
"ds_read_b64_tr_b16 a[60:61], v47 offset:8192\n"
"v_mfma_f32_16x16x32_bf16 v[120:123], a[28:31], a[112:115], v[120:123]\n"
"ds_read_b64_tr_b16 a[62:63], v47 offset:12288\n"
"s_waitcnt vmcnt(8)\n"
"s_waitcnt lgkmcnt(8)\n"
"v_mfma_f32_16x16x32_bf16 v[64:67], a[32:35], a[120:123], v[64:67]\n"
"ds_read_b64_tr_b16 a[0:1], v40 offset:16384\n"
"s_add_u32 m0, s78, 0\n"
"v_mfma_f32_16x16x32_bf16 v[68:71], a[32:35], a[124:127], v[68:71]\n"
"ds_read_b64_tr_b16 a[2:3], v40 offset:20480\n"
"buffer_load_dwordx4 v32, s[40:43], s74 offen offset:0 sc1 lds\n"
"v_mfma_f32_16x16x32_bf16 v[76:79], a[36:39], a[124:127], v[76:79]\n"
"ds_read_b64_tr_b16 a[4:5], v41 offset:16384\n"
"s_add_u32 s83, s83, 0x40000\n"
"v_cmp_eq_u64 vcc, v[14:15], v[12:13]\n"
"v_mfma_f32_16x16x32_bf16 v[72:75], a[36:39], a[120:123], v[72:75]\n"
"ds_read_b64_tr_b16 a[6:7], v41 offset:20480\n"
"v_mfma_f32_16x16x32_bf16 v[80:83], a[40:43], a[120:123], v[80:83]\n"
"ds_read_b64_tr_b16 a[8:9], v42 offset:16384\n"
"s_andn2_b64 s[86:87], s[84:85], vcc\n"
"s_cbranch_scc1 .Lg2slow\n"
".Lg2back:\n"
"v_mfma_f32_16x16x32_bf16 v[84:87], a[40:43], a[124:127], v[84:87]\n"
"ds_read_b64_tr_b16 a[10:11], v42 offset:20480\n"
"buffer_load_dwordx4 a[96:99], v52, s[48:51], s75 offen offset:0 sc1\n"
"v_mfma_f32_16x16x32_bf16 v[92:95], a[44:47], a[124:127], v[92:95]\n"
"ds_read_b64_tr_b16 a[12:13], v43 offset:16384\n"
"s_add_u32 s73, s73, 0x8000\n"
"v_mfma_f32_16x16x32_bf16 v[88:91], a[44:47], a[120:123], v[88:91]\n"
"ds_read_b64_tr_b16 a[14:15], v43 offset:20480\n"
"s_waitcnt lgkmcnt(8)\n"
"v_mfma_f32_16x16x32_bf16 v[96:99], a[48:51], a[120:123], v[96:99]\n"
"ds_read_b64_tr_b16 a[16:17], v44 offset:16384\n"
"v_mfma_f32_16x16x32_bf16 v[100:103], a[48:51], a[124:127], v[100:103]\n"
"ds_read_b64_tr_b16 a[18:19], v44 offset:20480\n"
"buffer_load_dwordx4 v33, s[40:43], s74 offen offset:1024 sc1 lds\n"
"v_mfma_f32_16x16x32_bf16 v[108:111], a[52:55], a[124:127], v[108:111]\n"
"ds_read_b64_tr_b16 a[20:21], v45 offset:16384\n"
"v_mfma_f32_16x16x32_bf16 v[104:107], a[52:55], a[120:123], v[104:107]\n"
"ds_read_b64_tr_b16 a[22:23], v45 offset:20480\n"
"v_mfma_f32_16x16x32_bf16 v[112:115], a[56:59], a[120:123], v[112:115]\n"
"ds_read_b64_tr_b16 a[24:25], v46 offset:16384\n"
"v_mfma_f32_16x16x32_bf16 v[116:119], a[56:59], a[124:127], v[116:119]\n"
"ds_read_b64_tr_b16 a[26:27], v46 offset:20480\n"
"buffer_load_dwordx4 a[100:103], v52, s[48:51], s75 offen offset:1024 sc1\n"
"v_mfma_f32_16x16x32_bf16 v[124:127], a[60:63], a[124:127], v[124:127]\n"
"ds_read_b64_tr_b16 a[28:29], v47 offset:16384\n"
"v_mfma_f32_16x16x32_bf16 v[120:123], a[60:63], a[120:123], v[120:123]\n"
"ds_read_b64_tr_b16 a[30:31], v47 offset:20480\n"
"s_waitcnt lgkmcnt(8)\n"
"v_mfma_f32_16x16x32_bf16 v[64:67], a[0:3], a[64:67], v[64:67]\n"
"ds_read_b64_tr_b16 a[32:33], v40 offset:24576\n"
"v_mfma_f32_16x16x32_bf16 v[68:71], a[0:3], a[68:71], v[68:71]\n"
"ds_read_b64_tr_b16 a[34:35], v40 offset:28672\n"
"buffer_load_dwordx4 v34, s[40:43], s74 offen offset:2048 sc1 lds\n"
"v_mfma_f32_16x16x32_bf16 v[76:79], a[4:7], a[68:71], v[76:79]\n"
"ds_read_b64_tr_b16 a[36:37], v41 offset:24576\n"
"v_mfma_f32_16x16x32_bf16 v[72:75], a[4:7], a[64:67], v[72:75]\n"
"ds_read_b64_tr_b16 a[38:39], v41 offset:28672\n"
"v_mfma_f32_16x16x32_bf16 v[80:83], a[8:11], a[64:67], v[80:83]\n"
"ds_read_b64_tr_b16 a[40:41], v42 offset:24576\n"
"v_mfma_f32_16x16x32_bf16 v[84:87], a[8:11], a[68:71], v[84:87]\n"
"ds_read_b64_tr_b16 a[42:43], v42 offset:28672\n"
"buffer_load_dwordx4 a[104:107], v36, s[48:51], s75 offen offset:0 sc1\n"
"v_mfma_f32_16x16x32_bf16 v[92:95], a[12:15], a[68:71], v[92:95]\n"
"ds_read_b64_tr_b16 a[44:45], v43 offset:24576\n"
"v_mfma_f32_16x16x32_bf16 v[88:91], a[12:15], a[64:67], v[88:91]\n"
"ds_read_b64_tr_b16 a[46:47], v43 offset:28672\n"
"s_waitcnt lgkmcnt(8)\n"
"v_mfma_f32_16x16x32_bf16 v[96:99], a[16:19], a[64:67], v[96:99]\n"
"ds_read_b64_tr_b16 a[48:49], v44 offset:24576\n"
"v_mfma_f32_16x16x32_bf16 v[100:103], a[16:19], a[68:71], v[100:103]\n"
"ds_read_b64_tr_b16 a[50:51], v44 offset:28672\n"
"buffer_load_dwordx4 v35, s[40:43], s74 offen offset:3072 sc1 lds\n"
"v_mfma_f32_16x16x32_bf16 v[108:111], a[20:23], a[68:71], v[108:111]\n"
"ds_read_b64_tr_b16 a[52:53], v45 offset:24576\n"
"s_xor_b32 s74, s83, s39\n"
"v_mfma_f32_16x16x32_bf16 v[104:107], a[20:23], a[64:67], v[104:107]\n"
"ds_read_b64_tr_b16 a[54:55], v45 offset:28672\n"
"v_mfma_f32_16x16x32_bf16 v[112:115], a[24:27], a[64:67], v[112:115]\n"
"ds_read_b64_tr_b16 a[56:57], v46 offset:24576\n"
"buffer_load_dwordx4 a[108:111], v36, s[48:51], s75 offen offset:1024 sc1\n"
"v_mfma_f32_16x16x32_bf16 v[116:119], a[24:27], a[68:71], v[116:119]\n"
"ds_read_b64_tr_b16 a[58:59], v46 offset:28672\n"
"s_xor_b32 s75, s73, s38\n"
"v_mfma_f32_16x16x32_bf16 v[124:127], a[28:31], a[68:71], v[124:127]\n"
"ds_read_b64_tr_b16 a[60:61], v47 offset:24576\n"
"s_sub_u32 s76, s76, 1\n"
"s_cmp_lg_u32 s76, 0\n"
"v_mfma_f32_16x16x32_bf16 v[120:123], a[28:31], a[64:67], v[120:123]\n"
"ds_read_b64_tr_b16 a[62:63], v47 offset:28672\n"
"s_waitcnt vmcnt(8)\n"
"s_barrier\n"
"s_cbranch_scc1 .Lg2loop\n"
"s_waitcnt lgkmcnt(8)\n"
"v_mfma_f32_16x16x32_bf16 v[64:67], a[32:35], a[72:75], v[64:67]\n"
"ds_read_b64_tr_b16 a[0:1], v44 offset:32768\n"
"v_mfma_f32_16x16x32_bf16 v[68:71], a[32:35], a[76:79], v[68:71]\n"
"ds_read_b64_tr_b16 a[2:3], v44 offset:36864\n"
"buffer_load_dwordx4 a[112:115], v52, s[48:51], s75 offen offset:0 sc1\n"
"v_mfma_f32_16x16x32_bf16 v[76:79], a[36:39], a[76:79], v[76:79]\n"
"ds_read_b64_tr_b16 a[4:5], v45 offset:32768\n"
"s_add_u32 s82, s72, 0\n"
"buffer_load_dword v16, v39, s[28:31], s82 offen offset:0 sc1\n"
"v_mfma_f32_16x16x32_bf16 v[72:75], a[36:39], a[72:75], v[72:75]\n"
"ds_read_b64_tr_b16 a[6:7], v45 offset:36864\n"
"v_mfma_f32_16x16x32_bf16 v[80:83], a[40:43], a[72:75], v[80:83]\n"
"ds_read_b64_tr_b16 a[8:9], v46 offset:32768\n"
"buffer_load_dword v20, v39, s[28:31], s82 offen offset:64 sc1\n"
"v_mfma_f32_16x16x32_bf16 v[84:87], a[40:43], a[76:79], v[84:87]\n"
"ds_read_b64_tr_b16 a[10:11], v46 offset:36864\n"
"buffer_load_dwordx4 a[116:119], v52, s[48:51], s75 offen offset:1024 sc1\n"
"v_mfma_f32_16x16x32_bf16 v[92:95], a[44:47], a[76:79], v[92:95]\n"
"ds_read_b64_tr_b16 a[12:13], v47 offset:32768\n"
"s_add_u32 s82, s72, 8192\n"
"buffer_load_dword v17, v39, s[28:31], s82 offen offset:0 sc1\n"
"v_mfma_f32_16x16x32_bf16 v[88:91], a[44:47], a[72:75], v[88:91]\n"
"ds_read_b64_tr_b16 a[14:15], v47 offset:36864\n"
"s_waitcnt lgkmcnt(8)\n"
"v_mfma_f32_16x16x32_bf16 v[96:99], a[48:51], a[72:75], v[96:99]\n"
"ds_read_b64_tr_b16 a[16:17], v44 offset:40960\n"
"buffer_load_dword v21, v39, s[28:31], s82 offen offset:64 sc1\n"
"v_mfma_f32_16x16x32_bf16 v[100:103], a[48:51], a[76:79], v[100:103]\n"
"ds_read_b64_tr_b16 a[18:19], v44 offset:45056\n"
"buffer_load_dwordx4 a[120:123], v36, s[48:51], s75 offen offset:0 sc1\n"
"v_mfma_f32_16x16x32_bf16 v[108:111], a[52:55], a[76:79], v[108:111]\n"
"ds_read_b64_tr_b16 a[20:21], v45 offset:40960\n"
"s_add_u32 s82, s72, 16384\n"
"buffer_load_dword v18, v39, s[28:31], s82 offen offset:0 sc1\n"
"v_mfma_f32_16x16x32_bf16 v[104:107], a[52:55], a[72:75], v[104:107]\n"
"ds_read_b64_tr_b16 a[22:23], v45 offset:45056\n"
"v_mfma_f32_16x16x32_bf16 v[112:115], a[56:59], a[72:75], v[112:115]\n"
"ds_read_b64_tr_b16 a[24:25], v46 offset:40960\n"
"buffer_load_dword v22, v39, s[28:31], s82 offen offset:64 sc1\n"
"v_mfma_f32_16x16x32_bf16 v[116:119], a[56:59], a[76:79], v[116:119]\n"
"ds_read_b64_tr_b16 a[26:27], v46 offset:45056\n"
"buffer_load_dwordx4 a[124:127], v36, s[48:51], s75 offen offset:1024 sc1\n"
"v_mfma_f32_16x16x32_bf16 v[124:127], a[60:63], a[76:79], v[124:127]\n"
"ds_read_b64_tr_b16 a[28:29], v47 offset:40960\n"
"s_add_u32 s82, s72, 24576\n"
"buffer_load_dword v19, v39, s[28:31], s82 offen offset:0 sc1\n"
"v_mfma_f32_16x16x32_bf16 v[120:123], a[60:63], a[72:75], v[120:123]\n"
"ds_read_b64_tr_b16 a[30:31], v47 offset:45056\n"
"buffer_load_dword v23, v39, s[28:31], s82 offen offset:64 sc1\n"
"s_waitcnt lgkmcnt(8)\n"
"v_mfma_f32_16x16x32_bf16 v[96:99], a[0:3], a[80:83], v[96:99]\n"
"ds_read_b64_tr_b16 a[32:33], v44 offset:49152\n"
"s_add_u32 m0, s98, 16384\n"
"s_mov_b32 s82, 0\n"
"v_mfma_f32_16x16x32_bf16 v[100:103], a[0:3], a[84:87], v[100:103]\n"
"ds_read_b64_tr_b16 a[34:35], v44 offset:53248\n"
"buffer_load_dwordx4 v37, s[52:55], s82 offen sc1 lds\n"
"v_mfma_f32_16x16x32_bf16 v[108:111], a[4:7], a[84:87], v[108:111]\n"
"ds_read_b64_tr_b16 a[36:37], v45 offset:49152\n"
"v_mfma_f32_16x16x32_bf16 v[104:107], a[4:7], a[80:83], v[104:107]\n"
"ds_read_b64_tr_b16 a[38:39], v45 offset:53248\n"
"v_mfma_f32_16x16x32_bf16 v[112:115], a[8:11], a[80:83], v[112:115]\n"
"ds_read_b64_tr_b16 a[40:41], v46 offset:49152\n"
"s_add_u32 m0, s98, 20480\n"
"s_mov_b32 s82, 65536\n"
"v_mfma_f32_16x16x32_bf16 v[116:119], a[8:11], a[84:87], v[116:119]\n"
"ds_read_b64_tr_b16 a[42:43], v46 offset:53248\n"
"buffer_load_dwordx4 v37, s[52:55], s82 offen sc1 lds\n"
"v_mfma_f32_16x16x32_bf16 v[124:127], a[12:15], a[84:87], v[124:127]\n"
"ds_read_b64_tr_b16 a[44:45], v47 offset:49152\n"
"v_mfma_f32_16x16x32_bf16 v[120:123], a[12:15], a[80:83], v[120:123]\n"
"ds_read_b64_tr_b16 a[46:47], v47 offset:53248\n"
"s_waitcnt lgkmcnt(8)\n"
"v_mfma_f32_16x16x32_bf16 v[96:99], a[16:19], a[88:91], v[96:99]\n"
"ds_read_b64_tr_b16 a[48:49], v44 offset:57344\n"
"s_add_u32 m0, s98, 24576\n"
"s_mov_b32 s82, 131072\n"
"v_mfma_f32_16x16x32_bf16 v[100:103], a[16:19], a[92:95], v[100:103]\n"
"ds_read_b64_tr_b16 a[50:51], v44 offset:61440\n"
"buffer_load_dwordx4 v37, s[52:55], s82 offen sc1 lds\n"
"v_mfma_f32_16x16x32_bf16 v[108:111], a[20:23], a[92:95], v[108:111]\n"
"ds_read_b64_tr_b16 a[52:53], v45 offset:57344\n"
"v_xor_b32 v44, 0x10000, v44\n"
"v_mfma_f32_16x16x32_bf16 v[104:107], a[20:23], a[88:91], v[104:107]\n"
"ds_read_b64_tr_b16 a[54:55], v45 offset:61440\n"
"v_mfma_f32_16x16x32_bf16 v[112:115], a[24:27], a[88:91], v[112:115]\n"
"ds_read_b64_tr_b16 a[56:57], v46 offset:57344\n"
"s_add_u32 m0, s98, 28672\n"
"s_mov_b32 s82, 196608\n"
"v_xor_b32 v45, 0x10000, v45\n"
"v_mfma_f32_16x16x32_bf16 v[116:119], a[24:27], a[92:95], v[116:119]\n"
"ds_read_b64_tr_b16 a[58:59], v46 offset:61440\n"
"buffer_load_dwordx4 v37, s[52:55], s82 offen sc1 lds\n"
"v_mfma_f32_16x16x32_bf16 v[124:127], a[28:31], a[92:95], v[124:127]\n"
"ds_read_b64_tr_b16 a[60:61], v47 offset:57344\n"
"v_xor_b32 v46, 0x10000, v46\n"
"v_mfma_f32_16x16x32_bf16 v[120:123], a[28:31], a[88:91], v[120:123]\n"
"ds_read_b64_tr_b16 a[62:63], v47 offset:61440\n"
"v_xor_b32 v47, 0x10000, v47\n"
"s_waitcnt vmcnt(16)\n"
"s_barrier\n"
"s_waitcnt lgkmcnt(8)\n"
"v_mfma_f32_16x16x32_bf16 v[96:99], a[32:35], a[96:99], v[96:99]\n"
"ds_read_b64_tr_b16 a[0:1], v44 offset:0\n"
"v_mfma_f32_16x16x32_bf16 v[100:103], a[32:35], a[100:103], v[100:103]\n"
"ds_read_b64_tr_b16 a[2:3], v44 offset:4096\n"
"s_add_u32 s83, s66, 0\n"
"buffer_load_dwordx4 a[128:131], v52, s[20:23], s83 offen offset:0\n"
"v_mfma_f32_16x16x32_bf16 v[108:111], a[36:39], a[100:103], v[108:111]\n"
"ds_read_b64_tr_b16 a[4:5], v45 offset:0\n"
"v_mfma_f32_16x16x32_bf16 v[104:107], a[36:39], a[96:99], v[104:107]\n"
"ds_read_b64_tr_b16 a[6:7], v45 offset:4096\n"
"v_mfma_f32_16x16x32_bf16 v[112:115], a[40:43], a[96:99], v[112:115]\n"
"ds_read_b64_tr_b16 a[8:9], v46 offset:0\n"
"v_mfma_f32_16x16x32_bf16 v[116:119], a[40:43], a[100:103], v[116:119]\n"
"ds_read_b64_tr_b16 a[10:11], v46 offset:4096\n"
"buffer_load_dwordx4 a[132:135], v52, s[20:23], s83 offen offset:1024\n"
"v_mfma_f32_16x16x32_bf16 v[124:127], a[44:47], a[100:103], v[124:127]\n"
"ds_read_b64_tr_b16 a[12:13], v47 offset:0\n"
"v_mfma_f32_16x16x32_bf16 v[120:123], a[44:47], a[96:99], v[120:123]\n"
"ds_read_b64_tr_b16 a[14:15], v47 offset:4096\n"
"s_waitcnt lgkmcnt(8)\n"
"v_mfma_f32_16x16x32_bf16 v[96:99], a[48:51], a[104:107], v[96:99]\n"
"ds_read_b64_tr_b16 a[16:17], v44 offset:8192\n"
"v_mfma_f32_16x16x32_bf16 v[100:103], a[48:51], a[108:111], v[100:103]\n"
"ds_read_b64_tr_b16 a[18:19], v44 offset:12288\n"
"s_add_u32 s83, s66, 8192\n"
"buffer_load_dwordx4 a[136:139], v52, s[20:23], s83 offen offset:0\n"
"v_mfma_f32_16x16x32_bf16 v[108:111], a[52:55], a[108:111], v[108:111]\n"
"ds_read_b64_tr_b16 a[20:21], v45 offset:8192\n"
"v_mfma_f32_16x16x32_bf16 v[104:107], a[52:55], a[104:107], v[104:107]\n"
"ds_read_b64_tr_b16 a[22:23], v45 offset:12288\n"
"v_mfma_f32_16x16x32_bf16 v[112:115], a[56:59], a[104:107], v[112:115]\n"
"ds_read_b64_tr_b16 a[24:25], v46 offset:8192\n"
"v_mfma_f32_16x16x32_bf16 v[116:119], a[56:59], a[108:111], v[116:119]\n"
"ds_read_b64_tr_b16 a[26:27], v46 offset:12288\n"
"buffer_load_dwordx4 a[140:143], v52, s[20:23], s83 offen offset:1024\n"
"v_mfma_f32_16x16x32_bf16 v[124:127], a[60:63], a[108:111], v[124:127]\n"
"ds_read_b64_tr_b16 a[28:29], v47 offset:8192\n"
"v_mfma_f32_16x16x32_bf16 v[120:123], a[60:63], a[104:107], v[120:123]\n"
"ds_read_b64_tr_b16 a[30:31], v47 offset:12288\n"
"s_waitcnt vmcnt(10)\n"
"s_waitcnt lgkmcnt(8)\n"
"v_mfma_f32_16x16x32_bf16 v[96:99], a[0:3], a[112:115], v[96:99]\n"
"ds_read_b64_tr_b16 a[32:33], v40 offset:32768\n"
"v_mfma_f32_16x16x32_bf16 v[100:103], a[0:3], a[116:119], v[100:103]\n"
"ds_read_b64_tr_b16 a[34:35], v40 offset:36864\n"
"s_add_u32 s83, s66, 16384\n"
"buffer_load_dwordx4 a[144:147], v52, s[20:23], s83 offen offset:0\n"
"v_mfma_f32_16x16x32_bf16 v[108:111], a[4:7], a[116:119], v[108:111]\n"
"ds_read_b64_tr_b16 a[36:37], v41 offset:32768\n"
"v_mfma_f32_16x16x32_bf16 v[104:107], a[4:7], a[112:115], v[104:107]\n"
"ds_read_b64_tr_b16 a[38:39], v41 offset:36864\n"
"v_mfma_f32_16x16x32_bf16 v[112:115], a[8:11], a[112:115], v[112:115]\n"
"ds_read_b64_tr_b16 a[40:41], v42 offset:32768\n"
"v_mfma_f32_16x16x32_bf16 v[116:119], a[8:11], a[116:119], v[116:119]\n"
"ds_read_b64_tr_b16 a[42:43], v42 offset:36864\n"
"buffer_load_dwordx4 a[148:151], v52, s[20:23], s83 offen offset:1024\n"
"v_mfma_f32_16x16x32_bf16 v[124:127], a[12:15], a[116:119], v[124:127]\n"
"ds_read_b64_tr_b16 a[44:45], v43 offset:32768\n"
"v_mfma_f32_16x16x32_bf16 v[120:123], a[12:15], a[112:115], v[120:123]\n"
"ds_read_b64_tr_b16 a[46:47], v43 offset:36864\n"
"s_waitcnt lgkmcnt(8)\n"
"v_mfma_f32_16x16x32_bf16 v[96:99], a[16:19], a[120:123], v[96:99]\n"
"ds_read_b64_tr_b16 a[48:49], v40 offset:40960\n"
"v_mfma_f32_16x16x32_bf16 v[100:103], a[16:19], a[124:127], v[100:103]\n"
"ds_read_b64_tr_b16 a[50:51], v40 offset:45056\n"
"s_add_u32 s83, s66, 24576\n"
"buffer_load_dwordx4 a[152:155], v52, s[20:23], s83 offen offset:0\n"
"v_mfma_f32_16x16x32_bf16 v[108:111], a[20:23], a[124:127], v[108:111]\n"
"ds_read_b64_tr_b16 a[52:53], v41 offset:40960\n"
"v_mfma_f32_16x16x32_bf16 v[104:107], a[20:23], a[120:123], v[104:107]\n"
"ds_read_b64_tr_b16 a[54:55], v41 offset:45056\n"
"v_mfma_f32_16x16x32_bf16 v[112:115], a[24:27], a[120:123], v[112:115]\n"
"ds_read_b64_tr_b16 a[56:57], v42 offset:40960\n"
"v_mfma_f32_16x16x32_bf16 v[116:119], a[24:27], a[124:127], v[116:119]\n"
"ds_read_b64_tr_b16 a[58:59], v42 offset:45056\n"
"buffer_load_dwordx4 a[156:159], v52, s[20:23], s83 offen offset:1024\n"
"v_mfma_f32_16x16x32_bf16 v[124:127], a[28:31], a[124:127], v[124:127]\n"
"ds_read_b64_tr_b16 a[60:61], v43 offset:40960\n"
"v_mfma_f32_16x16x32_bf16 v[120:123], a[28:31], a[120:123], v[120:123]\n"
"ds_read_b64_tr_b16 a[62:63], v43 offset:45056\n"
"s_waitcnt lgkmcnt(8)\n"
"v_mfma_f32_16x16x32_bf16 v[64:67], a[32:35], a[80:83], v[64:67]\n"
"ds_read_b64_tr_b16 a[0:1], v40 offset:49152\n"
"v_mfma_f32_16x16x32_bf16 v[68:71], a[32:35], a[84:87], v[68:71]\n"
"ds_read_b64_tr_b16 a[2:3], v40 offset:53248\n"
"v_cvt_pk_bf16_f32 v192, v96, v97\n"
"v_cvt_pk_bf16_f32 v193, v98, v99\n"
"v_mfma_f32_16x16x32_bf16 v[76:79], a[36:39], a[84:87], v[76:79]\n"
"ds_read_b64_tr_b16 a[4:5], v41 offset:49152\n"
"v_cvt_pk_bf16_f32 v194, v100, v101\n"
"v_cvt_pk_bf16_f32 v195, v102, v103\n"
"v_mfma_f32_16x16x32_bf16 v[72:75], a[36:39], a[80:83], v[72:75]\n"
"ds_read_b64_tr_b16 a[6:7], v41 offset:53248\n"
"buffer_store_dwordx4 v[192:195], v52, s[60:63], s67 offen offset:0 sc1\n"
"v_mfma_f32_16x16x32_bf16 v[80:83], a[40:43], a[80:83], v[80:83]\n"
"ds_read_b64_tr_b16 a[8:9], v42 offset:49152\n"
"v_cvt_pk_bf16_f32 v196, v104, v105\n"
"v_cvt_pk_bf16_f32 v197, v106, v107\n"
"v_mfma_f32_16x16x32_bf16 v[84:87], a[40:43], a[84:87], v[84:87]\n"
"ds_read_b64_tr_b16 a[10:11], v42 offset:53248\n"
"v_cvt_pk_bf16_f32 v198, v108, v109\n"
"v_cvt_pk_bf16_f32 v199, v110, v111\n"
"v_mfma_f32_16x16x32_bf16 v[92:95], a[44:47], a[84:87], v[92:95]\n"
"ds_read_b64_tr_b16 a[12:13], v43 offset:49152\n"
"buffer_store_dwordx4 v[196:199], v52, s[60:63], s67 offen offset:1024 sc1\n"
"v_mfma_f32_16x16x32_bf16 v[88:91], a[44:47], a[80:83], v[88:91]\n"
"ds_read_b64_tr_b16 a[14:15], v43 offset:53248\n"
"v_cvt_pk_bf16_f32 v200, v112, v113\n"
"v_cvt_pk_bf16_f32 v201, v114, v115\n"
"s_waitcnt lgkmcnt(8)\n"
"v_mfma_f32_16x16x32_bf16 v[64:67], a[48:51], a[88:91], v[64:67]\n"
"ds_read_b64_tr_b16 a[16:17], v40 offset:57344\n"
"v_cvt_pk_bf16_f32 v202, v116, v117\n"
"v_cvt_pk_bf16_f32 v203, v118, v119\n"
"v_mfma_f32_16x16x32_bf16 v[68:71], a[48:51], a[92:95], v[68:71]\n"
"ds_read_b64_tr_b16 a[18:19], v40 offset:61440\n"
"buffer_store_dwordx4 v[200:203], v52, s[60:63], s67 offen offset:2048 sc1\n"
"v_mfma_f32_16x16x32_bf16 v[76:79], a[52:55], a[92:95], v[76:79]\n"
"ds_read_b64_tr_b16 a[20:21], v41 offset:57344\n"
"v_xor_b32 v40, 0x10000, v40\n"
"v_mfma_f32_16x16x32_bf16 v[72:75], a[52:55], a[88:91], v[72:75]\n"
"ds_read_b64_tr_b16 a[22:23], v41 offset:61440\n"
"v_cvt_pk_bf16_f32 v204, v120, v121\n"
"v_cvt_pk_bf16_f32 v205, v122, v123\n"
"v_mfma_f32_16x16x32_bf16 v[80:83], a[56:59], a[88:91], v[80:83]\n"
"ds_read_b64_tr_b16 a[24:25], v42 offset:57344\n"
"v_cvt_pk_bf16_f32 v206, v124, v125\n"
"v_cvt_pk_bf16_f32 v207, v126, v127\n"
"v_xor_b32 v41, 0x10000, v41\n"
"v_mfma_f32_16x16x32_bf16 v[84:87], a[56:59], a[92:95], v[84:87]\n"
"ds_read_b64_tr_b16 a[26:27], v42 offset:61440\n"
"buffer_store_dwordx4 v[204:207], v52, s[60:63], s67 offen offset:3072 sc1\n"
"v_mfma_f32_16x16x32_bf16 v[92:95], a[60:63], a[92:95], v[92:95]\n"
"ds_read_b64_tr_b16 a[28:29], v43 offset:57344\n"
"v_xor_b32 v42, 0x10000, v42\n"
"v_mfma_f32_16x16x32_bf16 v[88:91], a[60:63], a[88:91], v[88:91]\n"
"ds_read_b64_tr_b16 a[30:31], v43 offset:61440\n"
"v_xor_b32 v43, 0x10000, v43\n"
"s_waitcnt vmcnt(12)\n"
"s_barrier\n"
"s_waitcnt lgkmcnt(8)\n"
"v_mfma_f32_16x16x32_bf16 v[64:67], a[0:3], a[96:99], v[64:67]\n"
"ds_read_b64_tr_b16 a[32:33], v40 offset:0\n"
"v_add_f32 v16, v16, v17\n"
"v_mfma_f32_16x16x32_bf16 v[68:71], a[0:3], a[100:103], v[68:71]\n"
"ds_read_b64_tr_b16 a[34:35], v40 offset:4096\n"
"v_add_f32 v18, v18, v19\n"
"v_mfma_f32_16x16x32_bf16 v[76:79], a[4:7], a[100:103], v[76:79]\n"
"ds_read_b64_tr_b16 a[36:37], v41 offset:0\n"
"v_add_f32 v16, v16, v18\n"
"v_mfma_f32_16x16x32_bf16 v[72:75], a[4:7], a[96:99], v[72:75]\n"
"ds_read_b64_tr_b16 a[38:39], v41 offset:4096\n"
"v_mov_b32 v17, v16\n"
"v_mfma_f32_16x16x32_bf16 v[80:83], a[8:11], a[96:99], v[80:83]\n"
"ds_read_b64_tr_b16 a[40:41], v42 offset:0\n"
"v_mfma_f32_16x16x32_bf16 v[84:87], a[8:11], a[100:103], v[84:87]\n"
"ds_read_b64_tr_b16 a[42:43], v42 offset:4096\n"
"v_permlane16_swap_b32 v16, v17\n"
"v_mfma_f32_16x16x32_bf16 v[92:95], a[12:15], a[100:103], v[92:95]\n"
"ds_read_b64_tr_b16 a[44:45], v43 offset:0\n"
"v_add_f32 v16, v16, v17\n"
"v_mfma_f32_16x16x32_bf16 v[88:91], a[12:15], a[96:99], v[88:91]\n"
"ds_read_b64_tr_b16 a[46:47], v43 offset:4096\n"
"v_mov_b32 v17, v16\n"
"s_waitcnt lgkmcnt(8)\n"
"v_mfma_f32_16x16x32_bf16 v[64:67], a[16:19], a[104:107], v[64:67]\n"
"ds_read_b64_tr_b16 a[48:49], v40 offset:8192\n"
"v_mfma_f32_16x16x32_bf16 v[68:71], a[16:19], a[108:111], v[68:71]\n"
"ds_read_b64_tr_b16 a[50:51], v40 offset:12288\n"
"v_permlane32_swap_b32 v16, v17\n"
"v_mfma_f32_16x16x32_bf16 v[76:79], a[20:23], a[108:111], v[76:79]\n"
"ds_read_b64_tr_b16 a[52:53], v41 offset:8192\n"
"v_add_f32 v16, v16, v17\n"
"v_mfma_f32_16x16x32_bf16 v[72:75], a[20:23], a[104:107], v[72:75]\n"
"ds_read_b64_tr_b16 a[54:55], v41 offset:12288\n"
"v_cvt_pk_bf16_f32 v16, v16, v16\n"
"v_mfma_f32_16x16x32_bf16 v[80:83], a[24:27], a[104:107], v[80:83]\n"
"ds_read_b64_tr_b16 a[56:57], v42 offset:8192\n"
"v_and_b32 v16, 0xffff0000, v16\n"
"v_mfma_f32_16x16x32_bf16 v[84:87], a[24:27], a[108:111], v[84:87]\n"
"ds_read_b64_tr_b16 a[58:59], v42 offset:12288\n"
"v_mul_f32 v16, 0xbfb8aa3b, v16\n"
"v_mfma_f32_16x16x32_bf16 v[92:95], a[28:31], a[108:111], v[92:95]\n"
"ds_read_b64_tr_b16 a[60:61], v43 offset:8192\n"
"v_exp_f32 v16, v16\n"
"v_mfma_f32_16x16x32_bf16 v[88:91], a[28:31], a[104:107], v[88:91]\n"
"ds_read_b64_tr_b16 a[62:63], v43 offset:12288\n"
"v_add_f32 v16, 1.0, v16\n"
"s_waitcnt lgkmcnt(8)\n"
"v_mfma_f32_16x16x32_bf16 v[64:67], a[32:35], a[112:115], v[64:67]\n"
"ds_read_b64_tr_b16 a[0:1], v48 offset:0\n"
"v_rcp_f32 v16, v16\n"
"v_mfma_f32_16x16x32_bf16 v[68:71], a[32:35], a[116:119], v[68:71]\n"
"ds_read_b64_tr_b16 a[2:3], v48 offset:2048\n"
"v_cvt_pk_bf16_f32 v28, v16, v16\n"
"v_mfma_f32_16x16x32_bf16 v[76:79], a[36:39], a[116:119], v[76:79]\n"
"ds_read_b64_tr_b16 a[4:5], v49 offset:0\n"
"v_and_b32 v28, 0xffff0000, v28\n"
"v_mfma_f32_16x16x32_bf16 v[72:75], a[36:39], a[112:115], v[72:75]\n"
"ds_read_b64_tr_b16 a[6:7], v49 offset:2048\n"
"v_sub_f32 v30, 1.0, v28\n"
"v_mfma_f32_16x16x32_bf16 v[80:83], a[40:43], a[112:115], v[80:83]\n"
"ds_read_b64_tr_b16 a[8:9], v50 offset:0\n"
"v_cvt_pk_bf16_f32 v30, v30, v30\n"
"v_mfma_f32_16x16x32_bf16 v[84:87], a[40:43], a[116:119], v[84:87]\n"
"ds_read_b64_tr_b16 a[10:11], v50 offset:2048\n"
"v_and_b32 v30, 0xffff0000, v30\n"
"v_mfma_f32_16x16x32_bf16 v[92:95], a[44:47], a[116:119], v[92:95]\n"
"ds_read_b64_tr_b16 a[12:13], v51 offset:0\n"
"v_add_f32 v20, v20, v21\n"
"v_mfma_f32_16x16x32_bf16 v[88:91], a[44:47], a[112:115], v[88:91]\n"
"ds_read_b64_tr_b16 a[14:15], v51 offset:2048\n"
"v_add_f32 v22, v22, v23\n"
"s_waitcnt lgkmcnt(8)\n"
"v_mfma_f32_16x16x32_bf16 v[64:67], a[48:51], a[120:123], v[64:67]\n"
"ds_read_b64_tr_b16 a[16:17], v48 offset:4096\n"
"v_add_f32 v20, v20, v22\n"
"v_mfma_f32_16x16x32_bf16 v[68:71], a[48:51], a[124:127], v[68:71]\n"
"ds_read_b64_tr_b16 a[18:19], v48 offset:6144\n"
"v_mov_b32 v21, v20\n"
"v_mfma_f32_16x16x32_bf16 v[76:79], a[52:55], a[124:127], v[76:79]\n"
"ds_read_b64_tr_b16 a[20:21], v49 offset:4096\n"
"v_mfma_f32_16x16x32_bf16 v[72:75], a[52:55], a[120:123], v[72:75]\n"
"ds_read_b64_tr_b16 a[22:23], v49 offset:6144\n"
"v_permlane16_swap_b32 v20, v21\n"
"v_mfma_f32_16x16x32_bf16 v[80:83], a[56:59], a[120:123], v[80:83]\n"
"ds_read_b64_tr_b16 a[24:25], v50 offset:4096\n"
"v_add_f32 v20, v20, v21\n"
"v_mfma_f32_16x16x32_bf16 v[84:87], a[56:59], a[124:127], v[84:87]\n"
"ds_read_b64_tr_b16 a[26:27], v50 offset:6144\n"
"v_mov_b32 v21, v20\n"
"v_mfma_f32_16x16x32_bf16 v[92:95], a[60:63], a[124:127], v[92:95]\n"
"ds_read_b64_tr_b16 a[28:29], v51 offset:4096\n"
"v_mfma_f32_16x16x32_bf16 v[88:91], a[60:63], a[120:123], v[88:91]\n"
"ds_read_b64_tr_b16 a[30:31], v51 offset:6144\n"
"v_permlane32_swap_b32 v20, v21\n"
"s_mov_b32 s81, 0\n"
"s_waitcnt vmcnt(4)\n"
"s_waitcnt lgkmcnt(8)\n"
"v_mfma_f32_16x16x32_bf16 v[128:131], a[0:3], a[128:131], 0\n"
"ds_read_b64_tr_b16 a[32:33], v48 offset:8192\n"
"v_add_f32 v20, v20, v21\n"
"v_mfma_f32_16x16x32_bf16 v[132:135], a[0:3], a[132:135], 0\n"
"ds_read_b64_tr_b16 a[34:35], v48 offset:10240\n"
"v_cvt_pk_bf16_f32 v20, v20, v20\n"
"v_mfma_f32_16x16x32_bf16 v[140:143], a[4:7], a[132:135], 0\n"
"ds_read_b64_tr_b16 a[36:37], v49 offset:8192\n"
"v_and_b32 v20, 0xffff0000, v20\n"
"v_mfma_f32_16x16x32_bf16 v[136:139], a[4:7], a[128:131], 0\n"
"ds_read_b64_tr_b16 a[38:39], v49 offset:10240\n"
"v_mul_f32 v20, 0xbfb8aa3b, v20\n"
"v_mfma_f32_16x16x32_bf16 v[144:147], a[8:11], a[128:131], 0\n"
"ds_read_b64_tr_b16 a[40:41], v50 offset:8192\n"
"v_exp_f32 v20, v20\n"
"v_mfma_f32_16x16x32_bf16 v[148:151], a[8:11], a[132:135], 0\n"
"ds_read_b64_tr_b16 a[42:43], v50 offset:10240\n"
"v_add_f32 v20, 1.0, v20\n"
"v_mfma_f32_16x16x32_bf16 v[156:159], a[12:15], a[132:135], 0\n"
"ds_read_b64_tr_b16 a[44:45], v51 offset:8192\n"
"v_rcp_f32 v20, v20\n"
"v_mfma_f32_16x16x32_bf16 v[152:155], a[12:15], a[128:131], 0\n"
"ds_read_b64_tr_b16 a[46:47], v51 offset:10240\n"
"v_cvt_pk_bf16_f32 v24, v20, v20\n"
"s_waitcnt lgkmcnt(8)\n"
"v_mfma_f32_16x16x32_bf16 v[128:131], a[16:19], a[136:139], v[128:131]\n"
"ds_read_b64_tr_b16 a[48:49], v48 offset:12288\n"
"v_and_b32 v24, 0xffff0000, v24\n"
"buffer_load_dwordx4 v[160:163], v52, s[60:63], s68 offen offset:0 sc1\n"
"v_mfma_f32_16x16x32_bf16 v[132:135], a[16:19], a[140:143], v[132:135]\n"
"ds_read_b64_tr_b16 a[50:51], v48 offset:14336\n"
"v_sub_f32 v26, 1.0, v24\n"
"buffer_load_dwordx4 v[164:167], v52, s[60:63], s68 offen offset:1024 sc1\n"
"v_mfma_f32_16x16x32_bf16 v[140:143], a[20:23], a[140:143], v[140:143]\n"
"ds_read_b64_tr_b16 a[52:53], v49 offset:12288\n"
"v_cvt_pk_bf16_f32 v26, v26, v26\n"
"buffer_load_dwordx4 v[168:171], v52, s[60:63], s68 offen offset:2048 sc1\n"
"v_mfma_f32_16x16x32_bf16 v[136:139], a[20:23], a[136:139], v[136:139]\n"
"ds_read_b64_tr_b16 a[54:55], v49 offset:14336\n"
"v_and_b32 v26, 0xffff0000, v26\n"
"buffer_load_dwordx4 v[172:175], v52, s[60:63], s68 offen offset:3072 sc1\n"
"v_mfma_f32_16x16x32_bf16 v[144:147], a[24:27], a[136:139], v[144:147]\n"
"ds_read_b64_tr_b16 a[56:57], v50 offset:12288\n"
"v_mfma_f32_16x16x32_bf16 v[148:151], a[24:27], a[140:143], v[148:151]\n"
"ds_read_b64_tr_b16 a[58:59], v50 offset:14336\n"
"v_mfma_f32_16x16x32_bf16 v[156:159], a[28:31], a[140:143], v[156:159]\n"
"ds_read_b64_tr_b16 a[60:61], v51 offset:12288\n"
"v_mfma_f32_16x16x32_bf16 v[152:155], a[28:31], a[136:139], v[152:155]\n"
"ds_read_b64_tr_b16 a[62:63], v51 offset:14336\n"
"s_waitcnt lgkmcnt(8)\n"
"v_mfma_f32_16x16x32_bf16 v[128:131], a[32:35], a[144:147], v[128:131]\n"
"v_mfma_f32_16x16x32_bf16 v[132:135], a[32:35], a[148:151], v[132:135]\n"
"v_mfma_f32_16x16x32_bf16 v[140:143], a[36:39], a[148:151], v[140:143]\n"
"v_mfma_f32_16x16x32_bf16 v[136:139], a[36:39], a[144:147], v[136:139]\n"
"v_mfma_f32_16x16x32_bf16 v[144:147], a[40:43], a[144:147], v[144:147]\n"
"v_mfma_f32_16x16x32_bf16 v[148:151], a[40:43], a[148:151], v[148:151]\n"
"v_mfma_f32_16x16x32_bf16 v[156:159], a[44:47], a[148:151], v[156:159]\n"
"v_mfma_f32_16x16x32_bf16 v[152:155], a[44:47], a[144:147], v[152:155]\n"
"s_waitcnt lgkmcnt(0)\n"
"v_mfma_f32_16x16x32_bf16 v[128:131], a[48:51], a[152:155], v[128:131]\n"
"v_mfma_f32_16x16x32_bf16 v[132:135], a[48:51], a[156:159], v[132:135]\n"
"v_mfma_f32_16x16x32_bf16 v[140:143], a[52:55], a[156:159], v[140:143]\n"
"v_mfma_f32_16x16x32_bf16 v[136:139], a[52:55], a[152:155], v[136:139]\n"
"v_mfma_f32_16x16x32_bf16 v[144:147], a[56:59], a[152:155], v[144:147]\n"
"v_mfma_f32_16x16x32_bf16 v[148:151], a[56:59], a[156:159], v[148:151]\n"
"v_mfma_f32_16x16x32_bf16 v[156:159], a[60:63], a[156:159], v[156:159]\n"
"v_mfma_f32_16x16x32_bf16 v[152:155], a[60:63], a[152:155], v[152:155]\n"
"v_pk_mul_f32 v[64:65], v[64:65], v[30:31] op_sel_hi:[1,0]\n"
"v_pk_mul_f32 v[66:67], v[66:67], v[30:31] op_sel_hi:[1,0]\n"
"v_pk_mul_f32 v[68:69], v[68:69], v[26:27] op_sel_hi:[1,0]\n"
"v_pk_mul_f32 v[70:71], v[70:71], v[26:27] op_sel_hi:[1,0]\n"
"v_pk_mul_f32 v[72:73], v[72:73], v[30:31] op_sel_hi:[1,0]\n"
"v_pk_mul_f32 v[74:75], v[74:75], v[30:31] op_sel_hi:[1,0]\n"
"v_pk_mul_f32 v[76:77], v[76:77], v[26:27] op_sel_hi:[1,0]\n"
"v_pk_mul_f32 v[78:79], v[78:79], v[26:27] op_sel_hi:[1,0]\n"
"v_pk_mul_f32 v[80:81], v[80:81], v[30:31] op_sel_hi:[1,0]\n"
"v_pk_mul_f32 v[82:83], v[82:83], v[30:31] op_sel_hi:[1,0]\n"
"v_pk_mul_f32 v[84:85], v[84:85], v[26:27] op_sel_hi:[1,0]\n"
"v_pk_mul_f32 v[86:87], v[86:87], v[26:27] op_sel_hi:[1,0]\n"
"v_pk_mul_f32 v[88:89], v[88:89], v[30:31] op_sel_hi:[1,0]\n"
"v_pk_mul_f32 v[90:91], v[90:91], v[30:31] op_sel_hi:[1,0]\n"
"v_pk_mul_f32 v[92:93], v[92:93], v[26:27] op_sel_hi:[1,0]\n"
"v_pk_mul_f32 v[94:95], v[94:95], v[26:27] op_sel_hi:[1,0]\n"
"v_pk_fma_f32 v[128:129], v[128:129], v[28:29], v[64:65] op_sel_hi:[1,0,1]\n"
"v_pk_fma_f32 v[130:131], v[130:131], v[28:29], v[66:67] op_sel_hi:[1,0,1]\n"
"v_pk_fma_f32 v[132:133], v[132:133], v[24:25], v[68:69] op_sel_hi:[1,0,1]\n"
"v_pk_fma_f32 v[134:135], v[134:135], v[24:25], v[70:71] op_sel_hi:[1,0,1]\n"
"v_pk_fma_f32 v[136:137], v[136:137], v[28:29], v[72:73] op_sel_hi:[1,0,1]\n"
"v_pk_fma_f32 v[138:139], v[138:139], v[28:29], v[74:75] op_sel_hi:[1,0,1]\n"
"v_pk_fma_f32 v[140:141], v[140:141], v[24:25], v[76:77] op_sel_hi:[1,0,1]\n"
"v_pk_fma_f32 v[142:143], v[142:143], v[24:25], v[78:79] op_sel_hi:[1,0,1]\n"
"v_pk_fma_f32 v[144:145], v[144:145], v[28:29], v[80:81] op_sel_hi:[1,0,1]\n"
"v_pk_fma_f32 v[146:147], v[146:147], v[28:29], v[82:83] op_sel_hi:[1,0,1]\n"
"v_pk_fma_f32 v[148:149], v[148:149], v[24:25], v[84:85] op_sel_hi:[1,0,1]\n"
"v_pk_fma_f32 v[150:151], v[150:151], v[24:25], v[86:87] op_sel_hi:[1,0,1]\n"
"v_pk_fma_f32 v[152:153], v[152:153], v[28:29], v[88:89] op_sel_hi:[1,0,1]\n"
"v_pk_fma_f32 v[154:155], v[154:155], v[28:29], v[90:91] op_sel_hi:[1,0,1]\n"
"v_pk_fma_f32 v[156:157], v[156:157], v[24:25], v[92:93] op_sel_hi:[1,0,1]\n"
"v_pk_fma_f32 v[158:159], v[158:159], v[24:25], v[94:95] op_sel_hi:[1,0,1]\n"
"v_xor_b32 v57, 0, v55\n"
"v_xor_b32 v58, 32, v55\n"
"v_xor_b32 v59, 64, v55\n"
"v_xor_b32 v60, 96, v55\n"
".Lg2rx:\n"
"s_waitcnt vmcnt(0)\n"
"v_max3_u32 v20, v160, v161, v162\n"
"v_max3_u32 v21, v163, v164, v165\n"
"v_max3_u32 v21, v166, v167, v21\n"
"v_max3_u32 v20, v168, v169, v20\n"
"v_max3_u32 v21, v170, v171, v21\n"
"v_max3_u32 v20, v172, v173, v20\n"
"v_max3_u32 v21, v174, v175, v21\n"
"v_max_u32 v20, v20, v21\n"
"v_cmp_eq_u32 vcc, -1, v20\n"
"s_add_u32 s81, s81, 1\n"
"s_nop 0\n"
"s_cmp_eq_u64 vcc, 0\n"
"s_cbranch_scc0 .Lg2rx2\n"
".Lg2rxd:\n"
"v_lshlrev_b32 v176, 16, v160\n"
"v_and_b32 v177, 0xffff0000, v160\n"
"v_lshlrev_b32 v178, 16, v161\n"
"v_and_b32 v179, 0xffff0000, v161\n"
"v_lshlrev_b32 v180, 16, v162\n"
"v_and_b32 v181, 0xffff0000, v162\n"
"v_lshlrev_b32 v182, 16, v163\n"
"v_and_b32 v183, 0xffff0000, v163\n"
"v_lshlrev_b32 v184, 16, v164\n"
"v_and_b32 v185, 0xffff0000, v164\n"
"v_lshlrev_b32 v186, 16, v165\n"
"v_and_b32 v187, 0xffff0000, v165\n"
"v_lshlrev_b32 v188, 16, v166\n"
"v_and_b32 v189, 0xffff0000, v166\n"
"v_lshlrev_b32 v190, 16, v167\n"
"v_and_b32 v191, 0xffff0000, v167\n"
"v_pk_fma_f32 v[128:129], v[176:177], v[30:31], v[128:129] op_sel_hi:[1,0,1]\n"
"v_pk_fma_f32 v[130:131], v[178:179], v[30:31], v[130:131] op_sel_hi:[1,0,1]\n"
"v_pk_fma_f32 v[132:133], v[180:181], v[26:27], v[132:133] op_sel_hi:[1,0,1]\n"
"v_pk_fma_f32 v[134:135], v[182:183], v[26:27], v[134:135] op_sel_hi:[1,0,1]\n"
"v_pk_fma_f32 v[136:137], v[184:185], v[30:31], v[136:137] op_sel_hi:[1,0,1]\n"
"v_pk_fma_f32 v[138:139], v[186:187], v[30:31], v[138:139] op_sel_hi:[1,0,1]\n"
"v_pk_fma_f32 v[140:141], v[188:189], v[26:27], v[140:141] op_sel_hi:[1,0,1]\n"
"v_pk_fma_f32 v[142:143], v[190:191], v[26:27], v[142:143] op_sel_hi:[1,0,1]\n"
"v_lshlrev_b32 v176, 16, v168\n"
"v_and_b32 v177, 0xffff0000, v168\n"
"v_lshlrev_b32 v178, 16, v169\n"
"v_and_b32 v179, 0xffff0000, v169\n"
"v_lshlrev_b32 v180, 16, v170\n"
"v_and_b32 v181, 0xffff0000, v170\n"
"v_lshlrev_b32 v182, 16, v171\n"
"v_and_b32 v183, 0xffff0000, v171\n"
"v_lshlrev_b32 v184, 16, v172\n"
"v_and_b32 v185, 0xffff0000, v172\n"
"v_lshlrev_b32 v186, 16, v173\n"
"v_and_b32 v187, 0xffff0000, v173\n"
"v_lshlrev_b32 v188, 16, v174\n"
"v_and_b32 v189, 0xffff0000, v174\n"
"v_lshlrev_b32 v190, 16, v175\n"
"v_and_b32 v191, 0xffff0000, v175\n"
"v_pk_fma_f32 v[144:145], v[176:177], v[30:31], v[144:145] op_sel_hi:[1,0,1]\n"
"v_pk_fma_f32 v[146:147], v[178:179], v[30:31], v[146:147] op_sel_hi:[1,0,1]\n"
"v_pk_fma_f32 v[148:149], v[180:181], v[26:27], v[148:149] op_sel_hi:[1,0,1]\n"
"v_pk_fma_f32 v[150:151], v[182:183], v[26:27], v[150:151] op_sel_hi:[1,0,1]\n"
"v_pk_fma_f32 v[152:153], v[184:185], v[30:31], v[152:153] op_sel_hi:[1,0,1]\n"
"v_pk_fma_f32 v[154:155], v[186:187], v[30:31], v[154:155] op_sel_hi:[1,0,1]\n"
"v_pk_fma_f32 v[156:157], v[188:189], v[26:27], v[156:157] op_sel_hi:[1,0,1]\n"
"v_pk_fma_f32 v[158:159], v[190:191], v[26:27], v[158:159] op_sel_hi:[1,0,1]\n"
"v_cvt_pk_bf16_f32 v192, v128, v129\n"
"v_cvt_pk_bf16_f32 v193, v130, v131\n"
"v_cvt_pk_bf16_f32 v194, v132, v133\n"
"v_cvt_pk_bf16_f32 v195, v134, v135\n"
"v_cvt_pk_bf16_f32 v196, v136, v137\n"
"v_cvt_pk_bf16_f32 v197, v138, v139\n"
"v_cvt_pk_bf16_f32 v198, v140, v141\n"
"v_cvt_pk_bf16_f32 v199, v142, v143\n"
"v_cvt_pk_bf16_f32 v200, v144, v145\n"
"v_cvt_pk_bf16_f32 v201, v146, v147\n"
"v_cvt_pk_bf16_f32 v202, v148, v149\n"
"v_cvt_pk_bf16_f32 v203, v150, v151\n"
"v_cvt_pk_bf16_f32 v204, v152, v153\n"
"v_cvt_pk_bf16_f32 v205, v154, v155\n"
"v_cvt_pk_bf16_f32 v206, v156, v157\n"
"v_cvt_pk_bf16_f32 v207, v158, v159\n"
"s_add_u32 s82, s71, 0\n"
"s_add_u32 s83, s71, 262144\n"
"ds_write_b64 v57, v[192:193] offset:0\n"
"ds_write_b64 v57, v[194:195] offset:2048\n"
"ds_write_b64 v58, v[196:197] offset:0\n"
"ds_write_b64 v58, v[198:199] offset:2048\n"
"ds_write_b64 v59, v[200:201] offset:0\n"
"ds_write_b64 v59, v[202:203] offset:2048\n"
"ds_write_b64 v60, v[204:205] offset:0\n"
"ds_write_b64 v60, v[206:207] offset:2048\n"
"ds_read_b128 v[208:211], v54 offset:0\n"
"ds_read_b128 v[212:215], v54 offset:1024\n"
"ds_read_b128 v[216:219], v54 offset:2048\n"
"ds_read_b128 v[220:223], v54 offset:3072\n"
"s_waitcnt lgkmcnt(3)\n"
"buffer_store_dwordx4 v[208:211], v53, s[32:35], s82 offen \n"
"s_nop 1\n"
"s_add_u32 s82, s71, 524288\n"
"s_waitcnt lgkmcnt(2)\n"
"buffer_store_dwordx4 v[212:215], v53, s[32:35], s83 offen \n"
"s_nop 1\n"
"s_add_u32 s83, s71, 786432\n"
"s_waitcnt lgkmcnt(1)\n"
"buffer_store_dwordx4 v[216:219], v53, s[32:35], s82 offen \n"
"s_waitcnt lgkmcnt(0)\n"
"buffer_store_dwordx4 v[220:223], v53, s[32:35], s83 offen \n"
"s_nop 1\n"
"s_endpgm\n"
".Lg2rx2:\n"
"s_cmp_gt_u32 s81, 100000\n"
"s_cbranch_scc1 .Lg2rxd\n"
"buffer_load_dwordx4 v[160:163], v52, s[60:63], s68 offen offset:0 sc1\n"
"buffer_load_dwordx4 v[164:167], v52, s[60:63], s68 offen offset:1024 sc1\n"
"buffer_load_dwordx4 v[168:171], v52, s[60:63], s68 offen offset:2048 sc1\n"
"buffer_load_dwordx4 v[172:175], v52, s[60:63], s68 offen offset:3072 sc1\n"
"s_branch .Lg2rx\n"
".Lg2fp:\n"
"v_lshrrev_b32 v15, 1, v10\n"
"s_mov_b64 exec, 0xffff\n"
"buffer_store_dwordx2 v[12:13], v15, s[28:31], s65 offen sc1\n"
"s_mov_b64 exec, -1\n"
"s_branch .Lg2flb\n"
".Lg2fl:\n"
"s_cmp_gt_u32 s76, 4\n"
"s_cbranch_scc1 .Lg2fp\n"
"s_cmp_eq_u32 s76, 1\n"
"s_cbranch_scc1 .Lg2fl1\n"
"s_add_u32 s82, s77, 131072\n"
"s_mov_b32 s84, 0x0fffffff\n"
"buffer_load_dwordx2 v[14:15], v38, s[28:31], s82 offen sc1\n"
"s_branch .Lg2flb\n"
".Lg2fl1:\n"
"s_mov_b32 s82, 0\n"
"s_mov_b32 s84, -1\n"
"s_mov_b32 s85, 1\n"
"v_mov_b32 v38, v56\n"
"buffer_load_dwordx2 v[14:15], v56, s[28:31], 0 offen sc1\n"
"s_branch .Lg2flb\n"
".Lg2slow:\n"
"s_mov_b32 s81, 0\n"
".Lpolls:\n"
"buffer_load_dwordx2 v[14:15], v38, s[28:31], s82 offen sc1\n"
"s_add_u32 s81, s81, 1\n"
"s_waitcnt vmcnt(0)\n"
"v_cmp_eq_u64 vcc, v[14:15], v[12:13]\n"
"s_cmp_gt_u32 s81, 100000\n"
"s_cbranch_scc1 .Lpollds\n"
"s_andn2_b64 s[86:87], s[84:85], vcc\n"
"s_cbranch_scc0 .Lpollds\n"
"s_sleep 2\n"
"s_branch .Lpolls\n"
".Lpollds:\n"
"s_branch .Lg2back\n"
".Lg1slow:\n"
"s_mov_b32 s81, 0\n"
".Lpollg:\n"
"buffer_load_dwordx2 v[14:15], v38, s[28:31], s77 offen sc1\n"
"s_add_u32 s81, s81, 1\n"
"s_waitcnt vmcnt(0)\n"
"v_cmp_eq_u64 vcc, v[14:15], v[12:13]\n"
"s_cmp_gt_u32 s81, 100000\n"
"s_cbranch_scc1 .Lpolldg\n"
"s_andn2_b64 s[86:87], s[84:85], vcc\n"
"s_cbranch_scc0 .Lpolldg\n"
"s_sleep 2\n"
"s_branch .Lpollg\n"
".Lpolldg:\n"
"s_branch .Lg1back\n"
".Lg2end:\n"
".Lfft0:\n"
  :: "{s[16:19]}"(kb_),"{s[20:23]}"(mksrd(TAB,sizeof(TAB))),"{v0}"(tid)
  : "v2","v3","v4","v5","v6","v7","v8","v9","v10","v11","v12","v13","v14","v15","v16","v17","v18","v19","v20","v21","v22","v23","v24","v25","v26","v27","v28","v29","v30","v31","v32","v33","v34","v35","v36","v37","v38","v39","v40","v41","v42","v43","v44","v45","v46","v47","v48","v49","v50","v51","v52","v53","v54","v55","v56","v57","v58","v59","v60","v61","v62","v63","v64","v65","v66","v67","v68","v69","v70","v71","v72","v73","v74","v75","v76","v77","v78","v79","v80","v81","v82","v83","v84","v85","v86","v87","v88","v89","v90","v91","v92","v93","v94","v95","v96","v97","v98","v99","v100","v101","v102","v103","v104","v105","v106","v107","v108","v109","v110","v111","v112","v113","v114","v115","v116","v117","v118","v119","v120","v121","v122","v123","v124","v125","v126","v127","v128","v129","v130","v131","v132","v133","v134","v135","v136","v137","v138","v139","v140","v141","v142","v143","v144","v145","v146","v147","v148","v149","v150","v151","v152","v153","v154","v155","v156","v157","v158","v159","v160","v161","v162","v163","v164","v165","v166","v167","v168","v169","v170","v171","v172","v173","v174","v175","v176","v177","v178","v179","v180","v181","v182","v183","v184","v185","v186","v187","v188","v189","v190","v191","v192","v193","v194","v195","v196","v197","v198","v199","v200","v201","v202","v203","v204","v205","v206","v207","v208","v209","v210","v211","v212","v213","v214","v215","v216","v217","v218","v219","v220","v221","v222","v223","v224","v225","v226","v227","v228","v229","v230","v231","v232","v233","v234","v235","v236","v237","v238","v239","v240","v241","v242","v243","v244","v245","v246","v247","v248","v249","v250","v251","v252","v253","v254","v255","a0","a1","a2","a3","a4","a5","a6","a7","a8","a9","a10","a11","a12","a13","a14","a15","a16","a17","a18","a19","a20","a21","a22","a23","a24","a25","a26","a27","a28","a29","a30","a31","a32","a33","a34","a35","a36","a37","a38","a39","a40","a41","a42","a43","a44","a45","a46","a47","a48","a49","a50","a51","a52","a53","a54","a55","a56","a57","a58","a59","a60","a61","a62","a63","a64","a65","a66","a67","a68","a69","a70","a71","a72","a73","a74","a75","a76","a77","a78","a79","a80","a81","a82","a83","a84","a85","a86","a87","a88","a89","a90","a91","a92","a93","a94","a95","a96","a97","a98","a99","a100","a101","a102","a103","a104","a105","a106","a107","a108","a109","a110","a111","a112","a113","a114","a115","a116","a117","a118","a119","a120","a121","a122","a123","a124","a125","a126","a127","a128","a129","a130","a131","a132","a133","a134","a135","a136","a137","a138","a139","a140","a141","a142","a143","a144","a145","a146","a147","a148","a149","a150","a151","a152","a153","a154","a155","a156","a157","a158","a159","a160","a161","a162","a163","a164","a165","a166","a167","a168","a169","a170","a171","a172","a173","a174","a175","a176","a177","a178","a179","a180","a181","a182","a183","a184","a185","a186","a187","a188","a189","a190","a191","a192","a193","a194","a195","a196","a197","a198","a199","a200","a201","a202","a203","a204","a205","a206","a207","a208","a209","a210","a211","a212","a213","a214","a215","a216","a217","a218","a219","a220","a221","a222","a223","s99","vcc","scc","memory");
  unsigned junk1=0;
  const u32x4 srdx=mksrd(x,2048*1024*2), srdm=mksrd(ws,WS_MISC), srd0=mksrd(ws+WS_T0,4u<<20), srdg=mksrd(gw,2048), srtb=mksrd(TAB,sizeof(TAB));
  u32x2 vp; vp[0]=(l>>3)*32768+((l&7)^(((l>>4)&1)<<2))*16; vp[1]=l*16;
  const u64 ka=(u64)__builtin_amdgcn_kernarg_segment_ptr(); u32x4 kb; kb[0]=(unsigned)ka; kb[1]=(unsigned)(ka>>32); kb[2]=0; kb[3]=b;
  {
    u32x4 tg;
    const unsigned e=cu*8+xcd, s1j=e>>1, cj=14+(e&1), uA=e, uB=e+32; const bool sp0=(e==0 && w==0), u0=(e==0);
    u32x4 sp; sp[0]=cj*128; sp[1]=s1j*2048+cj*128+w*262144; sp[2]=w*1024; sp[3]=0;
    tg[0]=tg[1]=0; tg[2]=16*w*65536+s1j*4096+cj*128; tg[3]=WS_Z+cj*8192+s1j*512+w*128;
    asm volatile(
"s_getpc_b64 s[94:95]\n"
".Lpff:\n"
"s_add_u32 s94, s94, .Lpff-.Lpff\n"
"s_addc_u32 s95, s95, (.Lpff-.Lpff)>>63\n"
"v_lshlrev_b32 v132, 7, %[tid]\n"
"v_min_u32 v132, .Lfftend-(.Lpff)-4, v132\n"
"global_load_dword a190, v132, s[94:95]\n"
"s_add_u32 m0, s98, 131072\n"
"s_add_u32 s99, s97, 0\n"
"buffer_load_dwordx4 v130, s[40:43], s99 offen lds\n"
"s_add_u32 m0, s98, 135168\n"
"s_add_u32 s99, s97, 1048576\n"
"buffer_load_dwordx4 v130, s[40:43], s99 offen lds\n"
"s_add_u32 m0, s98, 139264\n"
"s_add_u32 s99, s97, 2097152\n"
"buffer_load_dwordx4 v130, s[40:43], s99 offen lds\n"
"s_add_u32 m0, s98, 143360\n"
"s_add_u32 s99, s97, 3145728\n"
"buffer_load_dwordx4 v130, s[40:43], s99 offen lds\n"
"s_load_dwordx2 s[36:37], s[28:29], 0x0\n"
"v_and_b32 v251, 0x300, v131\n"
"v_lshrrev_b32 v251, 4, v251\n"
"buffer_load_dwordx4 v[112:115], v251, s[32:35], s96 offen offset:0\n"
"buffer_load_dwordx4 v[116:119], v251, s[32:35], s96 offen offset:64\n"
"s_and_b32 s99, s19, 31\n"
"s_lshl_b32 s99, s99, 11\n"
"s_lshr_b32 s95, s98, 1\n"
"s_add_u32 s99, s99, s95\n"
"v_min_u32 v132, 48, v131\n"
"v_lshlrev_b32 v132, 3, v132\n"
"buffer_load_dword a189, v132, s[20:23], s99 offen\n"
"v_lshrrev_b32 v168, 4, v131\n"
"v_and_b32 v170, 31, v168\n"
"s_lshr_b32 s99, s98, 5\n"
"v_add_u32 v176, s99, v170\n"
"v_cmp_eq_u32 vcc, 1, v176\n"
"v_lshrrev_b32 v177, 1, v176\n"
"v_and_b32 v178, 1, v176\n"
"v_lshrrev_b32 v169, 5, v168\n"
"v_cndmask_b32_e64 v177, v177, 64, vcc\n"
"v_cndmask_b32_e64 v178, v178, 0, vcc\n"
"v_cvt_f32_u32 v179, v169\n"
"v_cvt_f32_u32 v177, v177\n"
"v_cvt_f32_u32 v178, v178\n"
"v_mul_f32 v128, 0x3c000000, v177\n"
"v_mul_f32 v178, 0x3e800000, v178\n"
"v_mul_f32 v179, 0x41000000, v179\n"
"v_fma_f32 v129, v179, v128, v178\n"
"v_mov_b32 v180, v129\n"
"v_fmamk_f32 v181, v128, 0x3f800000, v129\n"
"v_fmamk_f32 v182, v128, 0x40000000, v129\n"
"v_fmamk_f32 v183, v128, 0x40400000, v129\n"
"v_fmamk_f32 v184, v128, 0x40800000, v129\n"
"v_fmamk_f32 v185, v128, 0x40a00000, v129\n"
"v_fmamk_f32 v186, v128, 0x40c00000, v129\n"
"v_fmamk_f32 v187, v128, 0x40e00000, v129\n"
"v_fmamk_f32 v188, v128, 0x41800000, v129\n"
"v_fmamk_f32 v189, v128, 0x41880000, v129\n"
"v_fmamk_f32 v190, v128, 0x41900000, v129\n"
"v_fmamk_f32 v191, v128, 0x41980000, v129\n"
"v_fmamk_f32 v192, v128, 0x41a00000, v129\n"
"v_fmamk_f32 v193, v128, 0x41a80000, v129\n"
"v_fmamk_f32 v194, v128, 0x41b00000, v129\n"
"v_fmamk_f32 v195, v128, 0x41b80000, v129\n"
"v_and_b32 v172, 15, v168\n"
"v_bfe_u32 v125, v168, 1, 1\n"
"s_mov_b32 s95, 0x80008000\n"
"v_cos_f32 v180, v180\n"
"v_cos_f32 v181, v181\n"
"v_cos_f32 v182, v182\n"
"v_cos_f32 v183, v183\n"
"v_cos_f32 v184, v184\n"
"v_cos_f32 v185, v185\n"
"v_cos_f32 v186, v186\n"
"v_cos_f32 v187, v187\n"
"v_cos_f32 v188, v188\n"
"v_cos_f32 v189, v189\n"
"v_cos_f32 v190, v190\n"
"v_cos_f32 v191, v191\n"
"v_cos_f32 v192, v192\n"
"v_cos_f32 v193, v193\n"
"v_cos_f32 v194, v194\n"
"v_cos_f32 v195, v195\n"
"v_lshrrev_b32 v173, 2, v172\n"
"v_bfe_u32 v174, v168, 1, 1\n"
"v_bfe_u32 v175, v168, 4, 1\n"
"v_mul_lo_u32 v125, v125, s95\n"
"s_lshl_b32 s99, s98, 2\n"
"v_lshl_or_b32 v174, v175, 1, v174\n"
"v_lshrrev_b32 v197, 1, v173\n"
"s_add_u32 s95, s99, 0x20000\n"
"v_cvt_pk_bf16_f32 v136, v180, v181\n"
"v_cvt_pk_bf16_f32 v137, v182, v183\n"
"v_cvt_pk_bf16_f32 v138, v184, v185\n"
"v_cvt_pk_bf16_f32 v139, v186, v187\n"
"v_cvt_pk_bf16_f32 v140, v188, v189\n"
"v_cvt_pk_bf16_f32 v141, v190, v191\n"
"v_cvt_pk_bf16_f32 v142, v192, v193\n"
"v_cvt_pk_bf16_f32 v143, v194, v195\n"
"v_and_b32 v175, 1, v168\n"
"v_lshlrev_b32 v197, 2, v197\n"
"v_bfe_u32 v198, v168, 1, 1\n"
"s_add_u32 s94, s99, 0x24000\n"
"v_and_b32 v199, 7, v168\n"
"v_fmamk_f32 v180, v128, 0x42000000, v129\n"
"v_fmamk_f32 v181, v128, 0x42040000, v129\n"
"v_fmamk_f32 v182, v128, 0x42080000, v129\n"
"v_fmamk_f32 v183, v128, 0x420c0000, v129\n"
"v_fmamk_f32 v184, v128, 0x42100000, v129\n"
"v_fmamk_f32 v185, v128, 0x42140000, v129\n"
"v_fmamk_f32 v186, v128, 0x42180000, v129\n"
"v_fmamk_f32 v187, v128, 0x421c0000, v129\n"
"v_fmamk_f32 v188, v128, 0x42400000, v129\n"
"v_fmamk_f32 v189, v128, 0x42440000, v129\n"
"v_fmamk_f32 v190, v128, 0x42480000, v129\n"
"v_fmamk_f32 v191, v128, 0x424c0000, v129\n"
"v_fmamk_f32 v192, v128, 0x42500000, v129\n"
"v_fmamk_f32 v193, v128, 0x42540000, v129\n"
"v_fmamk_f32 v194, v128, 0x42580000, v129\n"
"v_fmamk_f32 v195, v128, 0x425c0000, v129\n"
"v_lshrrev_b32 v171, 4, v168\n"
"v_lshlrev_b32 v175, 3, v175\n"
"v_xor_b32 v197, v197, v174\n"
"v_lshlrev_b32 v198, 2, v198\n"
"v_lshl_add_u32 v200, v199, 4, s94\n"
"v_lshrrev_b32 v201, 3, v168\n"
"v_cos_f32 v180, v180\n"
"v_cos_f32 v181, v181\n"
"v_cos_f32 v182, v182\n"
"v_cos_f32 v183, v183\n"
"v_cos_f32 v184, v184\n"
"v_cos_f32 v185, v185\n"
"v_cos_f32 v186, v186\n"
"v_cos_f32 v187, v187\n"
"v_cos_f32 v188, v188\n"
"v_cos_f32 v189, v189\n"
"v_cos_f32 v190, v190\n"
"v_cos_f32 v191, v191\n"
"v_cos_f32 v192, v192\n"
"v_cos_f32 v193, v193\n"
"v_cos_f32 v194, v194\n"
"v_cos_f32 v195, v195\n"
"v_lshl_add_u32 v196, v169, 3, v173\n"
"v_lshl_add_u32 v197, v197, 4, v175\n"
"v_xor_b32 v198, v198, v171\n"
"v_xor_b32 v199, v199, v201\n"
"v_lshl_add_u32 v197, v196, 7, v197\n"
"v_lshl_add_u32 v198, v198, 4, s95\n"
"v_lshlrev_b32 v199, 4, v199\n"
"v_and_b32 v201, 1, v201\n"
"v_cvt_pk_bf16_f32 v144, v180, v181\n"
"v_cvt_pk_bf16_f32 v145, v182, v183\n"
"v_cvt_pk_bf16_f32 v146, v184, v185\n"
"v_cvt_pk_bf16_f32 v147, v186, v187\n"
"v_cvt_pk_bf16_f32 v148, v188, v189\n"
"v_cvt_pk_bf16_f32 v149, v190, v191\n"
"v_cvt_pk_bf16_f32 v150, v192, v193\n"
"v_cvt_pk_bf16_f32 v151, v194, v195\n"
"v_add_u32 v120, 0x20000, v197\n"
"v_lshl_add_u32 v122, v172, 7, v198\n"
"v_lshl_add_u32 v200, v169, 3, v200\n"
"v_lshl_add_u32 v199, v201, 11, v199\n"
"v_xor_b32 v152, v125, v136\n"
"v_xor_b32 v153, v125, v137\n"
"v_xor_b32 v154, v125, v138\n"
"v_xor_b32 v155, v125, v139\n"
"v_xor_b32 v156, v125, v140\n"
"v_xor_b32 v157, v125, v141\n"
"v_xor_b32 v158, v125, v142\n"
"v_xor_b32 v159, v125, v143\n"
"v_xor_b32 v160, v125, v144\n"
"v_xor_b32 v161, v125, v145\n"
"v_xor_b32 v162, v125, v146\n"
"v_xor_b32 v163, v125, v147\n"
"v_xor_b32 v164, v125, v148\n"
"v_xor_b32 v165, v125, v149\n"
"v_xor_b32 v166, v125, v150\n"
"v_xor_b32 v167, v125, v151\n"
"v_xor_b32 v121, 64, v120\n"
"v_xor_b32 v123, 64, v122\n"
"v_lshl_add_u32 v248, v170, 7, v200\n"
"v_add_u32 v249, s94, v131\n"
"v_lshl_add_u32 v250, v171, 16, v199\n"
"s_nop 1\n"
"s_waitcnt vmcnt(0) lgkmcnt(0)\n"
"s_barrier\n"
"s_mul_i32 s99, s37, 0x7f4a7c15\n"
"s_mul_i32 s97, s36, 0x9e3779b9\n"
"s_add_u32 s99, s99, s97\n"
"s_mul_hi_u32 s97, s36, 0x7f4a7c15\n"
"s_add_u32 s99, s99, s97\n"
"s_mul_i32 s36, s36, 0x7f4a7c15\n"
"s_add_u32 s36, s36, 0xd192ed03\n"
"s_addc_u32 s37, s99, 0xd1b54a32\n"
"ds_read_b64_tr_b16 a[64:65], v120 offset:0\n"
"ds_read_b64_tr_b16 a[66:67], v120 offset:512\n"
"ds_read_b64_tr_b16 a[68:69], v121 offset:0\n"
"ds_read_b64_tr_b16 a[70:71], v121 offset:512\n"
"ds_read_b64_tr_b16 a[72:73], v120 offset:2048\n"
"ds_read_b64_tr_b16 a[74:75], v120 offset:2560\n"
"ds_read_b64_tr_b16 a[76:77], v121 offset:2048\n"
"ds_read_b64_tr_b16 a[78:79], v121 offset:2560\n"
"ds_read_b64_tr_b16 a[80:81], v120 offset:4096\n"
"ds_read_b64_tr_b16 a[82:83], v120 offset:4608\n"
"ds_read_b64_tr_b16 a[84:85], v121 offset:4096\n"
"ds_read_b64_tr_b16 a[86:87], v121 offset:4608\n"
"ds_read_b64_tr_b16 a[88:89], v120 offset:6144\n"
"v_xor_b32 v72, 0, v248\n"
"v_xor_b32 v73, 16, v248\n"
"v_xor_b32 v74, 32, v248\n"
"v_xor_b32 v75, 48, v248\n"
"v_xor_b32 v76, 64, v248\n"
"v_xor_b32 v77, 80, v248\n"
"v_xor_b32 v78, 96, v248\n"
"v_xor_b32 v79, 112, v248\n"
"s_add_u32 s97, s38, 0\n"
"s_add_u32 s99, s38, 262144\n"
"ds_read_b64_tr_b16 a[90:91], v120 offset:6656\n"
"ds_read_b64_tr_b16 a[92:93], v121 offset:6144\n"
"ds_read_b64_tr_b16 a[94:95], v121 offset:6656\n"
"ds_read_b64_tr_b16 a[96:97], v120 offset:8192\n"
"ds_read_b64_tr_b16 a[98:99], v120 offset:8704\n"
"ds_read_b64_tr_b16 a[100:101], v121 offset:8192\n"
"ds_read_b64_tr_b16 a[102:103], v121 offset:8704\n"
"ds_read_b64_tr_b16 a[104:105], v120 offset:10240\n"
"ds_read_b64_tr_b16 a[106:107], v120 offset:10752\n"
"ds_read_b64_tr_b16 a[108:109], v121 offset:10240\n"
"ds_read_b64_tr_b16 a[110:111], v121 offset:10752\n"
"s_waitcnt lgkmcnt(15)\n"
"v_mfma_f32_32x32x16_bf16 v[16:31], a[64:67], v[136:139], 0\n"
"ds_read_b64_tr_b16 a[112:113], v120 offset:12288\n"
"ds_read_b64_tr_b16 a[114:115], v120 offset:12800\n"
"ds_read_b64_tr_b16 a[116:117], v121 offset:12288\n"
"ds_read_b64_tr_b16 a[118:119], v121 offset:12800\n"
"v_mfma_f32_32x32x16_bf16 v[32:47], a[68:71], v[136:139], 0\n"
"ds_read_b64_tr_b16 a[120:121], v120 offset:14336\n"
"ds_read_b64_tr_b16 a[122:123], v120 offset:14848\n"
"ds_read_b64_tr_b16 a[124:125], v121 offset:14336\n"
"ds_read_b64_tr_b16 a[126:127], v121 offset:14848\n"
"v_mfma_f32_32x32x16_bf16 v[16:31], a[72:75], v[140:143], v[16:31]\n"
"ds_read_b128 v[96:99], v122 offset:0\n"
"v_mfma_f32_32x32x16_bf16 v[32:47], a[76:79], v[140:143], v[32:47]\n"
"ds_read_b128 v[104:107], v122 offset:2048\n"
"s_waitcnt lgkmcnt(15)\n"
"v_mfma_f32_32x32x16_bf16 v[16:31], a[80:83], v[144:147], v[16:31]\n"
"ds_read_b128 v[100:103], v123 offset:0\n"
"v_mfma_f32_32x32x16_bf16 v[32:47], a[84:87], v[144:147], v[32:47]\n"
"ds_read_b128 v[108:111], v123 offset:2048\n"
"v_mfma_f32_32x32x16_bf16 v[16:31], a[88:91], v[148:151], v[16:31]\n"
"v_mfma_f32_32x32x16_bf16 v[32:47], a[92:95], v[148:151], v[32:47]\n"
"v_mfma_f32_32x32x16_bf16 v[16:31], a[96:99], v[152:155], v[16:31]\n"
"s_waitcnt lgkmcnt(15)\n"
"v_mfma_f32_32x32x16_bf16 v[32:47], a[100:103], v[152:155], v[32:47]\n"
"s_waitcnt lgkmcnt(14)\n"
"v_mfma_f32_32x32x16_bf16 v[16:31], a[104:107], v[156:159], v[16:31]\n"
"s_waitcnt lgkmcnt(12)\n"
"v_mfma_f32_32x32x16_bf16 v[32:47], a[108:111], v[156:159], v[32:47]\n"
"s_waitcnt lgkmcnt(10)\n"
"v_mfma_f32_32x32x16_bf16 v[16:31], a[112:115], v[160:163], v[16:31]\n"
"s_waitcnt lgkmcnt(8)\n"
"v_mfma_f32_32x32x16_bf16 v[32:47], a[116:119], v[160:163], v[32:47]\n"
"s_waitcnt lgkmcnt(6)\n"
"v_mfma_f32_32x32x16_bf16 v[16:31], a[120:123], v[164:167], v[16:31]\n"
"s_waitcnt lgkmcnt(4)\n"
"v_mfma_f32_32x32x16_bf16 v[32:47], a[124:127], v[164:167], v[32:47]\n"
"s_nop 9\n"
"v_cvt_pk_bf16_f32 v56, v16, v17\n"
"v_cvt_pk_bf16_f32 v57, v18, v19\n"
"s_waitcnt lgkmcnt(3)\n"
"v_mfma_f32_16x16x32_bf16 v[240:243], v[96:99], v[112:115], 0\n"
"ds_write_b64 v72, v[56:57]\n"
"s_waitcnt lgkmcnt(3)\n"
"v_mfma_f32_16x16x32_bf16 v[244:247], v[104:107], v[112:115], 0\n"
"v_cvt_pk_bf16_f32 v58, v20, v21\n"
"v_cvt_pk_bf16_f32 v59, v22, v23\n"
"s_waitcnt lgkmcnt(2)\n"
"v_mfma_f32_16x16x32_bf16 v[240:243], v[100:103], v[116:119], v[240:243]\n"
"ds_write_b64 v73, v[58:59]\n"
"s_waitcnt lgkmcnt(2)\n"
"v_mfma_f32_16x16x32_bf16 v[244:247], v[108:111], v[116:119], v[244:247]\n"
"v_cvt_pk_bf16_f32 v60, v24, v25\n"
"v_cvt_pk_bf16_f32 v61, v26, v27\n"
"v_cvt_pk_bf16_f32 v62, v28, v29\n"
"v_cvt_pk_bf16_f32 v63, v30, v31\n"
"v_cvt_pk_bf16_f32 v64, v32, v33\n"
"v_cvt_pk_bf16_f32 v65, v34, v35\n"
"v_cvt_pk_bf16_f32 v66, v36, v37\n"
"v_cvt_pk_bf16_f32 v67, v38, v39\n"
"v_cvt_pk_bf16_f32 v68, v40, v41\n"
"v_cvt_pk_bf16_f32 v69, v42, v43\n"
"v_cvt_pk_bf16_f32 v70, v44, v45\n"
"v_cvt_pk_bf16_f32 v71, v46, v47\n"
"ds_write_b64 v74, v[60:61]\n"
"ds_write_b64 v75, v[62:63]\n"
"ds_write_b64 v76, v[64:65]\n"
"ds_write_b64 v77, v[66:67]\n"
"ds_write_b64 v78, v[68:69]\n"
"ds_write_b64 v79, v[70:71]\n"
"ds_read_b128 v[224:227], v249 offset:0\n"
"ds_read_b128 v[228:231], v249 offset:1024\n"
"ds_read_b128 v[232:235], v249 offset:2048\n"
"ds_read_b128 v[236:239], v249 offset:3072\n"
"s_waitcnt lgkmcnt(3)\n"
"buffer_store_dwordx4 v[224:227], v250, s[24:27], s97 offen sc1\n"
"s_nop 1\n"
"s_add_u32 s97, s38, 524288\n"
"s_waitcnt lgkmcnt(2)\n"
"buffer_store_dwordx4 v[228:231], v250, s[24:27], s99 offen sc1\n"
"s_nop 1\n"
"s_add_u32 s99, s38, 786432\n"
"s_waitcnt lgkmcnt(1)\n"
"buffer_store_dwordx4 v[232:235], v250, s[24:27], s97 offen sc1\n"
"s_waitcnt lgkmcnt(0)\n"
"buffer_store_dwordx4 v[236:239], v250, s[24:27], s99 offen sc1\n"
"s_mov_b32 exec_lo, 0x10001\n"
"s_mov_b32 exec_hi, 0x10001\n"
"buffer_store_dwordx4 v[240:243], v251, s[28:31], s39 offen offset:0 sc1\n"
"s_mov_b64 exec, -1\n"
"s_mov_b32 exec_lo, 0x10001\n"
"s_mov_b32 exec_hi, 0x10001\n"
"buffer_store_dwordx4 v[244:247], v251, s[28:31], s39 offen offset:64 sc1\n"
"s_mov_b64 exec, -1\n"
"s_nop 1\n"
    : "+{s[36:39]}"(tg),"+{s[96:99]}"(sp),"+v"(l)
    : "{s[16:19]}"(kb),"{s[20:23]}"(srtb),"{s[24:27]}"(srd0),"{s[28:31]}"(srdm),"{s[32:35]}"(srdg),"{s[40:43]}"(srdx),"{v[130:131]}"(vp),"{a191}"(junk1),[tid]"v"(tid)
    : "v16","v17","v18","v19","v20","v21","v22","v23","v24","v25","v26","v27","v28","v29","v30","v31","v32","v33","v34","v35","v36","v37","v38","v39","v40","v41","v42","v43","v44","v45","v46","v47","v48","v49","v50","v51","v52","v53","v54","v55","v56","v57","v58","v59","v60","v61","v62","v63","v64","v65","v66","v67","v68","v69","v70","v71","v72","v73","v74","v75","v76","v77","v78","v79","v80","v81","v82","v83","v84","v85","v86","v87","v88","v89","v90","v91","v92","v93","v94","v95","v96","v97","v98","v99","v100","v101","v102","v103","v104","v105","v106","v107","v108","v109","v110","v111","v112","v113","v114","v115","v116","v117","v118","v119","v120","v121","v122","v123","v124","v125","v126","v127","v128","v129","v132","v133","v134","v135","v136","v137","v138","v139","v140","v141","v142","v143","v144","v145","v146","v147","v148","v149","v150","v151","v152","v153","v154","v155","v156","v157","v158","v159","v160","v161","v162","v163","v164","v165","v166","v167","v168","v169","v170","v171","v172","v173","v174","v175","v176","v177","v178","v179","v180","v181","v182","v183","v184","v185","v186","v187","v188","v189","v190","v191","v192","v193","v194","v195","v196","v197","v198","v199","v200","v201","v202","v203","v204","v205","v206","v207","v208","v209","v210","v211","v212","v213","v214","v215","v216","v217","v218","v219","v220","v221","v222","v223","v224","v225","v226","v227","v228","v229","v230","v231","v232","v233","v234","v235","v236","v237","v238","v239","v240","v241","v242","v243","v244","v245","v246","v247","v248","v249","v250","v251","v252","v253","v254","v255","a64","a65","a66","a67","a68","a69","a70","a71","a72","a73","a74","a75","a76","a77","a78","a79","a80","a81","a82","a83","a84","a85","a86","a87","a88","a89","a90","a91","a92","a93","a94","a95","a96","a97","a98","a99","a100","a101","a102","a103","a104","a105","a106","a107","a108","a109","a110","a111","a112","a113","a114","a115","a116","a117","a118","a119","a120","a121","a122","a123","a124","a125","a126","a127","a128","a129","a130","a131","a132","a133","a134","a135","a136","a137","a138","a139","a140","a141","a142","a143","a144","a145","a146","a147","a148","a149","a150","a151","a152","a153","a154","a155","a156","a157","a158","a159","a160","a161","a162","a163","a164","a165","a166","a167","a168","a169","a170","a171","a172","a173","a174","a175","a176","a177","a178","a179","a180","a181","a182","a183","a184","a185","a186","a187","a188","a189","a190","s94","s95","vcc","scc","memory");

    const u64 tag=((u64)tg[1]<<32)|tg[0];
    const unsigned wpo=((e*4+w)*56+min(l,55u))*128;
    u32x32 tw; u32x16 a2,vb,gb,a0; u32x8 an,a1;
    { const unsigned h=l>>5, r=l&31; const float hf=(float)h, rf=(float)r, ar=rf*(1.f/32.f), b4=4.f*hf*ar, at=rf*(1.f/1024.f), bt=4.f*hf*at;
      #pragma unroll
      for(int k=0;k<8;k++){ const float c0=(float)(16*(k>>2)+8*((k>>1)&1)+2*(k&1)); const float x0=fmaf(ar,c0,b4), x1=fmaf(ar,c0+1.f,b4);
        a0[k]=pk2(cosv(x0),cosv(x1)); const unsigned s=pk2(sinv(x0),sinv(x1)); a0[8+k]=s; an[k]=s^0x80008000u; }
      #pragma unroll
      for(int g=0;g<16;g++){ const float t=fmaf(at,(float)((g&3)+8*(g>>2)),bt); tw[g]=fu(cosv(t)); tw[16+g]=fu(sinv(t)); } }
    asm volatile("s_waitcnt vmcnt(0)\n\ts_barrier":"+{v[2:33]}"(tw),"+v"(l),"+{a[0:15]}"(a0),"+{a[16:23]}"(an)::"memory");
    if(tid<16) __hip_atomic_store((u64*)(ws+WS_FP+b*128+tid*8),tag,__ATOMIC_RELAXED,__HIP_MEMORY_SCOPE_AGENT);

    asm volatile("":"+v"(l)::"memory");
    { const unsigned h=l>>5, r=l&31, blk=(l>>4)&1, q1=(l&15)>>2, p=l&3; const float hf=(float)h;
      const unsigned j1=r>>1, cp=r&1; const float cpf=0.25f*(float)cp, s1f=(float)j1*(1.f/2048.f);
      const unsigned jA=u0? (j1==0? cp*1024 : (j1<8? 128*j1 : 128*j1-960)) : uA+128*j1;
      const float ajA=(float)jA*(1.f/2048.f), ajB=(float)(uB+128*j1)*(1.f/2048.f);
      const float oA0=(u0&&j1==0)?0.f:cpf, oA1=u0?cpf:cpf-0.25f, bB0=fmaf(4.f*hf,ajB,cpf);
      const float iA0=s1f*((float)uA+512.f*hf)-(u0?0.f:cpf), iA1=s1f*((u0?64.f:(float)uA+1024.f)+512.f*hf)-(u0?0.f:cpf), iB0=s1f*((float)uB+512.f*hf)-cpf;
      const bool zz=u0&&h==0;
      gb[0]=fu(ajA); gb[1]=fu(fmaf(4.f*hf,ajA,oA0)); gb[2]=fu(fmaf(4.f*hf,ajA,oA1)); gb[3]=u0?(j1<8?0x0000ffffu:0xffff0000u):0xffffffffu;
      gb[4]=fu(ajB); gb[5]=fu(bB0); gb[6]=fu(bB0-0.25f); gb[7]=fu(s1f); gb[8]=fu(iA0); gb[9]=fu(iA1); gb[10]=fu(iB0); gb[11]=fu(fmaf(s1f,1024.f,iB0));
      gb[12]=(u0&&cp)?0u:0xffffffffu; gb[13]=(u0&&!cp)?0u:0xffffffffu; gb[14]=fu(zz? s1f*1024.f : iA0+0.25f); gb[15]=fu(zz?0.5f:1.f);
      #pragma unroll
      for(int n=0;n<8;n++){ const unsigned base=(l&15)^(h<<2)^(2*(w&1)+blk), kap=((n&1)<<3)|((n>>1)&3);
        a1[n]=h*2048+256*blk+16*(base^kap);
        a2[n]=w*BLK+(8*h+q1)*512+(((4*(n>>1))^(4*q1))|((2*blk+(p>>1))^(2*h)^(2*(w&1))^(n&1)))*16+8*(p&1); }
      a2[10]=w*BLK+r*512+(sig(r)^(2*(w&1)))*16+8*h; a2[8]=a2[10]^((r&2)<<2); a2[9]=w*BLK+l*16;
      #pragma unroll
      for(int n=11;n<16;n++) a2[n]=0;
      vb[0]=((4*q1+2*blk+(p>>1))^h)*16+8*(p&1); vb[1]=256*h; vb[2]=((4*(r&3))^((r>>2)&3))*16+8*(h^((r>>1)&1)); vb[3]=(r>>3)*BLK+((r>>2)&1)*256;
      vb[4]=2*UNIT+w*4096+2*r+256*h; vb[5]=2*UNIT+w*4096+2048+318-2*r-256*h; vb[6]=l==0?0x3f000000u:0u;
      const unsigned y=l&7, un=(y>>2)^1, jy=4*w+(y&3); unsigned j=(un?uB:uA)+128*jy; if(u0&&!un&&jy>=8) j-=960;
      const unsigned xr=un*UNIT+2*jy*512+sig(2*jy)*16, xi=un*UNIT+(2*jy+1)*512+sig(2*jy+1)*16, ZR=32*512, DU=UNIT+32*512;
      const bool z=(sp0&&l==4)||l==8||l==9;
      vb[7]= l==8? 512+sig(1)*16 : l==9? ZR : xr+8*(jy&1); vb[8]= z? ZR : xi+8*(jy&1); vb[9]= l==8? 512+sig(1)*16 : l==9? DU : xr; vb[10]= z? DU : xi;
      j= l==8? 1024 : l==9? 0 : j; vb[11]=j*1026; vb[12]=((2048-j)&2047)*1026+2; vb[13]=wpo; vb[14]=vb[15]=0; }
    asm volatile("":"+{a[0:15]}"(a0),"+{a[16:23]}"(an),"+{a[56:63]}"(a1),"+{a[64:79]}"(a2),"+{v[34:49]}"(vb),"+{v[2:33]}"(tw),"+{v[66:81]}"(gb)::"memory");
    const u32x4 srdt=mksrd(ws+WS_T,4u<<20), srdr=mksrd(wr,2048*513*2), srdi=mksrd(wi,2048*513*2);
    u32x8 sb; sb[0]=w*BLK; sb[1]=uA*65536+512*w; sb[2]=uB*65536+512*w; sb[3]=uA*4096+512*w; sb[4]=uB*4096+512*w; sb[5]=sp0?10:8; sb[6]=sb[7]=0;

    {
      u64* fp=(u64*)(ws+WS_FP+tid*128); bool ok=(l&31)!=(e&31); unsigned spin=0;
      for(;spin<100000u;spin++){ if(!ok) ok=__hip_atomic_load(fp,__ATOMIC_RELAXED,__HIP_MEMORY_SCOPE_AGENT)==tag; if(__all(ok)) break; __builtin_amdgcn_s_sleep(4); }
      ok=false;
      for(;spin<100000u;spin++){ if(!ok) ok=__hip_atomic_load(fp,__ATOMIC_RELAXED,__HIP_MEMORY_SCOPE_AGENT)==tag; if(__all(ok)) break; __builtin_amdgcn_s_sleep(1); }
    }
    asm volatile("s_waitcnt vmcnt(0) lgkmcnt(0)\n\ts_barrier":::"memory");
    if(tid==0 && e==0) __hip_atomic_store((u64*)(ws+WS_E),tag,__ATOMIC_RELAXED,__HIP_MEMORY_SCOPE_AGENT);

    u32x2 tg2; tg2[0]=rfl((unsigned)tag); tg2[1]=rfl((unsigned)(tag>>32));
    asm volatile(
"v_accvgpr_read_b32 v58, a56\n"
"v_accvgpr_read_b32 v59, a57\n"
"v_accvgpr_read_b32 v60, a58\n"
"v_accvgpr_read_b32 v61, a59\n"
"v_accvgpr_read_b32 v62, a60\n"
"v_accvgpr_read_b32 v63, a61\n"
"v_accvgpr_read_b32 v64, a62\n"
"v_accvgpr_read_b32 v65, a63\n"
"v_accvgpr_read_b32 v48, a64\n"
"v_accvgpr_read_b32 v49, a65\n"
"v_accvgpr_read_b32 v50, a66\n"
"v_accvgpr_read_b32 v51, a67\n"
"v_accvgpr_read_b32 v52, a68\n"
"v_accvgpr_read_b32 v53, a69\n"
"v_accvgpr_read_b32 v54, a70\n"
"v_accvgpr_read_b32 v55, a71\n"
"v_accvgpr_read_b32 v56, a72\n"
"v_accvgpr_read_b32 v57, a73\n"
"s_load_dwordx2 s[84:85], s[16:17], 0x28\n"
"s_load_dwordx2 s[86:87], s[16:17], 0x40\n"
"v_lshlrev_b32 v103, 4, %[lane]\n"
"s_add_u32 s79, s56, 16384\n"
"s_add_u32 m0, s56, 0\n"
"s_add_u32 s62, s57, 0\n"
"buffer_load_dwordx4 v58, s[40:43], s62 offen sc1 lds\n"
"s_add_u32 m0, s56, 1024\n"
"s_add_u32 s63, s57, 4096\n"
"buffer_load_dwordx4 v59, s[40:43], s63 offen sc1 lds\n"
"s_add_u32 m0, s56, 2048\n"
"s_add_u32 s62, s57, 8192\n"
"v_mov_b32 v140, v67\n"
"v_mov_b32 v141, v68\n"
"v_fmamk_f32 v143, v66, 0x3f800000, v67\n"
"v_fmamk_f32 v144, v66, 0x3f800000, v68\n"
"v_fmamk_f32 v146, v66, 0x40000000, v67\n"
"v_fmamk_f32 v147, v66, 0x40000000, v68\n"
"v_fmamk_f32 v149, v66, 0x40400000, v67\n"
"v_fmamk_f32 v150, v66, 0x40400000, v68\n"
"buffer_load_dwordx4 v60, s[40:43], s62 offen sc1 lds\n"
"s_add_u32 m0, s56, 3072\n"
"s_add_u32 s63, s57, 12288\n"
"v_fmamk_f32 v152, v66, 0x41000000, v67\n"
"v_fmamk_f32 v153, v66, 0x41000000, v68\n"
"v_fmamk_f32 v155, v66, 0x41100000, v67\n"
"v_fmamk_f32 v156, v66, 0x41100000, v68\n"
"v_fmamk_f32 v158, v66, 0x41200000, v67\n"
"v_fmamk_f32 v159, v66, 0x41200000, v68\n"
"v_fmamk_f32 v161, v66, 0x41300000, v67\n"
"v_fmamk_f32 v162, v66, 0x41300000, v68\n"
"v_cos_f32 v140, v140\n"
"v_cos_f32 v141, v141\n"
"buffer_load_dwordx4 v61, s[40:43], s63 offen sc1 lds\n"
"s_add_u32 m0, s56, 4096\n"
"s_add_u32 s62, s57, 16384\n"
"v_cos_f32 v143, v143\n"
"v_cos_f32 v144, v144\n"
"v_cos_f32 v146, v146\n"
"v_cos_f32 v147, v147\n"
"v_cos_f32 v149, v149\n"
"buffer_load_dwordx4 v62, s[40:43], s62 offen sc1 lds\n"
"s_add_u32 m0, s56, 5120\n"
"s_add_u32 s63, s57, 20480\n"
"v_cos_f32 v150, v150\n"
"v_cos_f32 v152, v152\n"
"v_cos_f32 v153, v153\n"
"v_cos_f32 v155, v155\n"
"v_cos_f32 v156, v156\n"
"v_cos_f32 v158, v158\n"
"buffer_load_dwordx4 v63, s[40:43], s63 offen sc1 lds\n"
"s_add_u32 m0, s56, 6144\n"
"s_add_u32 s62, s57, 24576\n"
"v_cos_f32 v159, v159\n"
"v_cos_f32 v161, v161\n"
"v_cos_f32 v162, v162\n"
"v_cvt_pk_bf16_f32 v142, v140, v141\n"
"v_cvt_pk_bf16_f32 v145, v143, v144\n"
"v_cvt_pk_bf16_f32 v148, v146, v147\n"
"v_cvt_pk_bf16_f32 v151, v149, v150\n"
"buffer_load_dwordx4 v64, s[40:43], s62 offen sc1 lds\n"
"s_add_u32 m0, s56, 7168\n"
"s_add_u32 s63, s57, 28672\n"
"v_cvt_pk_bf16_f32 v154, v152, v153\n"
"v_cvt_pk_bf16_f32 v157, v155, v156\n"
"v_cvt_pk_bf16_f32 v160, v158, v159\n"
"v_cvt_pk_bf16_f32 v163, v161, v162\n"
"v_mov_b32 v140, v71\n"
"v_mov_b32 v141, v72\n"
"v_fmamk_f32 v143, v70, 0x3f800000, v71\n"
"v_fmamk_f32 v144, v70, 0x3f800000, v72\n"
"v_fmamk_f32 v146, v70, 0x40000000, v71\n"
"v_fmamk_f32 v147, v70, 0x40000000, v72\n"
"v_fmamk_f32 v149, v70, 0x40400000, v71\n"
"buffer_load_dwordx4 v65, s[40:43], s63 offen sc1 lds\n"
"s_add_u32 m0, s56, 8192\n"
"s_add_u32 s62, s57, 32768\n"
"v_fmamk_f32 v150, v70, 0x40400000, v72\n"
"v_fmamk_f32 v152, v70, 0x41000000, v71\n"
"v_fmamk_f32 v153, v70, 0x41000000, v72\n"
"v_fmamk_f32 v155, v70, 0x41100000, v71\n"
"v_fmamk_f32 v156, v70, 0x41100000, v72\n"
"v_fmamk_f32 v158, v70, 0x41200000, v71\n"
"v_fmamk_f32 v159, v70, 0x41200000, v72\n"
"v_fmamk_f32 v161, v70, 0x41300000, v71\n"
"v_fmamk_f32 v162, v70, 0x41300000, v72\n"
"v_cos_f32 v140, v140\n"
"v_cos_f32 v141, v141\n"
"v_cos_f32 v143, v143\n"
"v_cos_f32 v144, v144\n"
"v_cos_f32 v146, v146\n"
"v_cos_f32 v147, v147\n"
"v_cos_f32 v149, v149\n"
"v_cos_f32 v150, v150\n"
"v_cos_f32 v152, v152\n"
"v_cos_f32 v153, v153\n"
"v_cos_f32 v155, v155\n"
"v_cos_f32 v156, v156\n"
"v_cos_f32 v158, v158\n"
"v_cos_f32 v159, v159\n"
"v_cos_f32 v161, v161\n"
"v_cos_f32 v162, v162\n"
"v_and_b32 v142, v69, v142\n"
"v_and_b32 v145, v69, v145\n"
"v_and_b32 v148, v69, v148\n"
"v_and_b32 v151, v69, v151\n"
"v_and_b32 v154, v69, v154\n"
"v_and_b32 v157, v69, v157\n"
"v_and_b32 v160, v69, v160\n"
"v_and_b32 v163, v69, v163\n"
"v_accvgpr_write_b32 a24, v142\n"
"v_accvgpr_write_b32 a25, v145\n"
"v_accvgpr_write_b32 a26, v148\n"
"v_accvgpr_write_b32 a27, v151\n"
"v_accvgpr_write_b32 a28, v154\n"
"v_accvgpr_write_b32 a29, v157\n"
"v_accvgpr_write_b32 a30, v160\n"
"v_accvgpr_write_b32 a31, v163\n"
"v_cvt_pk_bf16_f32 v142, v140, v141\n"
"v_cvt_pk_bf16_f32 v145, v143, v144\n"
"v_cvt_pk_bf16_f32 v148, v146, v147\n"
"v_cvt_pk_bf16_f32 v151, v149, v150\n"
"v_cvt_pk_bf16_f32 v154, v152, v153\n"
"v_cvt_pk_bf16_f32 v157, v155, v156\n"
"v_cvt_pk_bf16_f32 v160, v158, v159\n"
"v_cvt_pk_bf16_f32 v163, v161, v162\n"
"v_mov_b32 v140, v74\n"
"v_mov_b32 v141, v80\n"
"v_fmamk_f32 v143, v73, 0x43000000, v74\n"
"v_fmamk_f32 v146, v73, 0x43800000, v74\n"
"v_fmamk_f32 v149, v73, 0x43c00000, v74\n"
"v_mov_b32 v152, v75\n"
"buffer_load_dwordx4 v58, s[40:43], s62 offen sc1 lds\n"
"s_add_u32 m0, s56, 9216\n"
"s_add_u32 s63, s57, 36864\n"
"v_fmamk_f32 v155, v73, 0x43000000, v75\n"
"v_fmamk_f32 v158, v73, 0x43800000, v75\n"
"v_fmamk_f32 v161, v73, 0x43c00000, v75\n"
"v_cos_f32 v140, v140\n"
"v_cos_f32 v141, v141\n"
"v_add_f32 v144, 0x3e800000, v143\n"
"v_add_f32 v147, 0x3e800000, v146\n"
"v_add_f32 v150, 0x3e800000, v149\n"
"v_add_f32 v153, 0x3e800000, v152\n"
"v_add_f32 v156, 0x3e800000, v155\n"
"v_add_f32 v159, 0x3e800000, v158\n"
"v_add_f32 v162, 0x3e800000, v161\n"
"v_cos_f32 v143, v143\n"
"buffer_load_dwordx4 v59, s[40:43], s63 offen sc1 lds\n"
"s_add_u32 m0, s56, 10240\n"
"s_add_u32 s62, s57, 40960\n"
"v_cos_f32 v144, v144\n"
"v_cos_f32 v146, v146\n"
"v_cos_f32 v147, v147\n"
"v_cos_f32 v149, v149\n"
"v_cos_f32 v150, v150\n"
"v_cos_f32 v152, v152\n"
"v_cos_f32 v153, v153\n"
"buffer_load_dwordx4 v60, s[40:43], s62 offen sc1 lds\n"
"s_add_u32 m0, s56, 11264\n"
"s_add_u32 s63, s57, 45056\n"
"v_cos_f32 v155, v155\n"
"v_cos_f32 v156, v156\n"
"v_cos_f32 v158, v158\n"
"v_cos_f32 v159, v159\n"
"v_cos_f32 v161, v161\n"
"v_cos_f32 v162, v162\n"
"v_accvgpr_write_b32 a32, v142\n"
"v_accvgpr_write_b32 a33, v145\n"
"v_accvgpr_write_b32 a34, v148\n"
"buffer_load_dwordx4 v61, s[40:43], s63 offen sc1 lds\n"
"s_add_u32 m0, s56, 12288\n"
"s_add_u32 s62, s57, 49152\n"
"v_accvgpr_write_b32 a35, v151\n"
"v_accvgpr_write_b32 a36, v154\n"
"v_accvgpr_write_b32 a37, v157\n"
"v_accvgpr_write_b32 a38, v160\n"
"v_accvgpr_write_b32 a39, v163\n"
"v_mul_f32 v140, v81, v140\n"
"v_mul_f32 v141, v81, v141\n"
"v_cvt_pk_bf16_f32 v142, v140, v141\n"
"v_cvt_pk_bf16_f32 v145, v143, v144\n"
"v_cvt_pk_bf16_f32 v148, v146, v147\n"
"v_cvt_pk_bf16_f32 v151, v149, v150\n"
"v_cvt_pk_bf16_f32 v154, v152, v153\n"
"v_cvt_pk_bf16_f32 v157, v155, v156\n"
"v_cvt_pk_bf16_f32 v160, v158, v159\n"
"v_cvt_pk_bf16_f32 v163, v161, v162\n"
"buffer_load_dwordx4 v62, s[40:43], s62 offen sc1 lds\n"
"s_add_u32 m0, s56, 13312\n"
"s_add_u32 s63, s57, 53248\n"
"v_mov_b32 v140, v76\n"
"v_fmamk_f32 v143, v73, 0x43000000, v76\n"
"v_fmamk_f32 v146, v73, 0x43800000, v76\n"
"v_fmamk_f32 v149, v73, 0x43c00000, v76\n"
"v_mov_b32 v152, v77\n"
"v_fmamk_f32 v155, v73, 0x43000000, v77\n"
"v_fmamk_f32 v158, v73, 0x43800000, v77\n"
"v_fmamk_f32 v161, v73, 0x43c00000, v77\n"
"v_add_f32 v141, 0x3e800000, v140\n"
"v_add_f32 v144, 0x3e800000, v143\n"
"v_add_f32 v147, 0x3e800000, v146\n"
"v_add_f32 v150, 0x3e800000, v149\n"
"v_add_f32 v153, 0x3e800000, v152\n"
"v_add_f32 v156, 0x3e800000, v155\n"
"v_add_f32 v159, 0x3e800000, v158\n"
"buffer_load_dwordx4 v63, s[40:43], s63 offen sc1 lds\n"
"s_add_u32 m0, s56, 14336\n"
"s_add_u32 s62, s57, 57344\n"
"v_add_f32 v162, 0x3e800000, v161\n"
"v_lshrrev_b32 v84, 1, v57\n"
"v_cos_f32 v140, v140\n"
"v_cos_f32 v141, v141\n"
"v_cos_f32 v143, v143\n"
"v_cos_f32 v144, v144\n"
"v_cos_f32 v146, v146\n"
"v_cos_f32 v147, v147\n"
"v_cos_f32 v149, v149\n"
"buffer_load_dwordx4 v64, s[40:43], s62 offen sc1 lds\n"
"s_add_u32 m0, s56, 15360\n"
"s_add_u32 s63, s57, 61440\n"
"v_cos_f32 v150, v150\n"
"v_cos_f32 v152, v152\n"
"v_cos_f32 v153, v153\n"
"v_cos_f32 v155, v155\n"
"v_cos_f32 v156, v156\n"
"v_cos_f32 v158, v158\n"
"v_cos_f32 v159, v159\n"
"buffer_load_dwordx4 v65, s[40:43], s63 offen sc1 lds\n"
"s_add_u32 m0, s56, 67584\n"
"s_add_u32 s62, s58, 0\n"
"v_cos_f32 v161, v161\n"
"v_cos_f32 v162, v162\n"
"v_and_b32 v84, 0x1f8, v84\n"
"v_and_b32 v142, v78, v142\n"
"v_and_b32 v145, v78, v145\n"
"v_and_b32 v148, v78, v148\n"
"v_and_b32 v151, v78, v151\n"
"v_and_b32 v154, v79, v154\n"
"v_and_b32 v157, v79, v157\n"
"v_and_b32 v160, v79, v160\n"
"v_and_b32 v163, v79, v163\n"
"v_mov_b32 v82, 0\n"
"v_mov_b32 v83, 0\n"
"buffer_load_dwordx4 v58, s[40:43], s62 offen sc1 lds\n"
"s_add_u32 m0, s56, 68608\n"
"s_add_u32 s63, s58, 4096\n"
"v_add_u32 v84, s79, v84\n"
"v_accvgpr_write_b32 a40, v142\n"
"v_accvgpr_write_b32 a41, v145\n"
"v_accvgpr_write_b32 a42, v148\n"
"v_accvgpr_write_b32 a43, v151\n"
"v_accvgpr_write_b32 a44, v154\n"
"v_accvgpr_write_b32 a45, v157\n"
"v_accvgpr_write_b32 a46, v160\n"
"v_accvgpr_write_b32 a47, v163\n"
"v_cvt_pk_bf16_f32 v142, v140, v141\n"
"v_cvt_pk_bf16_f32 v145, v143, v144\n"
"v_cvt_pk_bf16_f32 v148, v146, v147\n"
"v_cvt_pk_bf16_f32 v151, v149, v150\n"
"v_cvt_pk_bf16_f32 v154, v152, v153\n"
"v_cvt_pk_bf16_f32 v157, v155, v156\n"
"buffer_load_dwordx4 v59, s[40:43], s63 offen sc1 lds\n"
"s_add_u32 m0, s56, 69632\n"
"s_add_u32 s62, s58, 8192\n"
"ds_write_b64 v84, v[82:83]\n"
"v_cvt_pk_bf16_f32 v160, v158, v159\n"
"v_cvt_pk_bf16_f32 v163, v161, v162\n"
"v_accvgpr_write_b32 a48, v142\n"
"v_accvgpr_write_b32 a49, v145\n"
"v_accvgpr_write_b32 a50, v148\n"
"v_accvgpr_write_b32 a51, v151\n"
"v_accvgpr_write_b32 a52, v154\n"
"v_accvgpr_write_b32 a53, v157\n"
"v_accvgpr_write_b32 a54, v160\n"
"v_accvgpr_write_b32 a55, v163\n"
"v_mov_b32 v104, 0\n"
"v_mov_b32 v105, 0\n"
"buffer_load_dwordx4 v60, s[40:43], s62 offen sc1 lds\n"
"s_add_u32 m0, s56, 70656\n"
"s_add_u32 s63, s58, 12288\n"
"v_mov_b32 v106, 0\n"
"v_mov_b32 v107, 0\n"
"v_mov_b32 v108, 0\n"
"v_mov_b32 v109, 0\n"
"v_mov_b32 v110, 0\n"
"v_mov_b32 v111, 0\n"
"v_mov_b32 v112, 0\n"
"v_mov_b32 v113, 0\n"
"v_mov_b32 v114, 0\n"
"v_mov_b32 v115, 0\n"
"v_mov_b32 v116, 0\n"
"v_mov_b32 v117, 0\n"
"v_mov_b32 v118, 0\n"
"v_mov_b32 v119, 0\n"
"v_mov_b32 v120, 0\n"
"buffer_load_dwordx4 v61, s[40:43], s63 offen sc1 lds\n"
"s_add_u32 m0, s56, 71680\n"
"s_add_u32 s62, s58, 16384\n"
"v_mov_b32 v121, 0\n"
"v_mov_b32 v122, 0\n"
"v_mov_b32 v123, 0\n"
"v_mov_b32 v124, 0\n"
"v_mov_b32 v125, 0\n"
"v_mov_b32 v126, 0\n"
"v_mov_b32 v127, 0\n"
"v_mov_b32 v128, 0\n"
"v_mov_b32 v129, 0\n"
"v_mov_b32 v130, 0\n"
"v_mov_b32 v131, 0\n"
"v_mov_b32 v132, 0\n"
"v_mov_b32 v133, 0\n"
"v_mov_b32 v134, 0\n"
"v_mov_b32 v135, 0\n"
"buffer_load_dwordx4 v62, s[40:43], s62 offen sc1 lds\n"
"s_add_u32 m0, s56, 72704\n"
"s_add_u32 s63, s58, 20480\n"
"v_mov_b32 v136, 0\n"
"v_mov_b32 v137, 0\n"
"v_mov_b32 v138, 0\n"
"v_mov_b32 v139, 0\n"
"v_mov_b32 v208, 0\n"
"v_mov_b32 v209, 0\n"
"v_mov_b32 v210, 0\n"
"v_mov_b32 v211, 0\n"
"v_mov_b32 v212, 0\n"
"v_mov_b32 v213, 0\n"
"v_mov_b32 v214, 0\n"
"v_mov_b32 v215, 0\n"
"v_mov_b32 v216, 0\n"
"v_mov_b32 v217, 0\n"
"v_mov_b32 v218, 0\n"
"buffer_load_dwordx4 v63, s[40:43], s63 offen sc1 lds\n"
"s_add_u32 m0, s56, 73728\n"
"s_add_u32 s62, s58, 24576\n"
"v_mov_b32 v219, 0\n"
"v_mov_b32 v220, 0\n"
"v_mov_b32 v221, 0\n"
"v_mov_b32 v222, 0\n"
"v_mov_b32 v223, 0\n"
"v_mov_b32 v224, 0\n"
"v_mov_b32 v225, 0\n"
"v_mov_b32 v226, 0\n"
"v_mov_b32 v227, 0\n"
"v_mov_b32 v228, 0\n"
"v_mov_b32 v229, 0\n"
"v_mov_b32 v230, 0\n"
"v_mov_b32 v231, 0\n"
"v_mov_b32 v232, 0\n"
"v_mov_b32 v233, 0\n"
"buffer_load_dwordx4 v64, s[40:43], s62 offen sc1 lds\n"
"s_add_u32 m0, s56, 74752\n"
"s_add_u32 s63, s58, 28672\n"
"v_mov_b32 v234, 0\n"
"v_mov_b32 v235, 0\n"
"v_mov_b32 v236, 0\n"
"v_mov_b32 v237, 0\n"
"v_mov_b32 v238, 0\n"
"v_mov_b32 v239, 0\n"
"v_mov_b32 v240, 0\n"
"v_mov_b32 v241, 0\n"
"v_mov_b32 v242, 0\n"
"v_mov_b32 v243, 0\n"
"v_readlane_b32 s72, v45, 0\n"
"v_readlane_b32 s73, v46, 0\n"
"v_readlane_b32 s74, v45, 1\n"
"v_readlane_b32 s75, v46, 1\n"
"v_add_u32 v90, 67584, v48\n"
"buffer_load_dwordx4 v65, s[40:43], s63 offen sc1 lds\n"
"v_add_u32 v91, 67584, v49\n"
"v_add_u32 v92, 67584, v50\n"
"v_add_u32 v93, 67584, v51\n"
"v_add_u32 v94, 67584, v52\n"
"v_add_u32 v95, 67584, v53\n"
"v_add_u32 v96, 67584, v54\n"
"v_add_u32 v97, 67584, v55\n"
"v_add_u32 v98, 67584, v56\n"
"v_add_u32 v99, 67584, v57\n"
"s_nop 1\n"
"s_waitcnt vmcnt(8) lgkmcnt(0)\n"
"s_add_u32 m0, s56, 75776\n"
"s_add_u32 s62, s58, 32768\n"
"buffer_load_dwordx4 v58, s[40:43], s62 offen sc1 lds\n"
"s_add_u32 m0, s56, 76800\n"
"s_add_u32 s63, s58, 36864\n"
"buffer_load_dwordx4 v59, s[40:43], s63 offen sc1 lds\n"
"s_add_u32 m0, s56, 77824\n"
"s_add_u32 s62, s58, 40960\n"
"ds_read_b64_tr_b16 a[80:81], v48 offset:0\n"
"ds_read_b64_tr_b16 a[82:83], v49 offset:2048\n"
"ds_read_b64_tr_b16 a[84:85], v48 offset:8192\n"
"ds_read_b64_tr_b16 a[86:87], v49 offset:10240\n"
"ds_read_b64_tr_b16 a[88:89], v50 offset:0\n"
"ds_read_b64_tr_b16 a[90:91], v51 offset:2048\n"
"ds_read_b64_tr_b16 a[92:93], v50 offset:8192\n"
"ds_read_b64_tr_b16 a[94:95], v51 offset:10240\n"
"buffer_load_dwordx4 v60, s[40:43], s62 offen sc1 lds\n"
"s_add_u32 m0, s56, 78848\n"
"s_add_u32 s63, s58, 45056\n"
"ds_read_b64_tr_b16 a[96:97], v52 offset:0\n"
"ds_read_b64_tr_b16 a[98:99], v53 offset:2048\n"
"ds_read_b64_tr_b16 a[100:101], v52 offset:8192\n"
"ds_read_b64_tr_b16 a[102:103], v53 offset:10240\n"
"ds_read_b64_tr_b16 a[104:105], v54 offset:0\n"
"ds_read_b64_tr_b16 a[106:107], v55 offset:2048\n"
"ds_read_b64_tr_b16 a[108:109], v54 offset:8192\n"
"ds_read_b64_tr_b16 a[110:111], v55 offset:10240\n"
"v_xor_b32 v244, 0, v56\n"
"v_xor_b32 v245, 16, v56\n"
"v_xor_b32 v246, 32, v56\n"
"buffer_load_dwordx4 v61, s[40:43], s63 offen sc1 lds\n"
"s_add_u32 m0, s56, 79872\n"
"s_add_u32 s62, s58, 49152\n"
"v_xor_b32 v247, 48, v56\n"
"v_xor_b32 v248, 64, v56\n"
"v_xor_b32 v249, 80, v56\n"
"v_xor_b32 v250, 96, v56\n"
"v_xor_b32 v251, 112, v56\n"
"s_waitcnt lgkmcnt(14)\n"
"v_mfma_f32_32x32x16_bf16 v[140:155], a[80:83], a[24:27], 0\n"
"ds_read_b64_tr_b16 a[80:81], v49 offset:256\n"
"ds_read_b64_tr_b16 a[82:83], v48 offset:2304\n"
"buffer_load_dwordx4 v62, s[40:43], s62 offen sc1 lds\n"
"s_add_u32 m0, s56, 80896\n"
"s_add_u32 s63, s58, 53248\n"
"s_waitcnt lgkmcnt(14)\n"
"v_mfma_f32_32x32x16_bf16 v[140:155], a[84:87], a[28:31], v[140:155]\n"
"ds_read_b64_tr_b16 a[84:85], v49 offset:8448\n"
"ds_read_b64_tr_b16 a[86:87], v48 offset:10496\n"
"s_waitcnt lgkmcnt(14)\n"
"v_mfma_f32_32x32x16_bf16 v[156:171], a[88:91], a[24:27], 0\n"
"ds_read_b64_tr_b16 a[88:89], v51 offset:256\n"
"buffer_load_dwordx4 v63, s[40:43], s63 offen sc1 lds\n"
"s_add_u32 m0, s56, 81920\n"
"s_add_u32 s62, s58, 57344\n"
"s_waitcnt lgkmcnt(13)\n"
"v_mfma_f32_32x32x16_bf16 v[156:171], a[92:95], a[28:31], v[156:171]\n"
"ds_read_b64_tr_b16 a[90:91], v50 offset:2304\n"
"ds_read_b64_tr_b16 a[92:93], v51 offset:8448\n"
"ds_read_b64_tr_b16 a[94:95], v50 offset:10496\n"
"v_cvt_pk_bf16_f32 v66, v140, v141\n"
"v_cvt_pk_bf16_f32 v67, v142, v143\n"
"v_cvt_pk_bf16_f32 v68, v144, v145\n"
"s_waitcnt lgkmcnt(14)\n"
"v_mfma_f32_32x32x16_bf16 v[172:187], a[96:99], a[24:27], 0\n"
"ds_write_b64 v244, v[66:67] offset:0\n"
"buffer_load_dwordx4 v64, s[40:43], s62 offen sc1 lds\n"
"s_add_u32 m0, s56, 82944\n"
"s_add_u32 s63, s58, 61440\n"
"s_waitcnt lgkmcnt(13)\n"
"v_mfma_f32_32x32x16_bf16 v[172:187], a[100:103], a[28:31], v[172:187]\n"
"ds_read_b64_tr_b16 a[96:97], v53 offset:256\n"
"ds_read_b64_tr_b16 a[98:99], v52 offset:2304\n"
"ds_read_b64_tr_b16 a[100:101], v53 offset:8448\n"
"ds_read_b64_tr_b16 a[102:103], v52 offset:10496\n"
"v_cvt_pk_bf16_f32 v69, v146, v147\n"
"v_cvt_pk_bf16_f32 v70, v148, v149\n"
"s_waitcnt lgkmcnt(15)\n"
"v_mfma_f32_32x32x16_bf16 v[188:203], a[104:107], a[24:27], 0\n"
"ds_write_b64 v245, v[68:69] offset:0\n"
"buffer_load_dwordx4 v65, s[40:43], s63 offen sc1 lds\n"
"s_add_u32 m0, s78, 0\n"
"ds_read_b64_tr_b16 a[104:105], v55 offset:256\n"
"s_waitcnt lgkmcnt(15)\n"
"v_mfma_f32_32x32x16_bf16 v[188:203], a[108:111], a[28:31], v[188:203]\n"
"ds_read_b64_tr_b16 a[106:107], v54 offset:2304\n"
"ds_read_b64_tr_b16 a[108:109], v55 offset:8448\n"
"ds_read_b64_tr_b16 a[110:111], v54 offset:10496\n"
"v_cvt_pk_bf16_f32 v71, v150, v151\n"
"v_cvt_pk_bf16_f32 v72, v152, v153\n"
"v_cvt_pk_bf16_f32 v73, v154, v155\n"
"s_waitcnt lgkmcnt(15)\n"
"v_mfma_f32_32x32x16_bf16 v[140:155], a[80:83], a[24:27], 0\n"
"ds_write_b64 v246, v[70:71] offset:0\n"
"buffer_load_dwordx4 v103, s[48:51], s72 offen lds\n"
"s_add_u32 m0, s78, 1024\n"
"ds_write_b64 v247, v[72:73] offset:0\n"
"s_waitcnt lgkmcnt(15)\n"
"v_mfma_f32_32x32x16_bf16 v[140:155], a[84:87], a[28:31], v[140:155]\n"
"v_cvt_pk_bf16_f32 v74, v156, v157\n"
"v_cvt_pk_bf16_f32 v75, v158, v159\n"
"v_cvt_pk_bf16_f32 v76, v160, v161\n"
"v_cvt_pk_bf16_f32 v77, v162, v163\n"
"v_cvt_pk_bf16_f32 v78, v164, v165\n"
"v_cvt_pk_bf16_f32 v79, v166, v167\n"
"v_cvt_pk_bf16_f32 v80, v168, v169\n"
"buffer_load_dwordx4 v103, s[52:55], s72 offen lds\n"
"s_add_u32 m0, s78, 2048\n"
"ds_write_b64 v248, v[74:75] offset:0\n"
"ds_write_b64 v249, v[76:77] offset:0\n"
"ds_write_b64 v250, v[78:79] offset:0\n"
"v_cvt_pk_bf16_f32 v81, v170, v171\n"
"s_waitcnt lgkmcnt(15)\n"
"v_mfma_f32_32x32x16_bf16 v[156:171], a[88:91], a[24:27], 0\n"
"buffer_load_dwordx4 v103, s[48:51], s73 offen lds\n"
"s_add_u32 m0, s78, 3072\n"
"ds_write_b64 v251, v[80:81] offset:0\n"
"v_mfma_f32_32x32x16_bf16 v[156:171], a[92:95], a[28:31], v[156:171]\n"
"v_cvt_pk_bf16_f32 v82, v172, v173\n"
"v_cvt_pk_bf16_f32 v83, v174, v175\n"
"v_xor_b32 v244, 128, v56\n"
"v_cvt_pk_bf16_f32 v84, v176, v177\n"
"v_cvt_pk_bf16_f32 v85, v178, v179\n"
"v_xor_b32 v245, 144, v56\n"
"v_cvt_pk_bf16_f32 v86, v180, v181\n"
"buffer_load_dwordx4 v103, s[52:55], s73 offen lds\n"
"ds_write_b64 v244, v[82:83] offset:0\n"
"ds_write_b64 v245, v[84:85] offset:0\n"
"v_cvt_pk_bf16_f32 v87, v182, v183\n"
"v_xor_b32 v246, 160, v56\n"
"v_cvt_pk_bf16_f32 v88, v184, v185\n"
"v_cvt_pk_bf16_f32 v89, v186, v187\n"
"s_waitcnt lgkmcnt(15)\n"
"v_mfma_f32_32x32x16_bf16 v[172:187], a[96:99], a[24:27], 0\n"
"ds_write_b64 v246, v[86:87] offset:0\n"
"buffer_load_short_d16_hi v138, off, s[48:51], s72 offset:1024\n"
"v_xor_b32 v247, 176, v56\n"
"v_cvt_pk_bf16_f32 v66, v188, v189\n"
"s_waitcnt lgkmcnt(14)\n"
"v_mfma_f32_32x32x16_bf16 v[172:187], a[100:103], a[28:31], v[172:187]\n"
"ds_write_b64 v247, v[88:89] offset:0\n"
"buffer_load_short_d16_hi v139, off, s[52:55], s72 offset:1024\n"
"v_cvt_pk_bf16_f32 v67, v190, v191\n"
"v_xor_b32 v248, 192, v56\n"
"v_cvt_pk_bf16_f32 v68, v192, v193\n"
"v_cvt_pk_bf16_f32 v69, v194, v195\n"
"v_xor_b32 v249, 208, v56\n"
"v_cvt_pk_bf16_f32 v70, v196, v197\n"
"v_cvt_pk_bf16_f32 v71, v198, v199\n"
"v_xor_b32 v250, 224, v56\n"
"v_cvt_pk_bf16_f32 v72, v200, v201\n"
"v_cvt_pk_bf16_f32 v73, v202, v203\n"
"s_waitcnt lgkmcnt(12)\n"
"v_mfma_f32_32x32x16_bf16 v[188:203], a[104:107], a[24:27], 0\n"
"ds_write_b64 v248, v[66:67] offset:0\n"
"ds_write_b64 v249, v[68:69] offset:0\n"
"s_waitcnt lgkmcnt(12)\n"
"v_mfma_f32_32x32x16_bf16 v[188:203], a[108:111], a[28:31], v[188:203]\n"
"ds_write_b64 v250, v[70:71] offset:0\n"
"buffer_load_short_d16_hi v242, off, s[48:51], s74 offset:1024\n"
"buffer_load_short_d16_hi v243, off, s[52:55], s74 offset:1024\n"
"v_xor_b32 v251, 240, v56\n"
"v_cvt_pk_bf16_f32 v74, v140, v141\n"
"v_cvt_pk_bf16_f32 v75, v142, v143\n"
"v_xor_b32 v244, 16, v56\n"
"v_cvt_pk_bf16_f32 v76, v144, v145\n"
"v_cvt_pk_bf16_f32 v77, v146, v147\n"
"v_xor_b32 v245, 0, v56\n"
"v_cvt_pk_bf16_f32 v78, v148, v149\n"
"v_cvt_pk_bf16_f32 v79, v150, v151\n"
"v_xor_b32 v246, 48, v56\n"
"v_cvt_pk_bf16_f32 v80, v152, v153\n"
"v_cvt_pk_bf16_f32 v81, v154, v155\n"
"v_xor_b32 v247, 32, v56\n"
"v_cvt_pk_bf16_f32 v82, v156, v157\n"
"v_cvt_pk_bf16_f32 v83, v158, v159\n"
"v_xor_b32 v248, 80, v56\n"
"v_cvt_pk_bf16_f32 v84, v160, v161\n"
"v_cvt_pk_bf16_f32 v85, v162, v163\n"
"v_xor_b32 v249, 64, v56\n"
"v_cvt_pk_bf16_f32 v86, v164, v165\n"
"v_cvt_pk_bf16_f32 v87, v166, v167\n"
"v_xor_b32 v250, 112, v56\n"
"v_cvt_pk_bf16_f32 v88, v168, v169\n"
"v_cvt_pk_bf16_f32 v89, v170, v171\n"
"v_cvt_pk_bf16_f32 v66, v172, v173\n"
"v_cvt_pk_bf16_f32 v67, v174, v175\n"
"v_cvt_pk_bf16_f32 v68, v176, v177\n"
"v_cvt_pk_bf16_f32 v69, v178, v179\n"
"v_cvt_pk_bf16_f32 v70, v180, v181\n"
"v_cvt_pk_bf16_f32 v71, v182, v183\n"
"s_sub_u32 s79, s73, 2\n"
"s_add_u32 m0, s78, 0\n"
"ds_write_b64 v251, v[72:73] offset:0\n"
"ds_write_b64 v244, v[74:75] offset:256\n"
"ds_write_b64 v245, v[76:77] offset:256\n"
"ds_write_b64 v246, v[78:79] offset:256\n"
"ds_write_b64 v247, v[80:81] offset:256\n"
"buffer_load_short_d16_hi v136, off, s[48:51], s79\n"
"buffer_load_short_d16_hi v137, off, s[52:55], s79\n"
"v_xor_b32 v251, 96, v56\n"
"v_xor_b32 v244, 144, v56\n"
"v_xor_b32 v245, 128, v56\n"
"v_xor_b32 v246, 176, v56\n"
"v_cvt_pk_bf16_f32 v72, v184, v185\n"
"v_cvt_pk_bf16_f32 v73, v186, v187\n"
"v_xor_b32 v247, 160, v56\n"
"v_cvt_pk_bf16_f32 v74, v188, v189\n"
"v_cvt_pk_bf16_f32 v75, v190, v191\n"
"v_cvt_pk_bf16_f32 v76, v192, v193\n"
"v_cvt_pk_bf16_f32 v77, v194, v195\n"
"v_cvt_pk_bf16_f32 v78, v196, v197\n"
"v_cvt_pk_bf16_f32 v79, v198, v199\n"
"v_cvt_pk_bf16_f32 v80, v200, v201\n"
"v_cvt_pk_bf16_f32 v81, v202, v203\n"
"ds_write_b64 v248, v[82:83] offset:256\n"
"ds_write_b64 v249, v[84:85] offset:256\n"
"ds_write_b64 v250, v[86:87] offset:256\n"
"ds_write_b64 v251, v[88:89] offset:256\n"
"ds_write_b64 v244, v[66:67] offset:256\n"
"v_xor_b32 v248, 208, v56\n"
"v_xor_b32 v244, 0, v98\n"
"v_xor_b32 v249, 192, v56\n"
"v_xor_b32 v250, 240, v56\n"
"v_xor_b32 v251, 224, v56\n"
"ds_write_b64 v245, v[68:69] offset:256\n"
"ds_write_b64 v246, v[70:71] offset:256\n"
"v_xor_b32 v245, 16, v98\n"
"v_xor_b32 v246, 32, v98\n"
"ds_write_b64 v247, v[72:73] offset:256\n"
"v_xor_b32 v247, 48, v98\n"
"s_waitcnt vmcnt(10)\n"
"ds_read_b64_tr_b16 a[80:81], v90 offset:0\n"
"ds_read_b64_tr_b16 a[82:83], v91 offset:2048\n"
"ds_read_b64_tr_b16 a[84:85], v90 offset:8192\n"
"ds_read_b64_tr_b16 a[86:87], v91 offset:10240\n"
"ds_write_b64 v248, v[74:75] offset:256\n"
"v_xor_b32 v248, 64, v98\n"
"ds_write_b64 v249, v[76:77] offset:256\n"
"v_xor_b32 v249, 80, v98\n"
"ds_write_b64 v250, v[78:79] offset:256\n"
"v_xor_b32 v250, 96, v98\n"
"ds_write_b64 v251, v[80:81] offset:256\n"
"v_xor_b32 v251, 112, v98\n"
"s_waitcnt vmcnt(9)\n"
"ds_read_u16_d16_hi v104, v38 offset:0\n"
"s_waitcnt vmcnt(8)\n"
"ds_read_u16_d16_hi v120, v38 offset:1024\n"
"ds_read_u16_d16_hi v105, v38 offset:64\n"
"s_waitcnt lgkmcnt(9)\n"
"v_mfma_f32_32x32x16_bf16 v[140:155], a[80:83], a[32:35], 0\n"
"ds_read_b64_tr_b16 a[88:89], v92 offset:0\n"
"ds_read_u16_d16_hi v121, v38 offset:1088\n"
"ds_read_b64_tr_b16 a[90:91], v93 offset:2048\n"
"ds_read_u16_d16_hi v106, v38 offset:128\n"
"s_waitcnt lgkmcnt(11)\n"
"v_mfma_f32_32x32x16_bf16 v[140:155], a[84:87], a[36:39], v[140:155]\n"
"ds_read_b64_tr_b16 a[92:93], v92 offset:8192\n"
"ds_read_u16_d16_hi v122, v38 offset:1152\n"
"ds_read_b64_tr_b16 a[94:95], v93 offset:10240\n"
"ds_read_u16_d16_hi v107, v38 offset:192\n"
"ds_read_u16_d16_hi v123, v38 offset:1216\n"
"ds_read_u16_d16_hi v108, v38 offset:512\n"
"ds_read_u16_d16_hi v124, v38 offset:1536\n"
"s_nop 4\n"
"v_cvt_pk_bf16_f32 v66, v140, v141\n"
"v_cvt_pk_bf16_f32 v67, v142, v143\n"
"v_cvt_pk_bf16_f32 v68, v144, v145\n"
"v_cvt_pk_bf16_f32 v69, v146, v147\n"
"v_cvt_pk_bf16_f32 v70, v148, v149\n"
"v_cvt_pk_bf16_f32 v71, v150, v151\n"
"v_cvt_pk_bf16_f32 v72, v152, v153\n"
"v_cvt_pk_bf16_f32 v73, v154, v155\n"
"ds_write_b64 v244, v[66:67] offset:0\n"
"ds_write_b64 v245, v[68:69] offset:0\n"
"ds_write_b64 v246, v[70:71] offset:0\n"
"s_waitcnt lgkmcnt(11)\n"
"v_mfma_f32_32x32x16_bf16 v[156:171], a[88:91], a[32:35], 0\n"
"ds_write_b64 v247, v[72:73] offset:0\n"
"ds_read_u16_d16_hi v109, v38 offset:576\n"
"v_xor_b32 v244, 128, v98\n"
"v_xor_b32 v245, 144, v98\n"
"s_waitcnt lgkmcnt(9)\n"
"v_mfma_f32_32x32x16_bf16 v[156:171], a[92:95], a[36:39], v[156:171]\n"
"ds_read_u16_d16_hi v125, v38 offset:1600\n"
"ds_read_u16_d16_hi v110, v38 offset:640\n"
"ds_read_u16_d16_hi v126, v38 offset:1664\n"
"ds_read_u16_d16_hi v111, v38 offset:704\n"
"v_xor_b32 v246, 160, v98\n"
"v_xor_b32 v247, 176, v98\n"
"ds_read_u16_d16_hi v127, v38 offset:1728\n"
"s_waitcnt vmcnt(7)\n"
"ds_read_u16_d16_hi v112, v39 offset:704\n"
"s_waitcnt vmcnt(6)\n"
"ds_read_u16_d16_hi v128, v39 offset:1728\n"
"ds_read_u16_d16_hi v113, v39 offset:640\n"
"v_cvt_pk_bf16_f32 v74, v156, v157\n"
"v_cvt_pk_bf16_f32 v75, v158, v159\n"
"v_cvt_pk_bf16_f32 v76, v160, v161\n"
"v_cvt_pk_bf16_f32 v77, v162, v163\n"
"v_cvt_pk_bf16_f32 v78, v164, v165\n"
"v_cvt_pk_bf16_f32 v79, v166, v167\n"
"v_cvt_pk_bf16_f32 v80, v168, v169\n"
"v_cvt_pk_bf16_f32 v81, v170, v171\n"
"ds_write_b64 v248, v[74:75] offset:0\n"
"ds_write_b64 v249, v[76:77] offset:0\n"
"ds_read_u16_d16_hi v129, v39 offset:1664\n"
"ds_write_b64 v250, v[78:79] offset:0\n"
"v_xor_b32 v248, 192, v98\n"
"v_xor_b32 v249, 208, v98\n"
"v_xor_b32 v250, 224, v98\n"
"ds_read_u16_d16_hi v114, v39 offset:576\n"
"ds_read_u16_d16_hi v130, v39 offset:1600\n"
"ds_read_u16_d16_hi v115, v39 offset:512\n"
"ds_read_u16_d16_hi v131, v39 offset:1536\n"
"ds_write_b64 v251, v[80:81] offset:0\n"
"v_xor_b32 v251, 240, v98\n"
"ds_read_u16_d16_hi v116, v39 offset:192\n"
"ds_read_b64_tr_b16 a[96:97], v94 offset:0\n"
"ds_read_u16_d16_hi v132, v39 offset:1216\n"
"ds_read_b64_tr_b16 a[98:99], v95 offset:2048\n"
"ds_read_u16_d16_hi v117, v39 offset:128\n"
"ds_read_b64_tr_b16 a[100:101], v94 offset:8192\n"
"ds_read_u16_d16_hi v133, v39 offset:1152\n"
"ds_read_b64_tr_b16 a[102:103], v95 offset:10240\n"
"ds_read_u16_d16_hi v118, v39 offset:64\n"
"ds_read_u16_d16_hi v134, v39 offset:1088\n"
"ds_read_u16_d16_hi v119, v39 offset:0\n"
"ds_read_u16_d16_hi v135, v39 offset:1024\n"
"ds_read_b64_tr_b16 a[104:105], v96 offset:0\n"
"ds_read_b64_tr_b16 a[106:107], v97 offset:2048\n"
"ds_read_b64_tr_b16 a[108:109], v96 offset:8192\n"
"ds_read_b64_tr_b16 a[110:111], v97 offset:10240\n"
"ds_read_b64_tr_b16 a[80:81], v91 offset:256\n"
"ds_read_b64_tr_b16 a[82:83], v90 offset:2304\n"
"ds_read_b64_tr_b16 a[84:85], v91 offset:8448\n"
"ds_read_b64_tr_b16 a[86:87], v90 offset:10496\n"
"ds_read_b64_tr_b16 a[88:89], v93 offset:256\n"
"s_waitcnt lgkmcnt(15)\n"
"v_mfma_f32_32x32x16_bf16 v[172:187], a[96:99], a[32:35], 0\n"
"ds_read_b64_tr_b16 a[90:91], v92 offset:2304\n"
"ds_read_b64_tr_b16 a[92:93], v93 offset:8448\n"
"ds_read_b64_tr_b16 a[94:95], v92 offset:10496\n"
"ds_read_b64_tr_b16 a[96:97], v95 offset:256\n"
"s_waitcnt lgkmcnt(15)\n"
"v_mfma_f32_32x32x16_bf16 v[172:187], a[100:103], a[36:39], v[172:187]\n"
"ds_read_b64_tr_b16 a[98:99], v94 offset:2304\n"
"ds_read_b64_tr_b16 a[100:101], v95 offset:8448\n"
"ds_read_b64_tr_b16 a[102:103], v94 offset:10496\n"
"s_waitcnt lgkmcnt(15)\n"
"buffer_load_dwordx4 v103, s[48:51], s74 offen lds\n"
"s_add_u32 m0, s78, 1024\n"
"s_nop 0\n"
"buffer_load_dwordx4 v103, s[52:55], s74 offen lds\n"
"s_waitcnt lgkmcnt(14)\n"
"v_mfma_f32_32x32x16_bf16 v[188:203], a[104:107], a[32:35], 0\n"
"ds_read_b64_tr_b16 a[104:105], v97 offset:256\n"
"ds_read_b64_tr_b16 a[106:107], v96 offset:2304\n"
"v_cvt_pk_bf16_f32 v82, v172, v173\n"
"v_cvt_pk_bf16_f32 v83, v174, v175\n"
"v_cvt_pk_bf16_f32 v84, v176, v177\n"
"v_cvt_pk_bf16_f32 v85, v178, v179\n"
"s_waitcnt lgkmcnt(14)\n"
"v_mfma_f32_32x32x16_bf16 v[188:203], a[108:111], a[36:39], v[188:203]\n"
"ds_write_b64 v244, v[82:83] offset:0\n"
"ds_write_b64 v245, v[84:85] offset:0\n"
"s_waitcnt lgkmcnt(14)\n"
"v_mfma_f32_32x32x16_bf16 v[140:155], a[80:83], a[32:35], 0\n"
"ds_read_b64_tr_b16 a[108:109], v97 offset:8448\n"
"ds_read_b64_tr_b16 a[110:111], v96 offset:10496\n"
"v_cvt_pk_bf16_f32 v86, v180, v181\n"
"v_cvt_pk_bf16_f32 v87, v182, v183\n"
"v_cvt_pk_bf16_f32 v88, v184, v185\n"
"v_cvt_pk_bf16_f32 v89, v186, v187\n"
"s_waitcnt lgkmcnt(14)\n"
"v_mfma_f32_32x32x16_bf16 v[140:155], a[84:87], a[36:39], v[140:155]\n"
"ds_write_b64 v246, v[86:87] offset:0\n"
"ds_write_b64 v247, v[88:89] offset:0\n"
"s_waitcnt lgkmcnt(14)\n"
"v_mfma_f32_32x32x16_bf16 v[156:171], a[88:91], a[32:35], 0\n"
"v_cvt_pk_bf16_f32 v66, v188, v189\n"
"v_cvt_pk_bf16_f32 v67, v190, v191\n"
"v_cvt_pk_bf16_f32 v68, v192, v193\n"
"v_cvt_pk_bf16_f32 v69, v194, v195\n"
"v_cvt_pk_bf16_f32 v70, v196, v197\n"
"v_cvt_pk_bf16_f32 v71, v198, v199\n"
"s_waitcnt lgkmcnt(12)\n"
"v_mfma_f32_32x32x16_bf16 v[156:171], a[92:95], a[36:39], v[156:171]\n"
"ds_write_b64 v248, v[66:67] offset:0\n"
"ds_write_b64 v249, v[68:69] offset:0\n"
"s_waitcnt lgkmcnt(12)\n"
"v_mfma_f32_32x32x16_bf16 v[172:187], a[96:99], a[32:35], 0\n"
"ds_write_b64 v250, v[70:71] offset:0\n"
"v_cvt_pk_bf16_f32 v72, v200, v201\n"
"v_cvt_pk_bf16_f32 v73, v202, v203\n"
"s_add_u32 m0, s78, 2048\n"
"s_waitcnt lgkmcnt(11)\n"
"v_mfma_f32_32x32x16_bf16 v[172:187], a[100:103], a[36:39], v[172:187]\n"
"ds_write_b64 v251, v[72:73] offset:0\n"
"buffer_load_dwordx4 v103, s[48:51], s75 offen lds\n"
"s_add_u32 m0, s78, 3072\n"
"s_sub_u32 s80, s75, 2\n"
"s_waitcnt lgkmcnt(10)\n"
"v_mfma_f32_32x32x16_bf16 v[188:203], a[104:107], a[32:35], 0\n"
"buffer_load_dwordx4 v103, s[52:55], s75 offen lds\n"
"v_cvt_pk_bf16_f32 v74, v140, v141\n"
"v_cvt_pk_bf16_f32 v75, v142, v143\n"
"v_xor_b32 v244, 16, v98\n"
"v_cvt_pk_bf16_f32 v76, v144, v145\n"
"v_cvt_pk_bf16_f32 v77, v146, v147\n"
"s_waitcnt lgkmcnt(6)\n"
"v_mfma_f32_32x32x16_bf16 v[188:203], a[108:111], a[36:39], v[188:203]\n"
"ds_write_b64 v244, v[74:75] offset:256\n"
"buffer_load_short_d16_hi v240, off, s[48:51], s80\n"
"v_xor_b32 v245, 0, v98\n"
"v_cvt_pk_bf16_f32 v78, v148, v149\n"
"v_cvt_pk_bf16_f32 v79, v150, v151\n"
"v_xor_b32 v246, 48, v98\n"
"v_cvt_pk_bf16_f32 v80, v152, v153\n"
"v_cvt_pk_bf16_f32 v81, v154, v155\n"
"v_xor_b32 v247, 32, v98\n"
"v_cvt_pk_bf16_f32 v82, v156, v157\n"
"v_cvt_pk_bf16_f32 v83, v158, v159\n"
"v_xor_b32 v248, 80, v98\n"
"v_cvt_pk_bf16_f32 v84, v160, v161\n"
"v_cvt_pk_bf16_f32 v85, v162, v163\n"
"v_xor_b32 v249, 64, v98\n"
"v_cvt_pk_bf16_f32 v86, v164, v165\n"
"v_cvt_pk_bf16_f32 v87, v166, v167\n"
"v_xor_b32 v250, 112, v98\n"
"v_cvt_pk_bf16_f32 v88, v168, v169\n"
"v_cvt_pk_bf16_f32 v89, v170, v171\n"
"v_xor_b32 v251, 96, v98\n"
"v_cvt_pk_bf16_f32 v66, v172, v173\n"
"v_cvt_pk_bf16_f32 v67, v174, v175\n"
"v_xor_b32 v244, 144, v98\n"
"v_cvt_pk_bf16_f32 v68, v176, v177\n"
"v_cvt_pk_bf16_f32 v69, v178, v179\n"
"v_cvt_pk_bf16_f32 v70, v180, v181\n"
"v_cvt_pk_bf16_f32 v71, v182, v183\n"
"v_cvt_pk_bf16_f32 v72, v184, v185\n"
"v_cvt_pk_bf16_f32 v73, v186, v187\n"
"v_cvt_pk_bf16_f32 v74, v188, v189\n"
"v_cvt_pk_bf16_f32 v75, v190, v191\n"
"buffer_load_short_d16_hi v241, off, s[52:55], s80\n"
"ds_write_b64 v245, v[76:77] offset:256\n"
"ds_write_b64 v246, v[78:79] offset:256\n"
"ds_write_b64 v247, v[80:81] offset:256\n"
"ds_write_b64 v248, v[82:83] offset:256\n"
"ds_write_b64 v249, v[84:85] offset:256\n"
"v_xor_b32 v245, 128, v98\n"
"v_xor_b32 v246, 176, v98\n"
"v_xor_b32 v247, 160, v98\n"
"v_xor_b32 v248, 208, v98\n"
"v_cvt_pk_bf16_f32 v76, v192, v193\n"
"v_cvt_pk_bf16_f32 v77, v194, v195\n"
"v_xor_b32 v249, 192, v98\n"
"v_cvt_pk_bf16_f32 v78, v196, v197\n"
"v_cvt_pk_bf16_f32 v79, v198, v199\n"
"v_cvt_pk_bf16_f32 v80, v200, v201\n"
"v_cvt_pk_bf16_f32 v81, v202, v203\n"
"ds_write_b64 v250, v[86:87] offset:256\n"
"ds_write_b64 v251, v[88:89] offset:256\n"
"ds_write_b64 v244, v[66:67] offset:256\n"
"ds_write_b64 v245, v[68:69] offset:256\n"
"v_xor_b32 v250, 240, v98\n"
"v_xor_b32 v251, 224, v98\n"
"ds_write_b64 v246, v[70:71] offset:256\n"
"ds_write_b64 v247, v[72:73] offset:256\n"
"ds_write_b64 v248, v[74:75] offset:256\n"
"ds_write_b64 v249, v[76:77] offset:256\n"
"ds_write_b64 v250, v[78:79] offset:256\n"
"ds_write_b64 v251, v[80:81] offset:256\n"
"s_mul_i32 s99, s98, 6\n"
"s_add_u32 s99, s99, 32768\n"
"s_add_u32 s95, s99, 4096\n"
"buffer_load_dwordx4 a[196:199], v103, s[20:23], s99 offen offset:0\n"
"buffer_load_dwordx4 a[200:203], v103, s[20:23], s99 offen offset:1024\n"
"buffer_load_dwordx4 a[204:207], v103, s[20:23], s99 offen offset:2048\n"
"buffer_load_dwordx4 a[208:211], v103, s[20:23], s99 offen offset:3072\n"
"buffer_load_dwordx4 a[212:215], v103, s[20:23], s95 offen offset:0\n"
"buffer_load_dwordx4 a[216:219], v103, s[20:23], s95 offen offset:1024\n"
"v_accvgpr_write_b32 a192, -1\n"
"v_accvgpr_write_b32 a193, -1\n"
"v_accvgpr_write_b32 a194, -1\n"
"v_accvgpr_write_b32 a195, -1\n"
"s_add_u32 s88, s28, 16777216\n"
"s_addc_u32 s89, s29, 0\n"
"s_mov_b32 s90, 8388608\n"
"s_mov_b32 s91, 0x20000\n"
"s_lshl_b32 s92, s19, 14\n"
"s_lshl_b32 s93, s98, 2\n"
"s_add_u32 s92, s92, s93\n"
"buffer_store_dwordx4 a[192:195], v103, s[88:91], s92 offen offset:0 sc1\n"
"buffer_store_dwordx4 a[192:195], v103, s[88:91], s92 offen offset:1024 sc1\n"
"buffer_store_dwordx4 a[192:195], v103, s[88:91], s92 offen offset:2048 sc1\n"
"buffer_store_dwordx4 a[192:195], v103, s[88:91], s92 offen offset:3072 sc1\n"
"s_mov_b32 s63, 0\n"
"s_mov_b32 s82, 4\n"
"s_mov_b32 s83, 67584\n"
"s_mov_b32 s81, 0\n"
"s_waitcnt vmcnt(10) lgkmcnt(0)\n"
"s_barrier\n"
"s_branch .Lp2loop\n"
".Lp2def:\n"
"v_accvgpr_read_b32 v57, a73\n"
"v_add_u32 v57, s96, v57\n"
"v_accvgpr_read_b32 v58, a56\n"
"s_add_u32 s92, s60, 0\n"
"v_accvgpr_read_b32 v59, a57\n"
"s_add_u32 s93, s60, 262144\n"
"v_accvgpr_read_b32 v60, a58\n"
"v_accvgpr_read_b32 v61, a59\n"
"v_accvgpr_read_b32 v62, a60\n"
"v_accvgpr_read_b32 v63, a61\n"
"v_accvgpr_read_b32 v64, a62\n"
"v_accvgpr_read_b32 v65, a63\n"
"ds_read_b128 v[66:69], v57 offset:0\n"
"ds_read_b128 v[70:73], v57 offset:1024\n"
"ds_read_b128 v[74:77], v57 offset:2048\n"
"ds_read_b128 v[78:81], v57 offset:3072\n"
"ds_read_b128 v[82:85], v57 offset:4096\n"
"ds_read_b128 v[86:89], v57 offset:5120\n"
"ds_read_b128 v[90:93], v57 offset:6144\n"
"ds_read_b128 v[94:97], v57 offset:7168\n"
"s_waitcnt lgkmcnt(7)\n"
"buffer_store_dwordx4 v[66:69], v58, s[44:47], s92 offen sc1\n"
"s_waitcnt lgkmcnt(6)\n"
"v_swap_b32 v70, v72\n"
"v_swap_b32 v71, v73\n"
"s_add_u32 s92, s60, 524288\n"
"s_waitcnt lgkmcnt(4)\n"
"v_swap_b32 v78, v80\n"
"v_swap_b32 v79, v81\n"
"buffer_store_dwordx4 v[70:73], v59, s[44:47], s93 offen sc1\n"
"s_waitcnt lgkmcnt(2)\n"
"v_swap_b32 v86, v88\n"
"v_swap_b32 v87, v89\n"
"s_add_u32 s93, s60, 786432\n"
"buffer_store_dwordx4 v[74:77], v60, s[44:47], s92 offen sc1\n"
"s_waitcnt lgkmcnt(0)\n"
"v_swap_b32 v94, v96\n"
"v_swap_b32 v95, v97\n"
"s_add_u32 s92, s60, 1048576\n"
"buffer_store_dwordx4 v[78:81], v61, s[44:47], s93 offen sc1\n"
"s_nop 1\n"
"s_add_u32 s93, s60, 1310720\n"
"buffer_store_dwordx4 v[82:85], v62, s[44:47], s92 offen sc1\n"
"s_nop 1\n"
"s_add_u32 s92, s60, 1572864\n"
"buffer_store_dwordx4 v[86:89], v63, s[44:47], s93 offen sc1\n"
"s_nop 1\n"
"s_add_u32 s93, s60, 1835008\n"
"buffer_store_dwordx4 v[90:93], v64, s[44:47], s92 offen sc1\n"
"buffer_store_dwordx4 v[94:97], v65, s[44:47], s93 offen sc1\n"
"s_nop 1\n"
"s_sub_u32 s81, s81, 1\n"
"s_add_u32 s96, s96, 8192\n"
"s_add_u32 s60, s60, 2097152\n"
"s_branch .Lp2defd\n"
".p2align 7\n"
"s_nop 0\n"
"s_nop 0\n"
"s_nop 0\n"
"s_nop 0\n"
"s_nop 0\n"
"s_nop 0\n"
"s_nop 0\n"
"s_nop 0\n"
"s_nop 0\n"
"s_nop 0\n"
"s_nop 0\n"
"s_nop 0\n"
"s_nop 0\n"
"s_nop 0\n"
"s_nop 0\n"
"s_nop 0\n"
".Lp2loop:\n"
"s_cmp_lg_u32 s81, 0\n"
"s_cbranch_scc1 .Lp2def\n"
".Lp2defd:\n"
"v_readlane_b32 s64, v41, s63\n"
"v_readlane_b32 s65, v42, s63\n"
"v_readlane_b32 s66, v43, s63\n"
"v_readlane_b32 s67, v44, s63\n"
"s_add_u32 s62, s63, 1\n"
"v_readlane_b32 s68, v41, s62\n"
"v_readlane_b32 s69, v42, s62\n"
"v_readlane_b32 s70, v43, s62\n"
"v_readlane_b32 s71, v44, s62\n"
"s_add_u32 s63, s63, 2\n"
"v_readlane_b32 s72, v45, s63\n"
"v_readlane_b32 s73, v46, s63\n"
"s_add_u32 s62, s63, 1\n"
"v_readlane_b32 s74, v45, s62\n"
"v_readlane_b32 s75, v46, s62\n"
"v_xad_u32 v144, s64, v34, v35\n"
"v_xor_b32 v145, 32, v144\n"
"v_xad_u32 v146, s65, v34, v35\n"
"v_xor_b32 v147, 32, v146\n"
"v_xad_u32 v248, s68, v34, v35\n"
"v_xor_b32 v249, 32, v248\n"
"v_xad_u32 v250, s69, v34, v35\n"
"v_xor_b32 v251, 32, v250\n"
"s_add_u32 m0, s78, 0\n"
"v_xad_u32 v148, s67, v36, v37\n"
"v_xor_b32 v149, 16, v148\n"
"v_xor_b32 v150, 32, v148\n"
"v_xor_b32 v151, 48, v148\n"
"v_xad_u32 v252, s71, v36, v37\n"
"v_xor_b32 v253, 16, v252\n"
"v_xor_b32 v254, 32, v252\n"
"v_xor_b32 v255, 48, v252\n"
"ds_read_b64_tr_b16 a[80:81], v144 offset:0\n"
"ds_read_b64_tr_b16 a[82:83], v145 offset:16896\n"
"ds_read_b64_tr_b16 a[84:85], v144 offset:33792\n"
"ds_read_b64_tr_b16 a[86:87], v145 offset:50688\n"
"ds_read_b64_tr_b16 a[88:89], v146 offset:0\n"
"ds_read_b64_tr_b16 a[90:91], v147 offset:16896\n"
"ds_read_b64_tr_b16 a[92:93], v146 offset:33792\n"
"ds_read_b64_tr_b16 a[94:95], v147 offset:50688\n"
"ds_read_u16_d16_hi v208, v38 offset:0\n"
"ds_read_u16_d16_hi v224, v38 offset:1024\n"
"ds_read_u16_d16_hi v209, v38 offset:64\n"
"ds_read_u16_d16_hi v225, v38 offset:1088\n"
"ds_read_u16_d16_hi v210, v38 offset:128\n"
"v_xad_u32 v144, s66, v36, v37\n"
"v_xor_b32 v145, 16, v144\n"
"v_xor_b32 v146, 32, v144\n"
"v_xor_b32 v147, 48, v144\n"
"ds_read_u16_d16_hi v226, v38 offset:1152\n"
"ds_read_u16_d16_hi v211, v38 offset:192\n"
"ds_read_u16_d16_hi v227, v38 offset:1216\n"
"ds_read_u16_d16_hi v212, v38 offset:512\n"
"ds_read_u16_d16_hi v228, v38 offset:1536\n"
"ds_read_u16_d16_hi v213, v38 offset:576\n"
"ds_read_u16_d16_hi v229, v38 offset:1600\n"
"ds_read_u16_d16_hi v214, v38 offset:640\n"
"ds_read_u16_d16_hi v230, v38 offset:1664\n"
"ds_read_u16_d16_hi v215, v38 offset:704\n"
"ds_read_u16_d16_hi v231, v38 offset:1728\n"
"s_waitcnt lgkmcnt(15)\n"
"v_mfma_f32_32x32x16_bf16 v[48:63], a[80:83], a[0:3], 0\n"
"ds_read_u16_d16_hi v216, v39 offset:704\n"
"ds_read_u16_d16_hi v232, v39 offset:1728\n"
"ds_read_u16_d16_hi v217, v39 offset:640\n"
"ds_read_u16_d16_hi v233, v39 offset:1664\n"
"v_mfma_f32_32x32x16_bf16 v[48:63], a[88:91], a[8:11], v[48:63]\n"
"ds_read_u16_d16_hi v218, v39 offset:576\n"
"ds_read_u16_d16_hi v234, v39 offset:1600\n"
"ds_read_u16_d16_hi v219, v39 offset:512\n"
"ds_read_b64_tr_b16 a[96:97], v248 offset:0\n"
"v_mfma_f32_32x32x16_bf16 v[48:63], a[84:87], a[4:7], v[48:63]\n"
"ds_read_u16_d16_hi v235, v39 offset:1536\n"
"ds_read_b64_tr_b16 a[98:99], v249 offset:16896\n"
"ds_read_u16_d16_hi v220, v39 offset:192\n"
"v_sub_f32 v140, v136, v104\n"
"v_add_f32 v141, v137, v120\n"
"v_sub_f32 v142, v138, v112\n"
"v_mfma_f32_32x32x16_bf16 v[48:63], a[92:95], a[12:15], v[48:63]\n"
"ds_read_b64_tr_b16 a[100:101], v248 offset:33792\n"
"ds_read_u16_d16_hi v236, v39 offset:1216\n"
"ds_read_b64_tr_b16 a[102:103], v249 offset:50688\n"
"ds_read_u16_d16_hi v221, v39 offset:128\n"
"ds_read_b64_tr_b16 a[104:105], v250 offset:0\n"
"buffer_load_short_d16_hi v138, off, s[48:51], s72 offset:1024\n"
"v_mfma_f32_32x32x16_bf16 v[64:79], a[88:91], a[0:3], 0\n"
"ds_read_u16_d16_hi v237, v39 offset:1152\n"
"ds_read_b64_tr_b16 a[106:107], v251 offset:16896\n"
"ds_read_u16_d16_hi v222, v39 offset:64\n"
"v_add_f32 v143, v139, v128\n"
"s_nop 0\n"
"v_mul_f32 v96, v48, v18\n"
"v_mul_f32 v48, v48, v2\n"
"v_mfma_f32_32x32x16_bf16 v[64:79], a[80:83], a[16:19], v[64:79]\n"
"ds_read_b64_tr_b16 a[108:109], v250 offset:33792\n"
"ds_read_u16_d16_hi v238, v39 offset:1088\n"
"ds_read_b64_tr_b16 a[110:111], v251 offset:50688\n"
"ds_read_u16_d16_hi v223, v39 offset:0\n"
"ds_read_u16_d16_hi v239, v39 offset:1024\n"
"buffer_load_short_d16_hi v139, off, s[52:55], s72 offset:1024\n"
"v_mfma_f32_32x32x16_bf16 v[64:79], a[92:95], a[4:7], v[64:79]\n"
"v_mul_f32 v97, v49, v19\n"
"v_mul_f32 v49, v49, v3\n"
"v_mul_f32 v98, v50, v20\n"
"v_mul_f32 v50, v50, v4\n"
"v_mul_f32 v99, v51, v21\n"
"v_mul_f32 v51, v51, v5\n"
"v_mfma_f32_32x32x16_bf16 v[64:79], a[84:87], a[20:23], v[64:79]\n"
"v_mul_f32 v100, v52, v22\n"
"v_sub_f32 v244, v240, v208\n"
"s_waitcnt lgkmcnt(15)\n"
"v_add_f32 v245, v241, v224\n"
"v_sub_f32 v246, v242, v216\n"
"v_add_f32 v247, v243, v232\n"
"v_mul_f32 v52, v52, v6\n"
"s_waitcnt lgkmcnt(14)\n"
"v_mfma_f32_32x32x16_bf16 v[152:167], a[96:99], a[0:3], 0\n"
"buffer_load_short_d16_hi v242, off, s[48:51], s74 offset:1024\n"
"buffer_load_short_d16_hi v243, off, s[52:55], s74 offset:1024\n"
"v_mul_f32 v101, v53, v23\n"
"v_mul_f32 v53, v53, v7\n"
"v_fma_f32 v48, v64, v18, v48\n"
"v_fma_f32 v64, v64, v2, -v96\n"
"s_waitcnt lgkmcnt(6)\n"
"v_mfma_f32_32x32x16_bf16 v[152:167], a[104:107], a[8:11], v[152:167]\n"
"v_fma_f32 v49, v65, v19, v49\n"
"v_fma_f32 v65, v65, v3, -v97\n"
"v_cvt_pk_bf16_f32 v80, v48, v49\n"
"v_cvt_pk_bf16_f32 v88, v64, v65\n"
"v_fma_f32 v50, v66, v20, v50\n"
"v_fma_f32 v66, v66, v4, -v98\n"
"v_mfma_f32_32x32x16_bf16 v[152:167], a[100:103], a[4:7], v[152:167]\n"
"s_waitcnt lgkmcnt(0)\n"
"buffer_load_dwordx4 v103, s[48:51], s72 offen lds\n"
"v_fma_f32 v51, v67, v21, v51\n"
"v_fma_f32 v67, v67, v5, -v99\n"
"v_cvt_pk_bf16_f32 v81, v50, v51\n"
"v_cvt_pk_bf16_f32 v89, v66, v67\n"
"v_fma_f32 v52, v68, v22, v52\n"
"v_mfma_f32_32x32x16_bf16 v[152:167], a[108:111], a[12:15], v[152:167]\n"
"v_fma_f32 v68, v68, v6, -v100\n"
"v_fma_f32 v140, v140, v40, v104\n"
"v_fma_f32 v141, -v141, v40, v120\n"
"v_fma_f32 v142, v142, v40, v112\n"
"v_fma_f32 v143, -v143, v40, v128\n"
"v_fma_f32 v244, v244, v40, v208\n"
"v_mfma_f32_32x32x16_bf16 v[168:183], a[104:107], a[0:3], 0\n"
"v_fma_f32 v245, -v245, v40, v224\n"
"v_fma_f32 v246, v246, v40, v216\n"
"v_fma_f32 v247, -v247, v40, v232\n"
"v_fma_f32 v53, v69, v23, v53\n"
"v_fma_f32 v69, v69, v7, -v101\n"
"v_cvt_pk_bf16_f32 v82, v52, v53\n"
"v_mfma_f32_32x32x16_bf16 v[168:183], a[96:99], a[16:19], v[168:183]\n"
"v_cvt_pk_bf16_f32 v90, v68, v69\n"
"v_mul_f32 v102, v54, v24\n"
"v_mul_f32 v200, v152, v18\n"
"v_mul_f32 v54, v54, v8\n"
"v_mul_f32 v152, v152, v2\n"
"s_add_u32 m0, s78, 1024\n"
"v_mfma_f32_32x32x16_bf16 v[168:183], a[108:111], a[4:7], v[168:183]\n"
"buffer_load_dwordx4 v103, s[52:55], s72 offen lds\n"
"v_fma_f32 v54, v70, v24, v54\n"
"v_fma_f32 v70, v70, v8, -v102\n"
"s_add_u32 m0, s78, 2048\n"
"v_mul_f32 v96, v55, v25\n"
"v_mul_f32 v201, v153, v19\n"
"v_mfma_f32_32x32x16_bf16 v[168:183], a[100:103], a[20:23], v[168:183]\n"
"buffer_load_dwordx4 v103, s[48:51], s73 offen lds\n"
"v_mul_f32 v55, v55, v9\n"
"v_mul_f32 v153, v153, v3\n"
"s_add_u32 m0, s78, 3072\n"
"v_fma_f32 v55, v71, v25, v55\n"
"v_fma_f32 v71, v71, v9, -v96\n"
"v_cvt_pk_bf16_f32 v83, v54, v55\n"
"v_cvt_pk_bf16_f32 v91, v70, v71\n"
"v_mul_f32 v97, v56, v26\n"
"v_mul_f32 v202, v154, v20\n"
"v_mul_f32 v56, v56, v10\n"
"v_mul_f32 v154, v154, v4\n"
"v_fma_f32 v152, v168, v18, v152\n"
"v_fma_f32 v168, v168, v2, -v200\n"
"v_fma_f32 v153, v169, v19, v153\n"
"v_fma_f32 v169, v169, v3, -v201\n"
"v_cvt_pk_bf16_f32 v184, v152, v153\n"
"v_cvt_pk_bf16_f32 v192, v168, v169\n"
"v_fma_f32 v56, v72, v26, v56\n"
"v_fma_f32 v154, v170, v20, v154\n"
"v_fma_f32 v72, v72, v10, -v97\n"
"v_fma_f32 v170, v170, v4, -v202\n"
"v_mul_f32 v98, v57, v27\n"
"v_mul_f32 v203, v155, v21\n"
"v_mul_f32 v57, v57, v11\n"
"v_mul_f32 v155, v155, v5\n"
"v_fma_f32 v57, v73, v27, v57\n"
"v_fma_f32 v155, v171, v21, v155\n"
"v_fma_f32 v73, v73, v11, -v98\n"
"v_fma_f32 v171, v171, v5, -v203\n"
"v_cvt_pk_bf16_f32 v84, v56, v57\n"
"v_cvt_pk_bf16_f32 v185, v154, v155\n"
"v_cvt_pk_bf16_f32 v92, v72, v73\n"
"v_cvt_pk_bf16_f32 v193, v170, v171\n"
"v_mul_f32 v99, v58, v28\n"
"s_sub_u32 s79, s73, 2\n"
"v_mul_f32 v204, v156, v22\n"
"v_mul_f32 v58, v58, v12\n"
"v_mul_f32 v156, v156, v6\n"
"v_fma_f32 v58, v74, v28, v58\n"
"v_fma_f32 v156, v172, v22, v156\n"
"v_fma_f32 v74, v74, v12, -v99\n"
"v_fma_f32 v172, v172, v6, -v204\n"
"v_mul_f32 v100, v59, v29\n"
"v_mul_f32 v205, v157, v23\n"
"v_mul_f32 v59, v59, v13\n"
"v_mul_f32 v157, v157, v7\n"
"v_fma_f32 v59, v75, v29, v59\n"
"v_fma_f32 v157, v173, v23, v157\n"
"v_fma_f32 v75, v75, v13, -v100\n"
"v_fma_f32 v173, v173, v7, -v205\n"
"v_cvt_pk_bf16_f32 v85, v58, v59\n"
"v_cvt_pk_bf16_f32 v186, v156, v157\n"
"v_cvt_pk_bf16_f32 v93, v74, v75\n"
"v_cvt_pk_bf16_f32 v194, v172, v173\n"
"v_mul_f32 v101, v60, v30\n"
"v_mul_f32 v206, v158, v24\n"
"v_mul_f32 v60, v60, v14\n"
"v_mul_f32 v158, v158, v8\n"
"v_fma_f32 v60, v76, v30, v60\n"
"v_fma_f32 v158, v174, v24, v158\n"
"v_fma_f32 v76, v76, v14, -v101\n"
"v_fma_f32 v174, v174, v8, -v206\n"
"v_mul_f32 v102, v61, v31\n"
"v_mul_f32 v200, v159, v25\n"
"v_mul_f32 v61, v61, v15\n"
"v_mul_f32 v159, v159, v9\n"
"v_fma_f32 v61, v77, v31, v61\n"
"v_fma_f32 v159, v175, v25, v159\n"
"v_fma_f32 v77, v77, v15, -v102\n"
"v_fma_f32 v175, v175, v9, -v200\n"
"v_cvt_pk_bf16_f32 v86, v60, v61\n"
"v_cvt_pk_bf16_f32 v187, v158, v159\n"
"v_cvt_pk_bf16_f32 v94, v76, v77\n"
"v_cvt_pk_bf16_f32 v195, v174, v175\n"
"v_mul_f32 v96, v62, v32\n"
"v_mul_f32 v201, v160, v26\n"
"v_mul_f32 v62, v62, v16\n"
"v_mul_f32 v160, v160, v10\n"
"v_fma_f32 v62, v78, v32, v62\n"
"v_fma_f32 v160, v176, v26, v160\n"
"v_fma_f32 v78, v78, v16, -v96\n"
"v_fma_f32 v176, v176, v10, -v201\n"
"v_mul_f32 v97, v63, v33\n"
"v_mul_f32 v202, v161, v27\n"
"v_mul_f32 v63, v63, v17\n"
"v_mul_f32 v161, v161, v11\n"
"v_fma_f32 v63, v79, v33, v63\n"
"v_fma_f32 v161, v177, v27, v161\n"
"v_fma_f32 v79, v79, v17, -v97\n"
"v_fma_f32 v177, v177, v11, -v202\n"
"v_cvt_pk_bf16_f32 v87, v62, v63\n"
"v_mfma_f32_32x32x16_bf16 v[48:63], a[0:3], v[80:83], 0\n"
"buffer_load_dwordx4 v103, s[52:55], s73 offen lds\n"
"buffer_load_short_d16_hi v136, off, s[48:51], s79\n"
"s_waitcnt vmcnt(4)\n"
"ds_read_u16_d16_hi v104, v38 offset:0\n"
"v_cvt_pk_bf16_f32 v188, v160, v161\n"
"v_cvt_pk_bf16_f32 v95, v78, v79\n"
"v_cvt_pk_bf16_f32 v196, v176, v177\n"
"v_mfma_f32_32x32x16_bf16 v[48:63], a[8:11], v[88:91], v[48:63]\n"
"buffer_load_short_d16_hi v137, off, s[52:55], s79\n"
"v_mul_f32 v203, v162, v28\n"
"v_mul_f32 v162, v162, v12\n"
"v_fma_f32 v162, v178, v28, v162\n"
"v_fma_f32 v178, v178, v12, -v203\n"
"v_mul_f32 v204, v163, v29\n"
"v_mfma_f32_32x32x16_bf16 v[48:63], a[4:7], v[84:87], v[48:63]\n"
"v_mul_f32 v163, v163, v13\n"
"v_fma_f32 v163, v179, v29, v163\n"
"v_fma_f32 v179, v179, v13, -v204\n"
"v_cvt_pk_bf16_f32 v189, v162, v163\n"
"v_cvt_pk_bf16_f32 v197, v178, v179\n"
"v_mul_f32 v205, v164, v30\n"
"v_mfma_f32_32x32x16_bf16 v[48:63], a[12:15], v[92:95], v[48:63]\n"
"s_waitcnt vmcnt(4)\n"
"ds_read_u16_d16_hi v120, v38 offset:1024\n"
"v_mul_f32 v164, v164, v14\n"
"v_fma_f32 v164, v180, v30, v164\n"
"v_fma_f32 v180, v180, v14, -v205\n"
"v_mul_f32 v206, v165, v31\n"
"v_mul_f32 v165, v165, v15\n"
"v_mfma_f32_32x32x16_bf16 v[64:79], a[0:3], v[88:91], 0\n"
"s_waitcnt vmcnt(3)\n"
"ds_read_u16_d16_hi v112, v39 offset:704\n"
"v_fma_f32 v165, v181, v31, v165\n"
"v_fma_f32 v181, v181, v15, -v206\n"
"v_cvt_pk_bf16_f32 v190, v164, v165\n"
"v_mul_f32 v98, v48, v141\n"
"v_mul_f32 v48, v48, v140\n"
"v_mfma_f32_32x32x16_bf16 v[64:79], a[16:19], v[80:83], v[64:79]\n"
"v_cvt_pk_bf16_f32 v198, v180, v181\n"
"v_mul_f32 v99, v49, v121\n"
"v_mul_f32 v200, v166, v32\n"
"v_mul_f32 v49, v49, v105\n"
"v_mul_f32 v166, v166, v16\n"
"v_fma_f32 v166, v182, v32, v166\n"
"v_mfma_f32_32x32x16_bf16 v[64:79], a[4:7], v[92:95], v[64:79]\n"
"v_fma_f32 v182, v182, v16, -v200\n"
"v_mul_f32 v201, v167, v33\n"
"v_mul_f32 v167, v167, v17\n"
"v_mul_f32 v100, v50, v122\n"
"v_fma_f32 v167, v183, v33, v167\n"
"v_mul_f32 v50, v50, v106\n"
"v_mfma_f32_32x32x16_bf16 v[64:79], a[20:23], v[84:87], v[64:79]\n"
"v_fma_f32 v183, v183, v17, -v201\n"
"v_cvt_pk_bf16_f32 v191, v166, v167\n"
"v_cvt_pk_bf16_f32 v199, v182, v183\n"
"v_mul_f32 v101, v51, v123\n"
"v_mul_f32 v51, v51, v107\n"
"v_mul_f32 v102, v52, v124\n"
"v_mfma_f32_32x32x16_bf16 v[152:167], a[0:3], v[184:187], 0\n"
"v_mul_f32 v52, v52, v108\n"
"v_mul_f32 v96, v53, v125\n"
"v_mul_f32 v53, v53, v109\n"
"v_mul_f32 v97, v54, v126\n"
"s_nop 0\n"
"v_fma_f32 v48, -v64, v141, v48\n"
"v_fma_f32 v64, v64, v140, v98\n"
"v_mfma_f32_32x32x16_bf16 v[152:167], a[8:11], v[192:195], v[152:167]\n"
"v_fma_f32 v49, -v65, v121, v49\n"
"v_fma_f32 v65, v65, v105, v99\n"
"v_cvt_pk_bf16_f32 v80, v48, v49\n"
"v_cvt_pk_bf16_f32 v88, v64, v65\n"
"v_fma_f32 v50, -v66, v122, v50\n"
"v_fma_f32 v66, v66, v106, v100\n"
"v_mfma_f32_32x32x16_bf16 v[152:167], a[4:7], v[188:191], v[152:167]\n"
"ds_read_u16_d16_hi v105, v38 offset:64\n"
"ds_read_u16_d16_hi v121, v38 offset:1088\n"
"ds_read_u16_d16_hi v106, v38 offset:128\n"
"ds_read_u16_d16_hi v122, v38 offset:1152\n"
"v_fma_f32 v51, -v67, v123, v51\n"
"v_fma_f32 v67, v67, v107, v101\n"
"v_mfma_f32_32x32x16_bf16 v[152:167], a[12:15], v[196:199], v[152:167]\n"
"ds_read_u16_d16_hi v107, v38 offset:192\n"
"ds_read_u16_d16_hi v123, v38 offset:1216\n"
"v_cvt_pk_bf16_f32 v81, v50, v51\n"
"v_cvt_pk_bf16_f32 v89, v66, v67\n"
"v_fma_f32 v52, -v68, v124, v52\n"
"v_fma_f32 v68, v68, v108, v102\n"
"v_mfma_f32_32x32x16_bf16 v[168:183], a[0:3], v[192:195], 0\n"
"ds_read_u16_d16_hi v108, v38 offset:512\n"
"ds_read_u16_d16_hi v124, v38 offset:1536\n"
"v_fma_f32 v53, -v69, v125, v53\n"
"v_fma_f32 v69, v69, v109, v96\n"
"v_cvt_pk_bf16_f32 v82, v52, v53\n"
"v_cvt_pk_bf16_f32 v90, v68, v69\n"
"v_mfma_f32_32x32x16_bf16 v[168:183], a[16:19], v[184:187], v[168:183]\n"
"ds_read_u16_d16_hi v109, v38 offset:576\n"
"ds_read_u16_d16_hi v125, v38 offset:1600\n"
"v_mul_f32 v202, v152, v245\n"
"v_mul_f32 v54, v54, v110\n"
"v_mul_f32 v152, v152, v244\n"
"v_fma_f32 v54, -v70, v126, v54\n"
"v_mfma_f32_32x32x16_bf16 v[168:183], a[4:7], v[196:199], v[168:183]\n"
"ds_read_u16_d16_hi v126, v38 offset:1664\n"
"v_fma_f32 v70, v70, v110, v97\n"
"v_mul_f32 v98, v55, v127\n"
"v_mul_f32 v203, v153, v225\n"
"v_mul_f32 v55, v55, v111\n"
"v_mul_f32 v153, v153, v209\n"
"v_mfma_f32_32x32x16_bf16 v[168:183], a[20:23], v[188:191], v[168:183]\n"
"ds_read_u16_d16_hi v110, v38 offset:640\n"
"v_fma_f32 v55, -v71, v127, v55\n"
"v_fma_f32 v71, v71, v111, v98\n"
"v_cvt_pk_bf16_f32 v83, v54, v55\n"
"v_cvt_pk_bf16_f32 v91, v70, v71\n"
"v_mul_f32 v99, v56, v143\n"
"v_mul_f32 v204, v154, v226\n"
"v_mul_f32 v56, v56, v142\n"
"v_mul_f32 v154, v154, v210\n"
"v_fma_f32 v56, v72, v143, v56\n"
"v_fma_f32 v72, v72, v142, -v99\n"
"v_mul_f32 v100, v57, v129\n"
"v_fma_f32 v152, -v168, v245, v152\n"
"v_fma_f32 v168, v168, v244, v202\n"
"v_fma_f32 v153, -v169, v225, v153\n"
"v_fma_f32 v169, v169, v209, v203\n"
"v_cvt_pk_bf16_f32 v184, v152, v153\n"
"v_cvt_pk_bf16_f32 v192, v168, v169\n"
"v_fma_f32 v154, -v170, v226, v154\n"
"v_fma_f32 v170, v170, v210, v204\n"
"s_add_u32 m0, s78, 0\n"
"v_mul_f32 v205, v155, v227\n"
"v_mul_f32 v57, v57, v113\n"
"v_mul_f32 v155, v155, v211\n"
"v_fma_f32 v57, v73, v129, v57\n"
"v_fma_f32 v155, -v171, v227, v155\n"
"v_fma_f32 v73, v73, v113, -v100\n"
"v_fma_f32 v171, v171, v211, v205\n"
"v_cvt_pk_bf16_f32 v84, v56, v57\n"
"v_cvt_pk_bf16_f32 v185, v154, v155\n"
"v_cvt_pk_bf16_f32 v92, v72, v73\n"
"v_cvt_pk_bf16_f32 v193, v170, v171\n"
"v_mul_f32 v101, v58, v130\n"
"v_mul_f32 v206, v156, v228\n"
"v_mul_f32 v58, v58, v114\n"
"v_mul_f32 v156, v156, v212\n"
"v_fma_f32 v58, v74, v130, v58\n"
"v_fma_f32 v156, -v172, v228, v156\n"
"v_fma_f32 v74, v74, v114, -v101\n"
"v_fma_f32 v172, v172, v212, v206\n"
"v_mul_f32 v102, v59, v131\n"
"v_mul_f32 v200, v157, v229\n"
"v_mul_f32 v59, v59, v115\n"
"v_mul_f32 v157, v157, v213\n"
"v_fma_f32 v59, v75, v131, v59\n"
"v_fma_f32 v157, -v173, v229, v157\n"
"v_fma_f32 v75, v75, v115, -v102\n"
"v_fma_f32 v173, v173, v213, v200\n"
"v_cvt_pk_bf16_f32 v85, v58, v59\n"
"v_cvt_pk_bf16_f32 v186, v156, v157\n"
"v_cvt_pk_bf16_f32 v93, v74, v75\n"
"v_cvt_pk_bf16_f32 v194, v172, v173\n"
"v_mul_f32 v96, v60, v132\n"
"v_mul_f32 v201, v158, v230\n"
"v_mul_f32 v60, v60, v116\n"
"v_mul_f32 v158, v158, v214\n"
"v_fma_f32 v60, v76, v132, v60\n"
"v_fma_f32 v158, -v174, v230, v158\n"
"v_fma_f32 v76, v76, v116, -v96\n"
"v_fma_f32 v174, v174, v214, v201\n"
"v_mul_f32 v97, v61, v133\n"
"v_mul_f32 v202, v159, v231\n"
"v_mul_f32 v61, v61, v117\n"
"v_mul_f32 v159, v159, v215\n"
"v_fma_f32 v61, v77, v133, v61\n"
"v_fma_f32 v159, -v175, v231, v159\n"
"v_fma_f32 v77, v77, v117, -v97\n"
"v_fma_f32 v175, v175, v215, v202\n"
"v_cvt_pk_bf16_f32 v86, v60, v61\n"
"v_cvt_pk_bf16_f32 v187, v158, v159\n"
"v_cvt_pk_bf16_f32 v94, v76, v77\n"
"v_cvt_pk_bf16_f32 v195, v174, v175\n"
"v_mul_f32 v98, v62, v134\n"
"v_mul_f32 v203, v160, v247\n"
"v_mul_f32 v62, v62, v118\n"
"v_mul_f32 v160, v160, v246\n"
"v_fma_f32 v62, v78, v134, v62\n"
"v_fma_f32 v160, v176, v247, v160\n"
"v_fma_f32 v78, v78, v118, -v98\n"
"v_fma_f32 v176, v176, v246, -v203\n"
"v_mul_f32 v99, v63, v135\n"
"v_mul_f32 v204, v161, v233\n"
"v_mul_f32 v63, v63, v119\n"
"v_mul_f32 v161, v161, v217\n"
"v_fma_f32 v63, v79, v135, v63\n"
"v_fma_f32 v161, v177, v233, v161\n"
"v_fma_f32 v79, v79, v119, -v99\n"
"v_fma_f32 v177, v177, v217, -v204\n"
"v_cvt_pk_bf16_f32 v87, v62, v63\n"
"v_mfma_f32_32x32x16_bf16 v[48:63], v[80:83], a[0:3], 0\n"
"ds_read_u16_d16_hi v111, v38 offset:704\n"
"ds_read_u16_d16_hi v127, v38 offset:1728\n"
"s_waitcnt vmcnt(2)\n"
"ds_read_u16_d16_hi v128, v39 offset:1728\n"
"ds_read_u16_d16_hi v113, v39 offset:640\n"
"ds_read_u16_d16_hi v129, v39 offset:1664\n"
"ds_read_u16_d16_hi v114, v39 offset:576\n"
"v_mfma_f32_32x32x16_bf16 v[48:63], v[88:91], a[16:19], v[48:63]\n"
"ds_read_u16_d16_hi v130, v39 offset:1600\n"
"ds_read_u16_d16_hi v115, v39 offset:512\n"
"ds_read_u16_d16_hi v131, v39 offset:1536\n"
"ds_read_u16_d16_hi v116, v39 offset:192\n"
"ds_read_u16_d16_hi v132, v39 offset:1216\n"
"ds_read_u16_d16_hi v117, v39 offset:128\n"
"v_mfma_f32_32x32x16_bf16 v[48:63], v[84:87], a[4:7], v[48:63]\n"
"ds_read_u16_d16_hi v133, v39 offset:1152\n"
"ds_read_u16_d16_hi v118, v39 offset:64\n"
"ds_read_u16_d16_hi v134, v39 offset:1088\n"
"ds_read_u16_d16_hi v119, v39 offset:0\n"
"ds_read_u16_d16_hi v135, v39 offset:1024\n"
"v_cvt_pk_bf16_f32 v188, v160, v161\n"
"v_cvt_pk_bf16_f32 v95, v78, v79\n"
"v_mfma_f32_32x32x16_bf16 v[64:79], v[88:91], a[0:3], 0\n"
"v_cvt_pk_bf16_f32 v196, v176, v177\n"
"v_mul_f32 v205, v162, v234\n"
"v_mul_f32 v162, v162, v218\n"
"v_fma_f32 v162, v178, v234, v162\n"
"v_fma_f32 v178, v178, v218, -v205\n"
"v_mul_f32 v206, v163, v235\n"
"v_mfma_f32_32x32x16_bf16 v[48:63], v[92:95], a[20:23], v[48:63]\n"
"v_mul_f32 v163, v163, v219\n"
"v_fma_f32 v163, v179, v235, v163\n"
"v_fma_f32 v179, v179, v219, -v206\n"
"v_cvt_pk_bf16_f32 v189, v162, v163\n"
"v_cvt_pk_bf16_f32 v197, v178, v179\n"
"v_mul_f32 v200, v164, v236\n"
"v_mfma_f32_32x32x16_bf16 v[64:79], v[80:83], a[8:11], v[64:79]\n"
"v_mul_f32 v164, v164, v220\n"
"v_fma_f32 v164, v180, v236, v164\n"
"v_fma_f32 v180, v180, v220, -v200\n"
"v_mul_f32 v201, v165, v237\n"
"s_nop 0\n"
"v_mul_f32 v100, v48, v18\n"
"v_mul_f32 v48, v48, v2\n"
"v_mfma_f32_32x32x16_bf16 v[64:79], v[92:95], a[4:7], v[64:79]\n"
"v_mul_f32 v101, v49, v19\n"
"v_mul_f32 v49, v49, v3\n"
"v_mul_f32 v165, v165, v221\n"
"v_fma_f32 v165, v181, v237, v165\n"
"v_fma_f32 v181, v181, v221, -v201\n"
"v_mul_f32 v102, v50, v20\n"
"v_mfma_f32_32x32x16_bf16 v[64:79], v[84:87], a[12:15], v[64:79]\n"
"s_waitcnt lgkmcnt(0)\n"
"buffer_load_dwordx4 v103, s[48:51], s74 offen lds\n"
"s_add_u32 m0, s78, 1024\n"
"v_cvt_pk_bf16_f32 v190, v164, v165\n"
"v_mul_f32 v50, v50, v4\n"
"v_cvt_pk_bf16_f32 v198, v180, v181\n"
"v_mul_f32 v202, v166, v238\n"
"v_mul_f32 v166, v166, v222\n"
"v_mul_f32 v96, v51, v21\n"
"v_fma_f32 v166, v182, v238, v166\n"
"v_mul_f32 v51, v51, v5\n"
"v_fma_f32 v182, v182, v222, -v202\n"
"v_mul_f32 v203, v167, v239\n"
"v_fma_f32 v48, -v64, v18, v48\n"
"v_fma_f32 v64, v64, v2, v100\n"
"v_fma_f32 v49, -v65, v19, v49\n"
"v_fma_f32 v65, v65, v3, v101\n"
"v_cvt_pk_bf16_f32 v80, v48, v49\n"
"v_cvt_pk_bf16_f32 v88, v64, v65\n"
"v_fma_f32 v50, -v66, v20, v50\n"
"v_fma_f32 v66, v66, v4, v102\n"
"v_fma_f32 v51, -v67, v21, v51\n"
"v_fma_f32 v67, v67, v5, v96\n"
"v_mul_f32 v167, v167, v223\n"
"v_cvt_pk_bf16_f32 v81, v50, v51\n"
"v_fma_f32 v167, v183, v239, v167\n"
"v_cvt_pk_bf16_f32 v89, v66, v67\n"
"v_fma_f32 v183, v183, v223, -v203\n"
"v_mul_f32 v97, v52, v22\n"
"v_cvt_pk_bf16_f32 v191, v166, v167\n"
"v_mfma_f32_32x32x16_bf16 v[152:167], v[184:187], a[0:3], 0\n"
"buffer_load_dwordx4 v103, s[52:55], s74 offen lds\n"
"s_add_u32 m0, s78, 2048\n"
"v_mul_f32 v52, v52, v6\n"
"v_cvt_pk_bf16_f32 v199, v182, v183\n"
"v_fma_f32 v52, -v68, v22, v52\n"
"v_fma_f32 v68, v68, v6, v97\n"
"v_mfma_f32_32x32x16_bf16 v[152:167], v[192:195], a[16:19], v[152:167]\n"
"buffer_load_dwordx4 v103, s[48:51], s75 offen lds\n"
"s_add_u32 m0, s78, 3072\n"
"s_sub_u32 s80, s75, 2\n"
"v_mul_f32 v98, v53, v23\n"
"v_mul_f32 v53, v53, v7\n"
"v_fma_f32 v53, -v69, v23, v53\n"
"v_mfma_f32_32x32x16_bf16 v[152:167], v[188:191], a[4:7], v[152:167]\n"
"buffer_load_short_d16_hi v240, off, s[48:51], s80\n"
"v_fma_f32 v69, v69, v7, v98\n"
"v_cvt_pk_bf16_f32 v82, v52, v53\n"
"v_cvt_pk_bf16_f32 v90, v68, v69\n"
"v_mul_f32 v99, v54, v24\n"
"v_mul_f32 v54, v54, v8\n"
"v_mfma_f32_32x32x16_bf16 v[152:167], v[196:199], a[20:23], v[152:167]\n"
"buffer_load_short_d16_hi v241, off, s[52:55], s80\n"
"v_fma_f32 v54, -v70, v24, v54\n"
"v_fma_f32 v70, v70, v8, v99\n"
"v_mul_f32 v100, v55, v25\n"
"v_mul_f32 v55, v55, v9\n"
"v_fma_f32 v55, -v71, v25, v55\n"
"v_mfma_f32_32x32x16_bf16 v[168:183], v[192:195], a[0:3], 0\n"
"buffer_load_dwordx4 v103, s[52:55], s75 offen lds\n"
"v_fma_f32 v71, v71, v9, v100\n"
"v_cvt_pk_bf16_f32 v83, v54, v55\n"
"v_cvt_pk_bf16_f32 v91, v70, v71\n"
"s_nop 0\n"
"v_mul_f32 v204, v152, v18\n"
"v_mul_f32 v152, v152, v2\n"
"v_mfma_f32_32x32x16_bf16 v[168:183], v[184:187], a[8:11], v[168:183]\n"
"v_mul_f32 v205, v153, v19\n"
"v_mul_f32 v153, v153, v3\n"
"v_mul_f32 v101, v56, v26\n"
"v_mul_f32 v206, v154, v20\n"
"v_mul_f32 v56, v56, v10\n"
"v_mul_f32 v154, v154, v4\n"
"v_mfma_f32_32x32x16_bf16 v[168:183], v[196:199], a[4:7], v[168:183]\n"
"v_fma_f32 v56, -v72, v26, v56\n"
"v_fma_f32 v72, v72, v10, v101\n"
"v_mul_f32 v102, v57, v27\n"
"v_mul_f32 v200, v155, v21\n"
"v_mul_f32 v57, v57, v11\n"
"v_mul_f32 v155, v155, v5\n"
"v_mfma_f32_32x32x16_bf16 v[168:183], v[188:191], a[12:15], v[168:183]\n"
"v_fma_f32 v57, -v73, v27, v57\n"
"v_fma_f32 v73, v73, v11, v102\n"
"v_cvt_pk_bf16_f32 v84, v56, v57\n"
"v_cvt_pk_bf16_f32 v92, v72, v73\n"
"v_mul_f32 v96, v58, v28\n"
"v_mul_f32 v201, v156, v22\n"
"v_mul_f32 v58, v58, v12\n"
"v_mul_f32 v156, v156, v6\n"
"v_fma_f32 v58, -v74, v28, v58\n"
"v_fma_f32 v74, v74, v12, v96\n"
"v_mul_f32 v97, v59, v29\n"
"v_mul_f32 v202, v157, v23\n"
"v_fma_f32 v152, -v168, v18, v152\n"
"v_fma_f32 v168, v168, v2, v204\n"
"v_fma_f32 v153, -v169, v19, v153\n"
"v_fma_f32 v169, v169, v3, v205\n"
"v_cvt_pk_bf16_f32 v184, v152, v153\n"
"v_cvt_pk_bf16_f32 v192, v168, v169\n"
"v_fma_f32 v154, -v170, v20, v154\n"
"v_fma_f32 v170, v170, v4, v206\n"
"v_fma_f32 v155, -v171, v21, v155\n"
"v_fma_f32 v171, v171, v5, v200\n"
"v_cvt_pk_bf16_f32 v185, v154, v155\n"
"v_cvt_pk_bf16_f32 v193, v170, v171\n"
"v_fma_f32 v156, -v172, v22, v156\n"
"v_fma_f32 v172, v172, v6, v201\n"
"v_mul_f32 v59, v59, v13\n"
"v_mul_f32 v157, v157, v7\n"
"v_fma_f32 v59, -v75, v29, v59\n"
"v_fma_f32 v157, -v173, v23, v157\n"
"v_fma_f32 v75, v75, v13, v97\n"
"v_fma_f32 v173, v173, v7, v202\n"
"v_cvt_pk_bf16_f32 v85, v58, v59\n"
"v_cvt_pk_bf16_f32 v186, v156, v157\n"
"v_cvt_pk_bf16_f32 v93, v74, v75\n"
"v_cvt_pk_bf16_f32 v194, v172, v173\n"
"v_mul_f32 v98, v60, v30\n"
"v_mul_f32 v203, v158, v24\n"
"v_mul_f32 v60, v60, v14\n"
"v_mul_f32 v158, v158, v8\n"
"v_fma_f32 v60, -v76, v30, v60\n"
"v_fma_f32 v158, -v174, v24, v158\n"
"v_fma_f32 v76, v76, v14, v98\n"
"v_fma_f32 v174, v174, v8, v203\n"
"v_mul_f32 v99, v61, v31\n"
"v_mul_f32 v204, v159, v25\n"
"v_mul_f32 v61, v61, v15\n"
"v_mul_f32 v159, v159, v9\n"
"v_fma_f32 v61, -v77, v31, v61\n"
"v_fma_f32 v159, -v175, v25, v159\n"
"v_fma_f32 v77, v77, v15, v99\n"
"v_fma_f32 v175, v175, v9, v204\n"
"v_cvt_pk_bf16_f32 v86, v60, v61\n"
"v_cvt_pk_bf16_f32 v187, v158, v159\n"
"v_cvt_pk_bf16_f32 v94, v76, v77\n"
"v_cvt_pk_bf16_f32 v195, v174, v175\n"
"v_mul_f32 v100, v62, v32\n"
"v_mul_f32 v205, v160, v26\n"
"v_mul_f32 v62, v62, v16\n"
"v_mul_f32 v160, v160, v10\n"
"v_fma_f32 v62, -v78, v32, v62\n"
"v_fma_f32 v160, -v176, v26, v160\n"
"v_fma_f32 v78, v78, v16, v100\n"
"v_fma_f32 v176, v176, v10, v205\n"
"v_mul_f32 v101, v63, v33\n"
"v_mul_f32 v206, v161, v27\n"
"v_mul_f32 v63, v63, v17\n"
"v_mul_f32 v161, v161, v11\n"
"v_fma_f32 v63, -v79, v33, v63\n"
"v_fma_f32 v161, -v177, v27, v161\n"
"v_fma_f32 v79, v79, v17, v101\n"
"v_fma_f32 v177, v177, v11, v206\n"
"v_cvt_pk_bf16_f32 v87, v62, v63\n"
"v_mfma_f32_32x32x16_bf16 v[48:63], v[80:83], a[0:3], 0\n"
"v_cvt_pk_bf16_f32 v188, v160, v161\n"
"v_cvt_pk_bf16_f32 v95, v78, v79\n"
"v_cvt_pk_bf16_f32 v196, v176, v177\n"
"v_mul_f32 v200, v162, v28\n"
"v_mul_f32 v162, v162, v12\n"
"v_fma_f32 v162, -v178, v28, v162\n"
"v_mfma_f32_32x32x16_bf16 v[48:63], v[88:91], a[16:19], v[48:63]\n"
"v_fma_f32 v178, v178, v12, v200\n"
"v_mul_f32 v201, v163, v29\n"
"v_mul_f32 v163, v163, v13\n"
"v_fma_f32 v163, -v179, v29, v163\n"
"v_fma_f32 v179, v179, v13, v201\n"
"v_cvt_pk_bf16_f32 v189, v162, v163\n"
"v_mfma_f32_32x32x16_bf16 v[48:63], v[84:87], a[4:7], v[48:63]\n"
"v_cvt_pk_bf16_f32 v197, v178, v179\n"
"v_mul_f32 v202, v164, v30\n"
"v_mul_f32 v164, v164, v14\n"
"v_fma_f32 v164, -v180, v30, v164\n"
"v_fma_f32 v180, v180, v14, v202\n"
"v_mul_f32 v203, v165, v31\n"
"v_mfma_f32_32x32x16_bf16 v[48:63], v[92:95], a[20:23], v[48:63]\n"
"v_mul_f32 v165, v165, v15\n"
"v_fma_f32 v165, -v181, v31, v165\n"
"v_fma_f32 v181, v181, v15, v203\n"
"v_cvt_pk_bf16_f32 v190, v164, v165\n"
"v_cvt_pk_bf16_f32 v198, v180, v181\n"
"v_mul_f32 v204, v166, v32\n"
"v_mfma_f32_32x32x16_bf16 v[64:79], v[88:91], a[0:3], 0\n"
"v_mul_f32 v166, v166, v16\n"
"v_fma_f32 v166, -v182, v32, v166\n"
"v_fma_f32 v182, v182, v16, v204\n"
"v_mul_f32 v205, v167, v33\n"
"v_mul_f32 v167, v167, v17\n"
"v_fma_f32 v167, -v183, v33, v167\n"
"v_mfma_f32_32x32x16_bf16 v[64:79], v[80:83], a[8:11], v[64:79]\n"
"v_cvt_pk_bf16_f32 v80, v48, v49\n"
"v_cvt_pk_bf16_f32 v81, v50, v51\n"
"v_cvt_pk_bf16_f32 v82, v52, v53\n"
"v_cvt_pk_bf16_f32 v83, v54, v55\n"
"v_fma_f32 v183, v183, v17, v205\n"
"v_cvt_pk_bf16_f32 v191, v166, v167\n"
"v_mfma_f32_32x32x16_bf16 v[64:79], v[92:95], a[4:7], v[64:79]\n"
"ds_write_b64 v144, v[80:81]\n"
"ds_write_b64 v145, v[82:83]\n"
"v_mfma_f32_32x32x16_bf16 v[64:79], v[84:87], a[12:15], v[64:79]\n"
"v_cvt_pk_bf16_f32 v84, v56, v57\n"
"v_cvt_pk_bf16_f32 v85, v58, v59\n"
"v_cvt_pk_bf16_f32 v86, v60, v61\n"
"v_cvt_pk_bf16_f32 v199, v182, v183\n"
"v_cvt_pk_bf16_f32 v87, v62, v63\n"
"v_xad_u32 v248, s70, v36, v37\n"
"v_mfma_f32_32x32x16_bf16 v[152:167], v[184:187], a[0:3], 0\n"
"ds_write_b64 v146, v[84:85]\n"
"ds_write_b64 v147, v[86:87]\n"
"v_mfma_f32_32x32x16_bf16 v[152:167], v[192:195], a[16:19], v[152:167]\n"
"s_nop 1\n"
"v_cvt_pk_bf16_f32 v88, v64, v65\n"
"v_cvt_pk_bf16_f32 v89, v66, v67\n"
"v_cvt_pk_bf16_f32 v90, v68, v69\n"
"v_cvt_pk_bf16_f32 v91, v70, v71\n"
"v_cvt_pk_bf16_f32 v92, v72, v73\n"
"v_cvt_pk_bf16_f32 v93, v74, v75\n"
"v_mfma_f32_32x32x16_bf16 v[152:167], v[188:191], a[4:7], v[152:167]\n"
"ds_write_b64 v148, v[88:89]\n"
"ds_write_b64 v149, v[90:91]\n"
"v_mfma_f32_32x32x16_bf16 v[152:167], v[196:199], a[20:23], v[152:167]\n"
"ds_write_b64 v150, v[92:93]\n"
"v_cvt_pk_bf16_f32 v94, v76, v77\n"
"v_cvt_pk_bf16_f32 v95, v78, v79\n"
"v_xor_b32 v249, 16, v248\n"
"v_mfma_f32_32x32x16_bf16 v[168:183], v[192:195], a[0:3], 0\n"
"ds_write_b64 v151, v[94:95]\n"
"v_xor_b32 v250, 32, v248\n"
"v_xor_b32 v251, 48, v248\n"
"v_mfma_f32_32x32x16_bf16 v[168:183], v[184:187], a[8:11], v[168:183]\n"
"s_nop 2\n"
"v_cvt_pk_bf16_f32 v184, v152, v153\n"
"v_cvt_pk_bf16_f32 v185, v154, v155\n"
"v_cvt_pk_bf16_f32 v186, v156, v157\n"
"v_cvt_pk_bf16_f32 v187, v158, v159\n"
"v_mfma_f32_32x32x16_bf16 v[168:183], v[196:199], a[4:7], v[168:183]\n"
"ds_write_b64 v248, v[184:185]\n"
"ds_write_b64 v249, v[186:187]\n"
"v_mfma_f32_32x32x16_bf16 v[168:183], v[188:191], a[12:15], v[168:183]\n"
"v_cvt_pk_bf16_f32 v188, v160, v161\n"
"v_cvt_pk_bf16_f32 v189, v162, v163\n"
"v_cvt_pk_bf16_f32 v190, v164, v165\n"
"v_cvt_pk_bf16_f32 v191, v166, v167\n"
"ds_write_b64 v250, v[188:189]\n"
"ds_write_b64 v251, v[190:191]\n"
"s_nop 5\n"
"v_cvt_pk_bf16_f32 v192, v168, v169\n"
"v_cvt_pk_bf16_f32 v193, v170, v171\n"
"v_cvt_pk_bf16_f32 v194, v172, v173\n"
"v_cvt_pk_bf16_f32 v195, v174, v175\n"
"v_cvt_pk_bf16_f32 v196, v176, v177\n"
"v_cvt_pk_bf16_f32 v197, v178, v179\n"
"v_cvt_pk_bf16_f32 v198, v180, v181\n"
"v_cvt_pk_bf16_f32 v199, v182, v183\n"
"ds_write_b64 v252, v[192:193]\n"
"ds_write_b64 v253, v[194:195]\n"
"ds_write_b64 v254, v[196:197]\n"
"ds_write_b64 v255, v[198:199]\n"
"s_cmp_lt_u32 s63, s82\n"
"s_waitcnt vmcnt(0)\n"
"s_cbranch_scc1 .Lp2loop\n"
"v_accvgpr_read_b32 v58, a56\n"
"v_accvgpr_read_b32 v59, a57\n"
"v_accvgpr_read_b32 v60, a58\n"
"v_accvgpr_read_b32 v61, a59\n"
"v_accvgpr_read_b32 v62, a60\n"
"v_accvgpr_read_b32 v63, a61\n"
"v_accvgpr_read_b32 v64, a62\n"
"v_accvgpr_read_b32 v65, a63\n"
"v_accvgpr_read_b32 v48, a64\n"
"v_accvgpr_read_b32 v49, a65\n"
"v_accvgpr_read_b32 v50, a66\n"
"v_accvgpr_read_b32 v51, a67\n"
"v_accvgpr_read_b32 v52, a68\n"
"v_accvgpr_read_b32 v53, a69\n"
"v_accvgpr_read_b32 v54, a70\n"
"v_accvgpr_read_b32 v55, a71\n"
"v_accvgpr_read_b32 v56, a72\n"
"v_accvgpr_read_b32 v57, a73\n"
"v_xor_b32 v52, 8, v52\n"
"v_xor_b32 v53, 8, v53\n"
"v_xor_b32 v54, 8, v54\n"
"v_xor_b32 v55, 8, v55\n"
"v_add_u32 v48, s83, v48\n"
"v_add_u32 v49, s83, v49\n"
"v_add_u32 v50, s83, v50\n"
"v_add_u32 v51, s83, v51\n"
"v_add_u32 v52, s83, v52\n"
"v_add_u32 v53, s83, v53\n"
"v_add_u32 v54, s83, v54\n"
"v_add_u32 v55, s83, v55\n"
"v_add_u32 v56, s83, v56\n"
"v_add_u32 v57, s83, v57\n"
"s_cmp_eq_u32 s83, 0\n"
"s_cbranch_scc0 .Lp2nos\n"
"s_mov_b64 s[40:41], s[84:85]\n"
"s_mov_b64 s[32:33], s[86:87]\n"
"s_mov_b32 s42, 7340032\n"
"s_mov_b32 s35, 0x20000\n"
"s_mov_b32 s65, s94\n"
"s_bfe_u32 s73, s19, 0x10001\n"
"s_lshl_b32 s64, s73, 7\n"
"s_bfe_u32 s74, s19, 0x10002\n"
"s_andn2_b32 s75, 1, s19\n"
"s_lshl_b32 s75, s75, 1\n"
"s_add_u32 s74, s74, s75\n"
"s_lshl_b32 s74, s74, 2\n"
"s_bfe_u32 s75, s19, 0x20003\n"
"s_add_u32 s74, s74, s75\n"
"s_lshr_b32 s75, s19, 5\n"
"s_lshl_b32 s72, s74, 9\n"
"s_lshr_b32 s78, s98, 3\n"
"s_add_u32 s72, s72, s78\n"
"s_add_u32 s72, s72, 65536\n"
"s_lshl_b32 s71, s74, 11\n"
"s_lshl_b32 s78, s98, 10\n"
"s_add_u32 s71, s71, s78\n"
"s_lshl_b32 s97, s75, 8\n"
"s_add_u32 s97, s97, s64\n"
"s_add_u32 s71, s71, s97\n"
"s_lshl_b32 s78, s74, 18\n"
"s_add_u32 s78, s78, s97\n"
"s_lshl_b32 s97, s98, 4\n"
"s_add_u32 s78, s78, s97\n"
"s_add_u32 s78, s78, 8388608\n"
"s_add_u32 s52, s28, s78\n"
"s_addc_u32 s53, s29, 0\n"
"s_mov_b32 s54, 4194304\n"
"s_mov_b32 s55, 0x20000\n"
"s_lshl_b32 s78, s74, 1\n"
"s_add_u32 s78, s78, s73\n"
"s_lshl_b32 s77, s78, 12\n"
"s_add_u32 s77, s77, 327680\n"
"s_mul_i32 s78, s78, 56\n"
"s_lshl_b32 s78, s78, 13\n"
"s_lshl_b32 s97, s98, 1\n"
"s_add_u32 s78, s78, s97\n"
"s_add_u32 s78, s78, 33554432\n"
"s_add_u32 s48, s28, s78\n"
"s_addc_u32 s49, s29, 0\n"
"s_mov_b32 s50, 14680064\n"
"s_add_u32 s60, s28, 16777216\n"
"s_addc_u32 s61, s29, 0\n"
"s_mov_b32 s62, 8388608\n"
"s_mov_b32 s63, 0x20000\n"
"s_lshl_b32 s97, s98, 3\n"
"s_lshl_b32 s66, s98, 1\n"
"s_add_u32 s66, s66, 0\n"
"s_lshl_b32 s67, s19, 14\n"
"s_lshl_b32 s68, s98, 2\n"
"s_add_u32 s67, s67, s68\n"
"s_xor_b32 s68, s67, 32768\n"
"s_lshl_b32 s69, s19, 9\n"
"s_lshr_b32 s78, s98, 3\n"
"s_add_u32 s69, s69, s78\n"
"s_xor_b32 s70, s69, 1024\n"
"s_add_u32 s69, s69, 196608\n"
"s_add_u32 s70, s70, 196608\n"
"s_lshr_b32 s79, s75, 1\n"
"s_lshl_b32 s79, s79, 12\n"
"s_add_u32 s79, s79, 36864\n"
"s_mov_b32 s34, 4194304\n"
"s_mul_i32 s78, s73, 3670016\n"
"s_lshl_b32 s88, s98, 2\n"
"s_lshl_b32 s97, s75, 8\n"
"s_add_u32 s78, s78, s97\n"
"s_add_u32 s40, s40, s78\n"
"s_addc_u32 s41, s41, 0\n"
"s_lshr_b32 s78, s19, 3\n"
"s_add_u32 s78, s78, s75\n"
"s_and_b32 s76, s78, 1\n"
"s_lshl_b32 s38, s76, 15\n"
"s_lshl_b32 s39, s76, 18\n"
"s_mul_i32 s99, s98, 6\n"
"s_add_u32 s99, s99, 32768\n"
"v_accvgpr_read_b32 v104, a196\n"
"v_accvgpr_read_b32 v105, a197\n"
"v_accvgpr_read_b32 v106, a198\n"
"v_accvgpr_read_b32 v107, a199\n"
"s_waitcnt lgkmcnt(0)\n"
"s_barrier\n"
"s_xor_b32 s74, s39, 0\n"
"s_add_u32 m0, s88, 81920\n"
"s_nop 0\n"
"buffer_load_dwordx4 v104, s[40:43], s74 offen offset:0 sc1 lds\n"
"buffer_load_dwordx4 v105, s[40:43], s74 offen offset:1024 sc1 lds\n"
"buffer_load_dwordx4 v106, s[40:43], s74 offen offset:2048 sc1 lds\n"
"buffer_load_dwordx4 v107, s[40:43], s74 offen offset:3072 sc1 lds\n"
"s_xor_b32 s74, s39, 262144\n"
"s_add_u32 m0, s88, 98304\n"
"s_nop 0\n"
"buffer_load_dwordx4 v104, s[40:43], s74 offen offset:0 sc1 lds\n"
"buffer_load_dwordx4 v105, s[40:43], s74 offen offset:1024 sc1 lds\n"
"buffer_load_dwordx4 v106, s[40:43], s74 offen offset:2048 sc1 lds\n"
"buffer_load_dwordx4 v107, s[40:43], s74 offen offset:3072 sc1 lds\n"
"s_branch .Lp2nos2\n"
".Lp2nos:\n"
"s_waitcnt lgkmcnt(0)\n"
"s_barrier\n"
".Lp2nos2:\n"
"ds_read_b64_tr_b16 a[80:81], v48 offset:0\n"
"ds_read_b64_tr_b16 a[82:83], v49 offset:2048\n"
"ds_read_b64_tr_b16 a[88:89], v50 offset:0\n"
"ds_read_b64_tr_b16 a[90:91], v51 offset:2048\n"
"ds_read_b64_tr_b16 a[84:85], v48 offset:8192\n"
"ds_read_b64_tr_b16 a[86:87], v49 offset:10240\n"
"ds_read_b64_tr_b16 a[96:97], v52 offset:0\n"
"ds_read_b64_tr_b16 a[98:99], v53 offset:2048\n"
"ds_read_b64_tr_b16 a[104:105], v54 offset:0\n"
"ds_read_b64_tr_b16 a[106:107], v55 offset:2048\n"
"ds_read_b64_tr_b16 a[92:93], v50 offset:8192\n"
"ds_read_b64_tr_b16 a[94:95], v51 offset:10240\n"
"ds_read_b64_tr_b16 a[100:101], v52 offset:8192\n"
"v_xor_b32 v244, 0, v56\n"
"v_xor_b32 v245, 16, v56\n"
"v_xor_b32 v246, 32, v56\n"
"v_xor_b32 v247, 48, v56\n"
"v_xor_b32 v248, 64, v56\n"
"v_xor_b32 v249, 80, v56\n"
"v_xor_b32 v250, 96, v56\n"
"v_xor_b32 v251, 112, v56\n"
"ds_read_b64_tr_b16 a[102:103], v53 offset:10240\n"
"ds_read_b64_tr_b16 a[108:109], v54 offset:8192\n"
"ds_read_b64_tr_b16 a[110:111], v55 offset:10240\n"
"s_waitcnt lgkmcnt(14)\n"
"v_mfma_f32_32x32x16_bf16 v[140:155], a[80:83], a[48:51], 0\n"
"ds_read_b64_tr_b16 a[80:81], v49 offset:256\n"
"ds_read_b64_tr_b16 a[82:83], v48 offset:2304\n"
"s_waitcnt lgkmcnt(14)\n"
"v_mfma_f32_32x32x16_bf16 v[156:171], a[88:91], a[48:51], 0\n"
"ds_read_b64_tr_b16 a[88:89], v51 offset:256\n"
"ds_read_b64_tr_b16 a[90:91], v50 offset:2304\n"
"s_waitcnt lgkmcnt(14)\n"
"v_mfma_f32_32x32x16_bf16 v[140:155], a[84:87], a[52:55], v[140:155]\n"
"ds_read_b64_tr_b16 a[84:85], v49 offset:8448\n"
"ds_read_b64_tr_b16 a[86:87], v48 offset:10496\n"
"s_waitcnt lgkmcnt(14)\n"
"v_mfma_f32_32x32x16_bf16 v[172:187], a[96:99], a[48:51], 0\n"
"ds_read_b64_tr_b16 a[96:97], v53 offset:256\n"
"ds_read_b64_tr_b16 a[98:99], v52 offset:2304\n"
"s_nop 5\n"
"v_cvt_pk_bf16_f32 v66, v140, v141\n"
"v_cvt_pk_bf16_f32 v67, v142, v143\n"
"s_waitcnt lgkmcnt(14)\n"
"v_mfma_f32_32x32x16_bf16 v[188:203], a[104:107], a[48:51], 0\n"
"ds_read_b64_tr_b16 a[104:105], v55 offset:256\n"
"ds_read_b64_tr_b16 a[106:107], v54 offset:2304\n"
"ds_write_b64 v244, v[66:67] offset:0\n"
"v_cvt_pk_bf16_f32 v68, v144, v145\n"
"s_waitcnt lgkmcnt(15)\n"
"v_mfma_f32_32x32x16_bf16 v[156:171], a[92:95], a[52:55], v[156:171]\n"
"ds_read_b64_tr_b16 a[92:93], v51 offset:8448\n"
"ds_read_b64_tr_b16 a[94:95], v50 offset:10496\n"
"v_cvt_pk_bf16_f32 v69, v146, v147\n"
"v_cvt_pk_bf16_f32 v70, v148, v149\n"
"v_cvt_pk_bf16_f32 v71, v150, v151\n"
"v_cvt_pk_bf16_f32 v72, v152, v153\n"
"s_waitcnt lgkmcnt(15)\n"
"v_mfma_f32_32x32x16_bf16 v[172:187], a[100:103], a[52:55], v[172:187]\n"
"ds_read_b64_tr_b16 a[100:101], v53 offset:8448\n"
"ds_read_b64_tr_b16 a[102:103], v52 offset:10496\n"
"ds_write_b64 v245, v[68:69] offset:0\n"
"ds_write_b64 v246, v[70:71] offset:0\n"
"s_waitcnt lgkmcnt(15)\n"
"v_mfma_f32_32x32x16_bf16 v[188:203], a[108:111], a[52:55], v[188:203]\n"
"ds_read_b64_tr_b16 a[108:109], v55 offset:8448\n"
"ds_read_b64_tr_b16 a[110:111], v54 offset:10496\n"
"v_cvt_pk_bf16_f32 v73, v154, v155\n"
"v_cvt_pk_bf16_f32 v74, v156, v157\n"
"v_cvt_pk_bf16_f32 v75, v158, v159\n"
"v_cvt_pk_bf16_f32 v76, v160, v161\n"
"v_mfma_f32_32x32x16_bf16 v[140:155], a[80:83], a[48:51], 0\n"
"ds_write_b64 v247, v[72:73] offset:0\n"
"ds_write_b64 v248, v[74:75] offset:0\n"
"s_waitcnt lgkmcnt(15)\n"
"v_mfma_f32_32x32x16_bf16 v[140:155], a[84:87], a[52:55], v[140:155]\n"
"v_cvt_pk_bf16_f32 v77, v162, v163\n"
"v_cvt_pk_bf16_f32 v78, v164, v165\n"
"v_cvt_pk_bf16_f32 v79, v166, v167\n"
"v_cvt_pk_bf16_f32 v80, v168, v169\n"
"v_cvt_pk_bf16_f32 v81, v170, v171\n"
"v_cvt_pk_bf16_f32 v82, v172, v173\n"
"v_mfma_f32_32x32x16_bf16 v[156:171], a[88:91], a[48:51], 0\n"
"ds_write_b64 v249, v[76:77] offset:0\n"
"ds_write_b64 v250, v[78:79] offset:0\n"
"s_waitcnt lgkmcnt(10)\n"
"v_mfma_f32_32x32x16_bf16 v[156:171], a[92:95], a[52:55], v[156:171]\n"
"ds_write_b64 v251, v[80:81] offset:0\n"
"v_cvt_pk_bf16_f32 v83, v174, v175\n"
"v_cvt_pk_bf16_f32 v84, v176, v177\n"
"v_cvt_pk_bf16_f32 v85, v178, v179\n"
"v_cvt_pk_bf16_f32 v86, v180, v181\n"
"v_cvt_pk_bf16_f32 v87, v182, v183\n"
"v_cvt_pk_bf16_f32 v88, v184, v185\n"
"v_cvt_pk_bf16_f32 v89, v186, v187\n"
"v_mfma_f32_32x32x16_bf16 v[172:187], a[96:99], a[48:51], 0\n"
"v_cvt_pk_bf16_f32 v66, v188, v189\n"
"v_cvt_pk_bf16_f32 v67, v190, v191\n"
"v_cvt_pk_bf16_f32 v68, v192, v193\n"
"v_cvt_pk_bf16_f32 v69, v194, v195\n"
"v_cvt_pk_bf16_f32 v70, v196, v197\n"
"v_cvt_pk_bf16_f32 v71, v198, v199\n"
"s_waitcnt lgkmcnt(9)\n"
"v_mfma_f32_32x32x16_bf16 v[172:187], a[100:103], a[52:55], v[172:187]\n"
"v_cvt_pk_bf16_f32 v72, v200, v201\n"
"v_cvt_pk_bf16_f32 v73, v202, v203\n"
"v_xor_b32 v244, 128, v56\n"
"v_xor_b32 v248, 192, v56\n"
"v_xor_b32 v245, 144, v56\n"
"v_xor_b32 v249, 208, v56\n"
"v_mfma_f32_32x32x16_bf16 v[188:203], a[104:107], a[48:51], 0\n"
"ds_write_b64 v244, v[82:83] offset:0\n"
"ds_write_b64 v248, v[66:67] offset:0\n"
"s_waitcnt lgkmcnt(7)\n"
"v_mfma_f32_32x32x16_bf16 v[188:203], a[108:111], a[52:55], v[188:203]\n"
"ds_write_b64 v245, v[84:85] offset:0\n"
"ds_write_b64 v249, v[68:69] offset:0\n"
"v_cvt_pk_bf16_f32 v74, v140, v141\n"
"v_cvt_pk_bf16_f32 v75, v142, v143\n"
"v_xor_b32 v244, 16, v56\n"
"v_cvt_pk_bf16_f32 v82, v156, v157\n"
"v_cvt_pk_bf16_f32 v83, v158, v159\n"
"v_xor_b32 v248, 80, v56\n"
"v_xor_b32 v246, 160, v56\n"
"v_xor_b32 v250, 224, v56\n"
"v_cvt_pk_bf16_f32 v76, v144, v145\n"
"v_cvt_pk_bf16_f32 v77, v146, v147\n"
"v_xor_b32 v245, 0, v56\n"
"v_cvt_pk_bf16_f32 v84, v160, v161\n"
"v_cvt_pk_bf16_f32 v85, v162, v163\n"
"v_xor_b32 v249, 64, v56\n"
"v_cvt_pk_bf16_f32 v66, v172, v173\n"
"v_cvt_pk_bf16_f32 v67, v174, v175\n"
"v_xor_b32 v247, 176, v56\n"
"v_xor_b32 v251, 240, v56\n"
"v_cvt_pk_bf16_f32 v78, v148, v149\n"
"v_cvt_pk_bf16_f32 v79, v150, v151\n"
"v_cvt_pk_bf16_f32 v68, v176, v177\n"
"v_cvt_pk_bf16_f32 v69, v178, v179\n"
"v_cvt_pk_bf16_f32 v80, v152, v153\n"
"v_cvt_pk_bf16_f32 v81, v154, v155\n"
"ds_write_b64 v244, v[74:75] offset:256\n"
"ds_write_b64 v248, v[82:83] offset:256\n"
"ds_write_b64 v246, v[86:87] offset:0\n"
"ds_write_b64 v250, v[70:71] offset:0\n"
"ds_write_b64 v245, v[76:77] offset:256\n"
"v_xor_b32 v244, 144, v56\n"
"v_cvt_pk_bf16_f32 v74, v188, v189\n"
"v_cvt_pk_bf16_f32 v75, v190, v191\n"
"v_xor_b32 v248, 208, v56\n"
"v_xor_b32 v246, 48, v56\n"
"v_cvt_pk_bf16_f32 v86, v164, v165\n"
"v_cvt_pk_bf16_f32 v87, v166, v167\n"
"v_xor_b32 v250, 112, v56\n"
"v_xor_b32 v245, 128, v56\n"
"v_cvt_pk_bf16_f32 v76, v192, v193\n"
"v_cvt_pk_bf16_f32 v77, v194, v195\n"
"v_cvt_pk_bf16_f32 v70, v180, v181\n"
"v_cvt_pk_bf16_f32 v71, v182, v183\n"
"ds_write_b64 v249, v[84:85] offset:256\n"
"ds_write_b64 v244, v[66:67] offset:256\n"
"ds_write_b64 v248, v[74:75] offset:256\n"
"ds_write_b64 v247, v[88:89] offset:0\n"
"ds_write_b64 v251, v[72:73] offset:0\n"
"v_xor_b32 v249, 192, v56\n"
"v_xor_b32 v247, 32, v56\n"
"v_cvt_pk_bf16_f32 v88, v168, v169\n"
"v_cvt_pk_bf16_f32 v89, v170, v171\n"
"v_xor_b32 v251, 96, v56\n"
"v_cvt_pk_bf16_f32 v72, v184, v185\n"
"v_cvt_pk_bf16_f32 v73, v186, v187\n"
"ds_write_b64 v246, v[78:79] offset:256\n"
"ds_write_b64 v250, v[86:87] offset:256\n"
"ds_write_b64 v245, v[68:69] offset:256\n"
"v_xor_b32 v246, 176, v56\n"
"v_cvt_pk_bf16_f32 v78, v196, v197\n"
"v_cvt_pk_bf16_f32 v79, v198, v199\n"
"v_xor_b32 v250, 240, v56\n"
"ds_write_b64 v249, v[76:77] offset:256\n"
"ds_write_b64 v247, v[80:81] offset:256\n"
"v_xor_b32 v247, 160, v56\n"
"v_cvt_pk_bf16_f32 v80, v200, v201\n"
"v_cvt_pk_bf16_f32 v81, v202, v203\n"
"ds_write_b64 v251, v[88:89] offset:256\n"
"v_xor_b32 v251, 224, v56\n"
"ds_write_b64 v246, v[70:71] offset:256\n"
"ds_write_b64 v250, v[78:79] offset:256\n"
"ds_write_b64 v247, v[72:73] offset:256\n"
"ds_write_b64 v251, v[80:81] offset:256\n"
"s_cmp_eq_u32 s83, 0\n"
"s_cbranch_scc1 .Lp2done\n"
"s_mov_b32 s96, s83\n"
"s_mov_b32 s83, 0\n"
"s_mov_b32 s82, s61\n"
"s_mov_b32 s81, 2\n"
"v_accvgpr_mov_b32 a48, a40\n"
"v_accvgpr_mov_b32 a49, a41\n"
"v_accvgpr_mov_b32 a50, a42\n"
"v_accvgpr_mov_b32 a51, a43\n"
"v_accvgpr_mov_b32 a52, a44\n"
"v_accvgpr_mov_b32 a53, a45\n"
"v_accvgpr_mov_b32 a54, a46\n"
"v_accvgpr_mov_b32 a55, a47\n"
"s_branch .Lp2loop\n"
".Lp2done:\n"
"s_xor_b32 s74, s39, 524288\n"
"s_add_u32 m0, s88, 114688\n"
"s_nop 0\n"
"buffer_load_dwordx4 v104, s[40:43], s74 offen offset:0 sc1 lds\n"
"buffer_load_dwordx4 v105, s[40:43], s74 offen offset:1024 sc1 lds\n"
"buffer_load_dwordx4 v106, s[40:43], s74 offen offset:2048 sc1 lds\n"
"buffer_load_dwordx4 v107, s[40:43], s74 offen offset:3072 sc1 lds\n"
"ds_read_b128 v[144:147], v57 offset:1024\n"
"ds_read_b128 v[152:155], v57 offset:3072\n"
"ds_read_b128 v[160:163], v57 offset:5120\n"
"ds_read_b128 v[168:171], v57 offset:7168\n"
"ds_read_b128 v[176:179], v57 offset:9216\n"
"v_accvgpr_read_b32 v38, a202\n"
"v_accvgpr_read_b32 v40, a204\n"
"v_accvgpr_read_b32 v41, a205\n"
"v_accvgpr_read_b32 v42, a206\n"
"v_accvgpr_read_b32 v43, a207\n"
"v_accvgpr_read_b32 v44, a208\n"
"v_accvgpr_read_b32 v45, a209\n"
"v_accvgpr_read_b32 v46, a210\n"
"v_accvgpr_read_b32 v47, a211\n"
"v_add_u32 v56, s79, v38\n"
"v_accvgpr_read_b32 v32, a196\n"
"v_accvgpr_read_b32 v33, a197\n"
"v_accvgpr_read_b32 v34, a198\n"
"v_accvgpr_read_b32 v35, a199\n"
"v_accvgpr_read_b32 v36, a200\n"
"v_accvgpr_read_b32 v37, a201\n"
"v_accvgpr_read_b32 v39, a203\n"
"v_accvgpr_read_b32 v48, a212\n"
"v_accvgpr_read_b32 v49, a213\n"
"v_accvgpr_read_b32 v50, a214\n"
"v_accvgpr_read_b32 v51, a215\n"
"v_accvgpr_read_b32 v52, a216\n"
"v_accvgpr_read_b32 v53, a217\n"
"v_accvgpr_read_b32 v54, a218\n"
"v_accvgpr_read_b32 v55, a219\n"
"v_mov_b32 v10, v103\n"
"v_mov_b32 v12, s36\n"
"v_mov_b32 v13, s37\n"
"v_xor_b32 v40, s64, v40\n"
"v_xor_b32 v41, s64, v41\n"
"s_waitcnt lgkmcnt(4)\n"
"v_swap_b32 v144, v146\n"
"v_swap_b32 v145, v147\n"
"s_waitcnt lgkmcnt(3)\n"
"v_swap_b32 v152, v154\n"
"v_swap_b32 v153, v155\n"
"s_waitcnt lgkmcnt(2)\n"
"v_swap_b32 v160, v162\n"
"v_swap_b32 v161, v163\n"
"s_waitcnt lgkmcnt(1)\n"
"v_swap_b32 v168, v170\n"
"v_swap_b32 v169, v171\n"
"s_waitcnt lgkmcnt(0)\n"
"v_swap_b32 v176, v178\n"
"v_swap_b32 v177, v179\n"
"v_xor_b32 v42, s64, v42\n"
"v_xor_b32 v43, s64, v43\n"
"v_xor_b32 v44, s64, v44\n"
"v_xor_b32 v45, s64, v45\n"
"v_xor_b32 v46, s64, v46\n"
"v_xor_b32 v47, s64, v47\n"
"v_writelane_b32 v56, s70, 32\n"
"ds_read_b128 v[184:187], v57 offset:11264\n"
"ds_read_b128 v[192:195], v57 offset:13312\n"
"ds_read_b128 v[200:203], v57 offset:15360\n"
"ds_read_b128 v[140:143], v57 offset:0\n"
"ds_read_b128 v[148:151], v57 offset:2048\n"
"ds_read_b128 v[156:159], v57 offset:4096\n"
"ds_read_b128 v[164:167], v57 offset:6144\n"
"ds_read_b128 v[172:175], v57 offset:8192\n"
"ds_read_b128 v[180:183], v57 offset:10240\n"
"ds_read_b128 v[188:191], v57 offset:12288\n"
"ds_read_b128 v[196:199], v57 offset:14336\n"
"s_waitcnt lgkmcnt(10)\n"
"v_swap_b32 v184, v186\n"
"v_swap_b32 v185, v187\n"
"s_waitcnt lgkmcnt(9)\n"
"v_swap_b32 v192, v194\n"
"v_swap_b32 v193, v195\n"
"s_waitcnt lgkmcnt(8)\n"
"v_swap_b32 v200, v202\n"
"v_swap_b32 v201, v203\n"
"s_nop 1\n"
"s_waitcnt lgkmcnt(0)\n"
"s_barrier\n"
"s_xor_b32 s74, s39, 786432\n"
"s_add_u32 m0, s88, 0\n"
"s_nop 0\n"
"buffer_load_dwordx4 v104, s[40:43], s74 offen offset:0 sc1 lds\n"
"buffer_load_dwordx4 v105, s[40:43], s74 offen offset:1024 sc1 lds\n"
"buffer_load_dwordx4 v106, s[40:43], s74 offen offset:2048 sc1 lds\n"
"buffer_load_dwordx4 v107, s[40:43], s74 offen offset:3072 sc1 lds\n"
"s_add_u32 s92, s59, 0\n"
"buffer_store_dwordx4 v[140:143], v58, s[44:47], s92 offen sc1\n"
"s_add_u32 s93, s59, 262144\n"
"buffer_store_dwordx4 v[144:147], v59, s[44:47], s93 offen sc1\n"
"s_add_u32 s92, s59, 524288\n"
"buffer_store_dwordx4 v[148:151], v60, s[44:47], s92 offen sc1\n"
"s_add_u32 s93, s59, 786432\n"
"buffer_store_dwordx4 v[152:155], v61, s[44:47], s93 offen sc1\n"
"s_add_u32 s92, s59, 1048576\n"
"buffer_store_dwordx4 v[156:159], v62, s[44:47], s92 offen sc1\n"
"s_add_u32 s93, s59, 1310720\n"
"buffer_store_dwordx4 v[160:163], v63, s[44:47], s93 offen sc1\n"
"s_add_u32 s92, s59, 1572864\n"
"buffer_store_dwordx4 v[164:167], v64, s[44:47], s92 offen sc1\n"
"s_add_u32 s93, s59, 1835008\n"
"buffer_store_dwordx4 v[168:171], v65, s[44:47], s93 offen sc1\n"
"s_add_u32 s92, s59, 2097152\n"
"buffer_store_dwordx4 v[172:175], v58, s[44:47], s92 offen sc1\n"
"s_add_u32 s93, s59, 2359296\n"
"buffer_store_dwordx4 v[176:179], v59, s[44:47], s93 offen sc1\n"
"s_add_u32 s92, s59, 2621440\n"
"buffer_store_dwordx4 v[180:183], v60, s[44:47], s92 offen sc1\n"
"s_add_u32 s93, s59, 2883584\n"
"buffer_store_dwordx4 v[184:187], v61, s[44:47], s93 offen sc1\n"
"s_add_u32 s92, s59, 3145728\n"
"buffer_store_dwordx4 v[188:191], v62, s[44:47], s92 offen sc1\n"
"s_add_u32 s93, s59, 3407872\n"
"buffer_store_dwordx4 v[192:195], v63, s[44:47], s93 offen sc1\n"
"s_add_u32 s92, s59, 3670016\n"
"buffer_store_dwordx4 v[196:199], v64, s[44:47], s92 offen sc1\n"
"s_add_u32 s93, s59, 3932160\n"
"buffer_store_dwordx4 v[200:203], v65, s[44:47], s93 offen sc1\n"
"s_waitcnt vmcnt(16)\n"
"v_lshrrev_b32 v15, 1, v10\n"
"s_mov_b64 exec, 0xffff\n"
"buffer_store_dwordx2 v[12:13], v15, s[28:31], s69 offen sc1\n"
"s_mov_b64 exec, -1\n"
"s_mov_b32 s84, 0x0fffffff\n"
"s_mov_b32 s85, 0\n"
"s_waitcnt vmcnt(0)\n"
"v_lshrrev_b32 v15, 1, v10\n"
"s_mov_b64 exec, 0xffff\n"
"buffer_store_dwordx2 v[12:13], v15, s[28:31], s65 offen sc1\n"
"s_mov_b64 exec, -1\n"
"s_mov_b32 s81, 0\n"
".Lpollf:\n"
"buffer_load_dwordx2 v[14:15], v38, s[28:31], s77 offen sc1\n"
"s_add_u32 s81, s81, 1\n"
"s_waitcnt vmcnt(0)\n"
"v_cmp_eq_u64 vcc, v[14:15], v[12:13]\n"
"s_cmp_gt_u32 s81, 100000\n"
"s_cbranch_scc1 .Lpolldf\n"
"s_andn2_b64 s[86:87], s[84:85], vcc\n"
"s_cbranch_scc0 .Lpolldf\n"
"s_sleep 4\n"
"s_branch .Lpollf\n"
".Lpolldf:\n"
"s_mov_b32 s46, 0\n"
"s_xor_b32 s75, s38, 0\n"
"buffer_load_dwordx4 a[64:67], v52, s[48:51], s75 offen offset:0 sc1\n"
"buffer_load_dwordx4 a[68:71], v52, s[48:51], s75 offen offset:1024 sc1\n"
"buffer_load_dwordx4 a[72:75], v36, s[48:51], s75 offen offset:0 sc1\n"
"buffer_load_dwordx4 a[76:79], v36, s[48:51], s75 offen offset:1024 sc1\n"
"s_xor_b32 s75, s38, 32768\n"
"buffer_load_dwordx4 a[80:83], v52, s[48:51], s75 offen offset:0 sc1\n"
"buffer_load_dwordx4 a[84:87], v52, s[48:51], s75 offen offset:1024 sc1\n"
"buffer_load_dwordx4 a[88:91], v36, s[48:51], s75 offen offset:0 sc1\n"
"buffer_load_dwordx4 a[92:95], v36, s[48:51], s75 offen offset:1024 sc1\n"
"s_xor_b32 s75, s38, 65536\n"
"buffer_load_dwordx4 a[96:99], v52, s[48:51], s75 offen offset:0 sc1\n"
"buffer_load_dwordx4 a[100:103], v52, s[48:51], s75 offen offset:1024 sc1\n"
"buffer_load_dwordx4 a[104:107], v36, s[48:51], s75 offen offset:0 sc1\n"
"buffer_load_dwordx4 a[108:111], v36, s[48:51], s75 offen offset:1024 sc1\n"
"s_branch .Lg2entry\n"
".Lfftend:\n"
    :: "{s[56:63]}"(sb),"{v[34:49]}"(vb),"{v[66:81]}"(gb),[lane]"{v0}"(l),"{s[16:19]}"(kb),"{s[20:23]}"(srtb),"{s[28:31]}"(srdm),"{s[36:37]}"(tg2),"{s94}"(WS_FTT+(w*32+e)*128),"{s98}"(w*1024),
       "{s[40:43]}"(srd0),"{s[44:47]}"(srdt),"{s[48:51]}"(srdr),"{s[52:55]}"(srdi),"{v[2:33]}"(tw),"{a[0:15]}"(a0),"{a[16:23]}"(an),"{a[56:63]}"(a1),"{a[64:79]}"(a2),"{s78}"(2*UNIT+w*4096),"{s[24:27]}"(mksrd(wg,1024*3584*2)),"{s[32:35]}"(mksrd(wu,1024*3584*2)),"{s97}"(10*458752)
    : "memory");
    __builtin_unreachable();
  }
}

extern "C" void kernel_launch(void* const* d_in, const int* in_sizes, int n_in, void* d_out, int out_size, void* d_ws, size_t ws_size, hipStream_t stream){
  (void)hipFuncSetAttribute((const void*)fused,hipFuncAttributeMaxDynamicSharedMemorySize,163840);
  fused<<<256,256,163840,stream>>>((const u16*)d_in[0],(const u16*)d_in[1],(const u16*)d_in[2],(const u16*)d_in[4],(const u16*)d_in[5],(const u16*)d_in[6],(const u16*)d_in[7],(char*)d_ws+0,(u16*)d_out);
}
```
